```python
import torch

def _softmax(x, axis=-1):
    return torch.softmax(x, dim=axis)
def _gelu(x):
    return torch.nn.functional.gelu(x, approximate="tanh")
def _silu(x):
    return torch.nn.functional.silu(x)
def _sigmoid(x):
    return torch.sigmoid(x)
def _layernorm(x, axis=-1, eps=1e-5):
    m = x.mean(dim=axis, keepdim=True)
    v = x.var(dim=axis, keepdim=True, unbiased=False)
    return (x - m) / torch.sqrt(v + eps)
def _scatter_add(tgt, idx, src, axis=0):
    return tgt.clone().index_add_(axis, idx, src)
def _segment_sum(src, seg, n):
    out = torch.zeros((n,) + src.shape[1:], dtype=src.dtype)
    return out.index_add_(0, seg, src)
def _pad(x, pw):
    flat = tuple(v for pair in reversed(pw) for v in pair)
    return torch.nn.functional.pad(x, flat)

kernel_name = 'parallel_sum__spectral_gfnet__glu_gated_bilinear__prompt'

def setup_inputs():

    _w = (lambda a: (lambda t: t.to(torch.bfloat16) if t.is_floating_point() else t)(torch.as_tensor(a)))
    def _A_setup():
        B, S, D = 1, 2048, 448
        Df = D // 2 + 1
        return {
            "x": _w((lambda *s: torch.randn(*s, dtype=torch.bfloat16) * 0.05)(B, S, D)),
            "w_r": _w((lambda *s: torch.randn(*s, dtype=torch.bfloat16) * 0.4)(S, Df) * 0.02),
            "w_i": _w((lambda *s: torch.randn(*s, dtype=torch.bfloat16) * 0.4)(S, Df) * 0.02),
        }
    _A_in = _A_setup()

    def _B_setup():
        B, S, D, F = 1, 2048, 448, 1792
        return {
            "x": _w((lambda *s: torch.randn(*s, dtype=torch.bfloat16) * 0.05)(B, S, D)),
            "w_gate": _w((lambda *s: torch.randn(*s, dtype=torch.bfloat16) * 0.2)(D, F) * (D ** -0.5)),
            "w_up": _w((lambda *s: torch.randn(*s, dtype=torch.bfloat16) * 0.2)(D, F) * (D ** -0.5)),
            "w_down": _w((lambda *s: torch.randn(*s, dtype=torch.bfloat16) * 0.2)(F, D) * (F ** -0.5)),
        }
    _B_in = _B_setup()

    return {
        "A_x": _A_in["x"],
        "A_w_r": _A_in["w_r"],
        "A_w_i": _A_in["w_i"],
        "B_x": _B_in["x"],
        "B_w_gate": _B_in["w_gate"],
        "B_w_up": _B_in["w_up"],
        "B_w_down": _B_in["w_down"],
    }

def reference(A_x, A_w_r, A_w_i, B_x, B_w_gate, B_w_up, B_w_down):
    def _A_ref(x, w_r, w_i):
        B, S, D = x.shape
        X = (lambda x, axes=(-2,-1): torch.fft.rfft2(x.float(), dim=axes))(x, axes=(-2, -1))
        W = w_r + 1j * w_i
        X = X * W
        y = (lambda x, s, axes=(-2,-1): torch.fft.irfft2(x, s=s, dim=axes).to(torch.bfloat16))(X, (S, D), axes=(-2, -1))
        return y

    def _B_ref(x, w_gate, w_up, w_down):
        g = torch.einsum("bsd,df->bsf", x, w_gate)
        u = torch.einsum("bsd,df->bsf", x, w_up)
        h = g * u
        return torch.einsum("bsf,fd->bsd", h, w_down)

    x = A_x
    xn = _layernorm(x, axis=-1)
    return x + _A_ref(xn, A_w_r, A_w_i) + _B_ref(xn, B_w_gate, B_w_up, B_w_down)
```

```cpp
#include <hip/hip_runtime.h>
#include <hip/hip_bf16.h>
#include <stdint.h>

typedef __bf16 bf16x8 __attribute__((ext_vector_type(8)));
typedef __bf16 bf16x2 __attribute__((ext_vector_type(2)));
typedef short s16x4 __attribute__((ext_vector_type(4)));
typedef float f32x2 __attribute__((ext_vector_type(2)));
typedef float f32x4 __attribute__((ext_vector_type(4)));
typedef float f32x16 __attribute__((ext_vector_type(16)));
typedef unsigned u32x4 __attribute__((ext_vector_type(4)));
typedef unsigned u32x2 __attribute__((ext_vector_type(2)));
#define LAS __attribute__((address_space(3)))
#define DI __device__ __forceinline__

constexpr unsigned WS_EPOCH = 0;
constexpr unsigned WS_FLAGE = 4096;
constexpr unsigned WS_FLAGZ = 8192;
constexpr unsigned WS_FLAGH = 65536;
constexpr unsigned WS_E = 1u << 20;
constexpr unsigned WS_Z = 3u << 20;
constexpr unsigned WS_H = 5u << 20;

constexpr unsigned L_RING = 0;
constexpr unsigned L_F = 114688;
constexpr unsigned L_EB = L_F + 28672;
constexpr unsigned L_ST = L_EB + 8192 + 512;
constexpr unsigned L_CS = L_ST + 1024;
constexpr unsigned L_TOTAL = 163840;

constexpr unsigned SPIN_CAP = 1u << 17;
constexpr unsigned CODE_BYTES = 16384;

template <int N> struct IC { static constexpr int v = N; };
template <int B, int E, class Fn> DI void sfor(Fn&& f) {
  if constexpr (B < E) { f(IC<B>{}); sfor<B + 1, E>(f); }
}

DI unsigned pk2(float a, float b) { f32x2 t = {a, b}; bf16x2 p = __builtin_convertvector(t, bf16x2); return __builtin_bit_cast(unsigned, p); }
DI bf16x8 asbf(u32x4 v) { return __builtin_bit_cast(bf16x8, v); }
DI f32x4 mfma16(u32x4 a, u32x4 b, f32x4 c) { return __builtin_amdgcn_mfma_f32_16x16x32_bf16(asbf(a), asbf(b), c, 0, 0, 0); }
DI f32x16 mfma32(u32x4 a, u32x4 b, f32x16 c) { return __builtin_amdgcn_mfma_f32_32x32x16_bf16(asbf(a), asbf(b), c, 0, 0, 0); }
DI float dot2(unsigned a, unsigned b, float c) {
  return __builtin_amdgcn_fdot2_f32_bf16(__builtin_bit_cast(bf16x2, a), __builtin_bit_cast(bf16x2, b), c, false);
}
DI float sum8(u32x4 v, float c) { unsigned a = v.x, b = v.y, d = v.z, e = v.w; const unsigned o = 0x3f803f80u;
  return dot2(e, o, dot2(d, o, dot2(b, o, dot2(a, o, c)))); }
DI float sq8(u32x4 v, float c) { unsigned a = v.x, b = v.y, d = v.z, e = v.w;
  return dot2(e, e, dot2(d, d, dot2(b, b, dot2(a, a, c)))); }
DI float cosr(float rev) { return __builtin_amdgcn_cosf(__builtin_amdgcn_fractf(rev)); }
DI float sinr(float rev) { return __builtin_amdgcn_sinf(__builtin_amdgcn_fractf(rev)); }
DI float qsum(float v) {
  unsigned u = __builtin_bit_cast(unsigned, v);
  auto a = __builtin_amdgcn_permlane16_swap(u, u, false, false);
  unsigned a0 = a[0], a1 = a[1];
  float s = __builtin_bit_cast(float, a0) + __builtin_bit_cast(float, a1);
  u = __builtin_bit_cast(unsigned, s);
  auto b = __builtin_amdgcn_permlane32_swap(u, u, false, false);
  unsigned b0 = b[0], b1 = b[1];
  return __builtin_bit_cast(float, b0) + __builtin_bit_cast(float, b1);
}
DI u32x4 lds128(unsigned a) { return *(LAS const u32x4*)(uintptr_t)a; }
DI f32x4 lds128f(unsigned a) { return *(LAS const f32x4*)(uintptr_t)a; }
DI u32x4 ldstr(unsigned a0, unsigned a1) {
  s16x4 lo = __builtin_amdgcn_ds_read_tr16_b64_v4i16((LAS s16x4*)(uintptr_t)a0);
  s16x4 hi = __builtin_amdgcn_ds_read_tr16_b64_v4i16((LAS s16x4*)(uintptr_t)a1);
  return __builtin_bit_cast(u32x4, __builtin_shufflevector(lo, hi, 0, 1, 2, 3, 4, 5, 6, 7));
}
DI void dma16(const char* base, unsigned voff, unsigned ldst) {
  asm volatile("s_mov_b32 m0, %2\n\ts_nop 0\n\tglobal_load_lds_dwordx4 %0, %1" ::"v"(voff), "s"(base), "s"(ldst) : "memory");
}
DI void st128_sc1(char* base, unsigned voff, u32x4 v) {
  asm volatile("global_store_dwordx4 %0, %1, %2 sc1\n\ts_nop 1" ::"v"(voff), "v"(v), "s"(base) : "memory");
}
DI void st32_sc1(char* base, unsigned voff, unsigned v) {
  asm volatile("global_store_dword %0, %1, %2 sc1" ::"v"(voff), "v"(v), "s"(base) : "memory");
}
template <int IMM> DI void ld128_sc1(u32x4& dst, const char* base, unsigned voff) {
  asm volatile("global_load_dwordx4 %0, %1, %2 offset:%3 sc1" : "=v"(dst) : "v"(voff), "s"(base), "i"(IMM) : "memory");
}
#define LD128_SC1(dst, base, voff, imm) ld128_sc1<imm>(dst, base, voff)
DI void dma16v(unsigned long long addr, unsigned ldst) {
  asm volatile("s_mov_b32 m0, %1\n\ts_nop 0\n\tglobal_load_lds_dwordx4 %0, off" ::"v"(addr), "s"(ldst) : "memory");
}
#define WAITV(n) asm volatile("s_waitcnt vmcnt(%0)" ::"i"(n) : "memory")
#define BARRIER() asm volatile("s_waitcnt lgkmcnt(0)\n\ts_barrier" ::: "memory")
template <class T> DI void PIN(T& x) { asm volatile("" : "+v"(x)); }

DI void poll_flags(const char* base, unsigned voff, bool active, unsigned tag) {
  for (unsigned spins = 0; spins < SPIN_CAP; ++spins) {
    unsigned got;
    asm volatile("global_load_dword %0, %1, %2 sc1\n\ts_waitcnt vmcnt(0)" : "=v"(got) : "v"(voff), "s"(base) : "memory");
    if (__all(!active || got == tag)) break;
    __builtin_amdgcn_s_sleep(2);
  }
}

#define STAMP(i)

__global__ __launch_bounds__(512, 2) void gf_glu_kernel(const char* __restrict__ x, const char* __restrict__ wr,
                                                        const char* __restrict__ wi, const char* __restrict__ wg,
                                                        const char* __restrict__ wu, const char* __restrict__ wd,
                                                        char* __restrict__ out, char* __restrict__ ws) {
  extern __shared__ __attribute__((aligned(16))) char smem[];
  STAMP(0);
  const int tid = threadIdx.x;
  const int w = __builtin_amdgcn_readfirstlane(tid >> 6);
  const int l = tid & 63, r = l & 15, q = l >> 4;
  const int b = blockIdx.x, xcd = b & 7, bi = b >> 3;
  const unsigned L0 = (unsigned)(uintptr_t)(LAS char*)smem;

  unsigned code_touch;
  {
    unsigned long long pc;
    asm volatile("s_getpc_b64 %0" : "=s"(pc));
    const char* pcb = (const char*)(pc & ~127ull);
    unsigned off = tid * 64;
    off = off < CODE_BYTES ? off : 0;
    asm volatile("global_load_dword %0, %1, %2" : "=v"(code_touch) : "v"(off), "s"(pcb) : "memory");
  }
  unsigned epoch;
  asm volatile("s_load_dword %0, %1, 0x0" : "=s"(epoch) : "s"(ws) : "memory");
#define GET_TAG() asm volatile("s_waitcnt lgkmcnt(0)" : "+s"(epoch)); const unsigned tag = epoch * 0x9E3779B1u + 0x7F4A7C15u

  if (bi < 28) {
    const int pn = 4 * (xcd & 3) + (bi & 3), tt = 7 * (xcd >> 2) + (bi >> 2);
    const int rot = ((bi >> 2) + 2 * (bi & 3)) % 7;
    const int m = w & 3, nh = w >> 2;
    const int rt = 4 * xcd + (bi & 3), ct = bi >> 2;
    auto rk = [&](int j) { int k = j + rot; return k >= 7 ? k - 7 : k; };

    unsigned xoff[2], woff[2], doff[2];
#pragma unroll
    for (int i = 0; i < 2; ++i) {
      int R = 16 * w + 8 * i + (l >> 3);
      int mm = R >> 5, n1 = R & 31;
      int n2 = (pn & 1) + 8 * (pn >> 1) + 2 * mm;
      xoff[i] = (unsigned)((64 * n1 + n2) * 896 + (((l & 7) ^ ((R >> 1) & 7)) << 4));
      int kgd = R >> 5, rr = R & 31;
      doff[i] = (unsigned)((128 * (kgd >> 1) + 32 * (kgd & 1) + rr) * 896 + ct * 128 + (((l & 7) ^ (((R >> 1) & 3) << 1)) << 4));
    }
    unsigned long long wlane[2];
#pragma unroll
    for (int i = 0; i < 2; ++i) {
      int row = 8 * w + 4 * i + q;
      int f = (row & 3) | (((row >> 3) & 1) << 2);
      int ch = r ^ (f << 1);
      woff[i] = (unsigned)(row * 3584 + tt * 256 + ((ch & 7) << 4));
      wlane[i] = (unsigned long long)((ch >> 3) ? wu : wg) + woff[i];
    }
    const unsigned ldw = L0 + L_RING + w * 2048;
    auto issue = [&](auto TI) {
      constexpr int T = decltype(TI)::v;
      constexpr int slot = T % 7, kind = T / 7;
      const int kt = rk(slot);
      if constexpr (kind == 0) {
        const char* bs = x + kt * 128;
        dma16(bs, xoff[0], ldw + slot * 16384);
        dma16(bs, xoff[1], ldw + slot * 16384 + 1024);
      } else if constexpr (kind <= 2) {
        const unsigned long long add = (unsigned long long)(kt * (64 * 3584) + (kind - 1) * 128);
        unsigned long long a0 = wlane[0] + add, a1 = wlane[1] + add;
        dma16v(a0, ldw + slot * 16384);
        dma16v(a1, ldw + slot * 16384 + 1024);
      } else {
        const char* bs = wd + (kt * 256 + (kind - 3) * 64) * 896;
        dma16(bs, doff[0], ldw + slot * 16384);
        dma16(bs, doff[1], ldw + slot * 16384 + 1024);
      }
    };
    sfor<0, 7>([&](auto T) { issue(T); });
    STAMP(1);

#pragma unroll
    for (int it = 0; it < 4; ++it) {
      const int item = w + 8 * it;
      if (item < 28) {
        const int unit = item / 14, ks = item % 14;
        int c = 16 * tt + 8 * unit + (r >> 1);
        float ph = (r & 1) ? 0.25f : 0.0f;
        if (c == 0 && (r & 1)) { c = 224; ph = 0.0f; }
        const int d0 = 64 * rk(ks >> 1) + 32 * (ks & 1) + 8 * q;
        const float cf = (float)c * (1.0f / 448.0f);
        float v[8];
#pragma unroll
        for (int e = 0; e < 8; ++e) v[e] = cosr(__builtin_fmaf((float)(d0 + e), cf, ph));
        u32x4 pk = {pk2(v[0], v[1]), pk2(v[2], v[3]), pk2(v[4], v[5]), pk2(v[6], v[7])};
        *(LAS u32x4*)(uintptr_t)(L0 + L_F + item * 1024 + l * 16) = pk;
      }
    }
    u32x4 p1c[2], p1s[2];
#pragma unroll
    for (int a = 0; a < 2; ++a) {
      float vc[8], vs[8];
#pragma unroll
      for (int e = 0; e < 8; ++e) {
        int n1 = 16 * ((e >> 2) ^ nh) + 4 * q + (e & 3);
        float rev = (float)(((16 * a + r) * n1) & 31) * (1.0f / 32.0f);
        vc[e] = __builtin_amdgcn_cosf(rev); vs[e] = __builtin_amdgcn_sinf(rev);
      }
      p1c[a] = u32x4{pk2(vc[0], vc[1]), pk2(vc[2], vc[3]), pk2(vc[4], vc[5]), pk2(vc[6], vc[7])};
      p1s[a] = u32x4{pk2(vs[0], vs[1]), pk2(vs[2], vs[3]), pk2(vs[4], vs[5]), pk2(vs[6], vs[7])};
    }
    GET_TAG();
    STAMP(2);

    u32x4 xf[14][2];
    f32x4 accD[2] = {{0, 0, 0, 0}, {0, 0, 0, 0}};
    float ssum = 0.f, ssq = 0.f, cF = 0.f;
    {
      unsigned xa[2][2];
#pragma unroll
      for (int i = 0; i < 2; ++i) {
        int R = 32 * m + 16 * (i ^ nh) + r;
#pragma unroll
        for (int kk = 0; kk < 2; ++kk) xa[i][kk] = L0 + L_RING + R * 128 + ((((4 * kk + q) ^ ((R >> 1) & 7))) << 4);
      }
      const unsigned fa = L0 + L_F + nh * 14336 + l * 16;
      sfor<0, 7>([&](auto IT) {
        constexpr int it = decltype(IT)::v;
        WAITV(it == 0 ? 12 : 10);
        BARRIER();
        if constexpr (it >= 1) issue(IC<it + 6>{});
#pragma unroll
        for (int kk = 0; kk < 2; ++kk) {
          const int ks = 2 * it + kk;
          xf[ks][0] = lds128(xa[0][kk] + it * 16384);
          xf[ks][1] = lds128(xa[1][kk] + it * 16384);
          u32x4 ff = lds128(fa + ks * 1024);
          accD[0] = mfma16(xf[ks][0], ff, accD[0]);
          accD[1] = mfma16(xf[ks][1], ff, accD[1]);
          ssum = sum8(xf[ks][0], ssum);
          ssq = sq8(xf[ks][0], ssq);
          cF = sum8(ff, cF);
        }
      });
      ssum = qsum(ssum); ssq = qsum(ssq); cF = qsum(cF);
      float mean = ssum * (1.0f / 448.0f);
      float var = __builtin_fmaf(-mean, mean, ssq * (1.0f / 448.0f));
      var = var > 0.f ? var : 0.f;
      float rstd = __builtin_amdgcn_rsqf(var + 1e-5f);
      if (q == 0) {
        int R = 32 * m + 16 * nh + r;
        *(LAS float*)(uintptr_t)(L0 + L_ST + R * 4) = mean;
        *(LAS float*)(uintptr_t)(L0 + L_ST + 512 + R * 4) = rstd;
      }
    }
    STAMP(3);

    unsigned wa[4];
    {
      const int qq = r >> 2, pp = r & 3;
      const int f = qq | ((q & 1) << 2);
#pragma unroll
      for (int i = 0; i < 4; ++i) {
        int lam = i ^ m;
        int u = 2 * nh + (lam & 1) + 4 * (lam >> 1);
        wa[i] = L0 + L_RING + (8 * q + qq) * 256 + ((u ^ f) << 5) + pp * 8;
      }
    }
    auto rdw = [&](u32x4* dst, int slot, int kk) {
#pragma unroll
      for (int i = 0; i < 4; ++i) dst[i] = ldstr(wa[i] + slot * 16384 + kk * 8192, wa[i] + slot * 16384 + kk * 8192 + 1024);
    };
    f32x4 acc[2][4][2];
#pragma unroll
    for (int a = 0; a < 2; ++a)
#pragma unroll
      for (int i = 0; i < 4; ++i) { acc[a][i][0] = f32x4{0, 0, 0, 0}; acc[a][i][1] = f32x4{0, 0, 0, 0}; }
    float mrow[2], r2row[2];
    const int g1 = 4 * pn + m;
    auto h_epilogue = [&](auto PS) {
      constexpr int ps = decltype(PS)::v;
      f32x4 cs[4];
#pragma unroll
      for (int i = 0; i < 4; ++i) {
        int lam = i ^ m;
        int u = 2 * nh + (lam & 1) + 4 * (lam >> 1);
        cs[i] = lds128f(L0 + L_CS + (ps * 8 + u) * 64 + q * 16);
      }
      const unsigned hoff = (unsigned)(((((g1 * 2 + ps) * 7 + (tt >> 1)) * 4 + 2 * (tt & 1) + nh) * 2) * 1024 + l * 16);
#pragma unroll
      for (int i2 = 0; i2 < 2; ++i2) {
        float hv[8];
#pragma unroll
        for (int i = 0; i < 2; ++i)
#pragma unroll
          for (int e = 0; e < 4; ++e) {
            float a = __builtin_fmaf(-mrow[i2], cs[i][e], acc[ps][i][i2][e]);
            float c = __builtin_fmaf(-mrow[i2], cs[i + 2][e], acc[ps][i + 2][i2][e]);
            hv[4 * i + e] = (a * r2row[i2]) * c;
          }
        u32x4 pk = {pk2(hv[0], hv[1]), pk2(hv[2], hv[3]), pk2(hv[4], hv[5]), pk2(hv[6], hv[7])};
        st128_sc1(ws + WS_H, hoff + ((i2 ^ nh) << 10), pk);
      }
    };
    auto h_flag = [&](int ps) {
      if (l == 0) st32_sc1(ws + WS_FLAGH, (unsigned)(((g1 * 2 + ps) * 4 + 2 * (tt & 1) + nh) * 128 + (tt >> 1) * 4), tag);
    };

    u32x4 fa_[4], fb_[4];
    float csum = 0.f;
    sfor<7, 21>([&](auto IT) {
      constexpr int it = decltype(IT)::v;
      constexpr int ps = (it - 7) / 7, j = (it - 7) % 7;
      WAITV(8);
      BARRIER();
      if constexpr (it == 8) {
        u32x4 v = lds128(L0 + L_EB + tid * 16);
        st128_sc1(ws + WS_E, (unsigned)((((tid >> 4) * 16 + pn) * 256 + 16 * tt + (tid & 15)) * 16), v);
        if (tt == 0 && tid < 32) {
          u32x4 v2 = lds128(L0 + L_EB + 8192 + tid * 16);
          st128_sc1(ws + WS_E, (unsigned)(((tid * 16 + pn) * 256 + 224) * 16), v2);
        }
      }
      if constexpr (it == 13) {
        if (tid == 0) st32_sc1(ws + WS_FLAGE, (unsigned)((tt >> 1) * 128 + pn * 8 + (tt & 1) * 4), tag);
      }
      if constexpr (it == 20) h_flag(0);
      issue(IC<it + 6>{});
      if constexpr (it == 7) {
        rdw(fa_, 0, 0);
        f32x4 mD[2], rD[2];
#pragma unroll
        for (int i = 0; i < 2; ++i) {
          int R0 = 32 * m + 16 * (i ^ nh);
          mrow[i] = *(LAS const float*)(uintptr_t)(L0 + L_ST + (R0 + r) * 4);
          float rs = *(LAS const float*)(uintptr_t)(L0 + L_ST + 512 + (R0 + r) * 4);
          r2row[i] = rs * rs;
          mD[i] = lds128f(L0 + L_ST + (R0 + 4 * q) * 4);
          rD[i] = lds128f(L0 + L_ST + 512 + (R0 + 4 * q) * 4);
        }
        float dv[8];
#pragma unroll
        for (int i = 0; i < 2; ++i)
#pragma unroll
          for (int e = 0; e < 4; ++e) dv[4 * i + e] = rD[i][e] * __builtin_fmaf(-mD[i][e], cF, accD[i][e]);
        u32x4 df = {pk2(dv[0], dv[1]), pk2(dv[2], dv[3]), pk2(dv[4], dv[5]), pk2(dv[6], dv[7])};
        const bool slot0 = (tt == 0) && (nh == 0) && (r < 2);
        const float kc = (slot0 && r == 1) ? 0.f : 1.f;
        const float ko = (r & 1) ? -1.f : (slot0 ? 0.f : 1.f);
#pragma unroll
        for (int a = 0; a < 2; ++a) {
          f32x4 z = {0, 0, 0, 0};
          f32x4 tc = mfma16(p1c[a], df, z), ts = mfma16(p1s[a], df, z);
#pragma unroll
          for (int e = 0; e < 4; ++e) {
            float tse = ts[e], tce = tc[e];
            float oth = __builtin_bit_cast(float, __builtin_amdgcn_update_dpp(0, __builtin_bit_cast(int, tse), 0xB1, 0xf, 0xf, true));
            float ev = __builtin_fmaf(ko, oth, kc * tce);
            int k1 = 16 * a + 4 * q + e;
            *(LAS unsigned short*)(uintptr_t)(L0 + L_EB + k1 * 256 + (16 * nh + r) * 8 + m * 2) = (unsigned short)pk2(ev, 0.f);
            if (slot0) {
              float nv = r ? tce : -oth;
              *(LAS unsigned short*)(uintptr_t)(L0 + L_EB + 8192 + k1 * 16 + (r ^ 1) * 8 + m * 2) = (unsigned short)pk2(nv, 0.f);
            }
          }
        }
      }
      rdw(fb_, j, 1);
#pragma unroll
      for (int i = 0; i < 4; ++i) {
        acc[ps][i][0] = mfma16(fa_[i], xf[2 * j][0], acc[ps][i][0]);
        acc[ps][i][1] = mfma16(fa_[i], xf[2 * j][1], acc[ps][i][1]);
      }
      csum = sum8(fa_[0], csum);
      if constexpr (it < 20) rdw(fa_, (j + 1) % 7, 0);
#pragma unroll
      for (int i = 0; i < 4; ++i) {
        acc[ps][i][0] = mfma16(fb_[i], xf[2 * j + 1][0], acc[ps][i][0]);
        acc[ps][i][1] = mfma16(fb_[i], xf[2 * j + 1][1], acc[ps][i][1]);
      }
      csum = sum8(fb_[0], csum);
      if constexpr (j == 6) {
        float cs = qsum(csum);
        csum = 0.f;
        int u = 2 * nh + (m & 1) + 4 * (m >> 1);
        if (q == 0) *(LAS float*)(uintptr_t)(L0 + L_CS + (ps * 8 + u) * 64 + r * 4) = cs;
      }
      if constexpr (it == 14) h_epilogue(IC<0>{});
      if constexpr (it == 13) STAMP(4);
    });
    STAMP(5);
    BARRIER();
    h_epilogue(IC<1>{});
    issue(IC<27>{});
    STAMP(6);

    const int kg = w >> 1, half = w & 1;
    const int g2 = 2 * rt + half;
    unsigned da[4][2];
    {
      const int qq = r >> 2, pp = r & 3;
#pragma unroll
      for (int t2 = 0; t2 < 2; ++t2) {
        int row = 32 * kg + 16 * (t2 ^ half) + 4 * q + qq;
#pragma unroll
        for (int dt = 0; dt < 4; ++dt) da[dt][t2] = L0 + L_RING + row * 128 + ((dt ^ ((row >> 1) & 3)) << 5) + pp * 8;
      }
    }
    f32x4 oacc[4][2];
#pragma unroll
    for (int dt = 0; dt < 4; ++dt) { oacc[dt][0] = f32x4{0, 0, 0, 0}; oacc[dt][1] = f32x4{0, 0, 0, 0}; }
    u32x4 hf[7][2];
    auto load_h = [&](int ps) {
      sfor<0, 7>([&](auto J) {
        constexpr int j = decltype(J)::v;
        const char* hb = ws + WS_H + (size_t)((((g2 * 2 + ps) * 7 + rk(j)) * 4 + kg) * 2048);
        const unsigned vo = l * 16;
        LD128_SC1(hf[j][0], hb, vo, 0);
        LD128_SC1(hf[j][1], hb, vo, 1024);
      });
    };
    const unsigned hfl = (unsigned)((g2 * 2 * 4 + kg) * 128 + (l & 7) * 4);
    {
      unsigned got;
      asm volatile("global_load_dword %0, %1, %2 sc1\n\ts_waitcnt vmcnt(0)" : "=v"(got) : "v"(hfl), "s"(ws + WS_FLAGH) : "memory");
      h_flag(1);
      if (!__all(l >= 7 || got == tag)) poll_flags(ws + WS_FLAGH, hfl, l < 7, tag);
    }
    STAMP(7);
    load_h(0);
    sfor<21, 35>([&](auto IT) {
      constexpr int it = decltype(IT)::v;
      constexpr int ps = (it - 21) / 7, j = (it - 21) % 7;
      if constexpr (it == 28) {
        STAMP(8);
        poll_flags(ws + WS_FLAGH, hfl + 512, l < 7, tag);
        STAMP(9);
        load_h(1);
      }
      if constexpr (ps == 0) {
        WAITV(2 * (6 - j) + (j >= 2 ? 2 * (j - 1) : 0));
      } else {
        WAITV(j == 6 ? 0 : 2 * (6 - j) + (j >= 1 ? 2 : 0));
      }
      PIN(hf[j][0]); PIN(hf[j][1]);
      BARRIER();
      if constexpr (it >= 22 && it <= 28) issue(IC<it + 6>{});
#pragma unroll
      for (int dt = 0; dt < 4; ++dt) {
        u32x4 af = ldstr(da[dt][0] + j * 16384, da[dt][1] + j * 16384);
        oacc[dt][0] = mfma16(af, hf[j][0], oacc[dt][0]);
        oacc[dt][1] = mfma16(af, hf[j][1], oacc[dt][1]);
      }
    });
    STAMP(10);

    poll_flags(ws + WS_FLAGZ, (unsigned)(kg * 256 + l * 4), true, tag);
    STAMP(11);
    u32x4 zr[8];
    u32x4 xres;
    {
      const char* zb = ws + WS_Z + (size_t)g2 * 32768 + kg * 256;
      sfor<0, 8>([&](auto I) {
        constexpr int i = decltype(I)::v;
        const unsigned vo = (4 * i + q) * 1024 + r * 16;
        LD128_SC1(zr[i], zb, vo, 0);
      });
      int row = tid >> 3;
      int gg = 2 * rt + (row >> 5);
      int n2 = ((gg >> 2) & 1) + 8 * (gg >> 3) + 2 * (gg & 3);
      unsigned xo = (unsigned)((64 * (row & 31) + n2) * 896 + ct * 128 + (tid & 7) * 16);
      asm volatile("global_load_dwordx4 %0, %1, %2" : "=v"(xres) : "v"(xo), "s"(x) : "memory");
    }
    u32x4 p2c[2], p2s[2];
#pragma unroll
    for (int i2 = 0; i2 < 2; ++i2) {
      float vc[8], vs[8];
#pragma unroll
      for (int e = 0; e < 8; ++e) {
        float rev = (float)(((16 * i2 + r) * (8 * q + e)) & 31) * (1.0f / 32.0f);
        vc[e] = __builtin_amdgcn_cosf(rev); vs[e] = __builtin_amdgcn_sinf(rev);
      }
      p2c[i2] = u32x4{pk2(vc[0], vc[1]), pk2(vc[2], vc[3]), pk2(vc[4], vc[5]), pk2(vc[6], vc[7])};
      p2s[i2] = u32x4{pk2(vs[0], vs[1]), pk2(vs[2], vs[3]), pk2(vs[4], vs[5]), pk2(vs[6], vs[7])};
    }
    BARRIER();
    const unsigned zl = L0 + L_RING + 65536 + w * 8192;
    WAITV(1);
#pragma unroll
    for (int i = 0; i < 8; ++i) {
      PIN(zr[i]);
      int row = 4 * i + q;
      int f = (row & 3) | (((row >> 3) & 1) << 2);
      *(LAS u32x4*)(uintptr_t)(zl + row * 256 + ((r ^ (f << 1)) << 4)) = zr[i];
    }
    {
      const int qq = r >> 2, pp = r & 3;
      const int f = qq | ((q & 1) << 2);
      const unsigned zbase = zl + (8 * q + qq) * 256 + pp * 8;
#pragma unroll
      for (int bk = 0; bk < 4; ++bk) {
        unsigned yy[2][4];
#pragma unroll
        for (int aa = 0; aa < 2; ++aa) {
          const int a = 2 * bk + aa;
          const unsigned ad = zbase + ((a ^ f) << 5);
          u32x4 zf = ldstr(ad, ad + 1024);
#pragma unroll
          for (int i2 = 0; i2 < 2; ++i2) {
            f32x4 z = {0, 0, 0, 0};
            f32x4 tc = mfma16(zf, p2c[i2], z), ts = mfma16(zf, p2s[i2], z);
            yy[i2][2 * aa] = pk2(tc[0] - ts[1], tc[1] + ts[0]);
            yy[i2][2 * aa + 1] = pk2(tc[2] - ts[3], tc[3] + ts[2]);
          }
        }
        u32x4 y0 = {yy[0][0], yy[0][1], yy[0][2], yy[0][3]}, y1 = {yy[1][0], yy[1][1], yy[1][2], yy[1][3]};
#pragma unroll
        for (int dt = 0; dt < 4; ++dt) {
          const float df = (float)(64 * ct + 16 * dt + r) * (1.0f / 448.0f);
          unsigned gg[4];
#pragma unroll
          for (int i = 0; i < 4; ++i) {
            int c = 64 * kg + 16 * bk + 8 * (i >> 1) + 2 * q + (i & 1);
            float rev = __builtin_amdgcn_fractf((float)c * df);
            gg[i] = pk2(__builtin_amdgcn_cosf(rev), -__builtin_amdgcn_sinf(rev));
          }
          u32x4 gf = {gg[0], gg[1], gg[2], gg[3]};
          oacc[dt][0] = mfma16(gf, y0, oacc[dt][0]);
          oacc[dt][1] = mfma16(gf, y1, oacc[dt][1]);
        }
      }
    }
    STAMP(12);
#pragma unroll
    for (int i2 = 0; i2 < 2; ++i2) {
      int row = 32 * half + 16 * i2 + r;
#pragma unroll
      for (int dt = 0; dt < 4; ++dt)
        *(LAS f32x4*)(uintptr_t)(L0 + L_RING + kg * 16384 + row * 256 + (((4 * dt + q) ^ (row & 7)) << 4)) = oacc[dt][i2];
    }
    BARRIER();
    {
      int row = tid >> 3, cp = tid & 7;
      f32x4 s0 = {0, 0, 0, 0}, s1 = {0, 0, 0, 0};
#pragma unroll
      for (int k = 0; k < 4; ++k) {
        s0 += lds128f(L0 + L_RING + k * 16384 + row * 256 + (((2 * cp) ^ (row & 7)) << 4));
        s1 += lds128f(L0 + L_RING + k * 16384 + row * 256 + (((2 * cp + 1) ^ (row & 7)) << 4));
      }
      WAITV(0);
      PIN(xres);
      unsigned x0 = xres.x, x1 = xres.y, x2 = xres.z, x3 = xres.w;
      auto lo = [](unsigned u) { return __builtin_bit_cast(float, u << 16); };
      auto hi = [](unsigned u) { return __builtin_bit_cast(float, u & 0xffff0000u); };
      u32x4 o = {pk2(s0[0] + lo(x0), s0[1] + hi(x0)), pk2(s0[2] + lo(x1), s0[3] + hi(x1)),
                 pk2(s1[0] + lo(x2), s1[1] + hi(x2)), pk2(s1[2] + lo(x3), s1[3] + hi(x3))};
      int gg = 2 * rt + (row >> 5);
      int n2 = ((gg >> 2) & 1) + 8 * (gg >> 3) + 2 * (gg & 3);
      *(u32x4*)(out + (size_t)((64 * (row & 31) + n2) * 896 + ct * 128 + cp * 16)) = o;
    }
    if (b == 0 && tid == 0) *(unsigned*)(ws + WS_EPOCH) = tag;
    STAMP(13);
  } else {
    const int k1 = (bi - 28) * 8 + xcd;
    const int c31 = l & 31, h = l >> 5;
    const int c = 32 * w + c31;
    const bool cok = c < 225;
    float wpr[16], wpi[16], wmr[16], wmi[16];
    {
      const unsigned short* pr = (const unsigned short*)wr;
      const unsigned short* pi = (const unsigned short*)wi;
      const int cc = cok ? c : 224;
      const float sc = cok ? ((c == 0 || c == 224) ? 1.0f : 2.0f) * (1.0f / (2048.0f * 448.0f)) : 0.f;
#pragma unroll
      for (int i = 0; i < 16; ++i) {
        int k2 = (i & 3) + 8 * (i >> 2) + 4 * h;
        int i0 = (k1 + 32 * k2) * 225 + cc, i1 = i0 + 1024 * 225;
        float a0 = __builtin_bit_cast(float, (unsigned)pr[i0] << 16), b0 = __builtin_bit_cast(float, (unsigned)pi[i0] << 16);
        float a1 = __builtin_bit_cast(float, (unsigned)pr[i1] << 16), b1 = __builtin_bit_cast(float, (unsigned)pi[i1] << 16);
        wpr[i] = (a0 + a1) * sc; wpi[i] = (b0 + b1) * sc;
        wmr[i] = (a0 - a1) * sc; wmi[i] = (b0 - b1) * sc;
      }
    }
    {
      const int s = w, par = s >> 2;
      float vc[4], vs[4];
#pragma unroll
      for (int mm = 0; mm < 4; ++mm) {
        int n2 = par + 8 * (2 * (s & 3) + h) + 2 * mm;
        float rev = (float)((n2 * (k1 + 32 * c31)) & 2047) * (1.0f / 2048.0f);
        vc[mm] = __builtin_amdgcn_cosf(rev); vs[mm] = __builtin_amdgcn_sinf(rev);
      }
      u32x4 fre = {pk2(vc[0], vc[1]), pk2(vc[2], vc[3]), pk2(vs[0], vs[1]), pk2(vs[2], vs[3])};
      u32x4 fim = {pk2(-vs[0], -vs[1]), pk2(-vs[2], -vs[3]), pk2(vc[0], vc[1]), pk2(vc[2], vc[3])};
      *(LAS u32x4*)(uintptr_t)(L0 + (2 * s) * 1024 + l * 16) = fre;
      *(LAS u32x4*)(uintptr_t)(L0 + (2 * s + 1) * 1024 + l * 16) = fim;
    }
    if (w < 4) {
      const int par = w >> 1, s2 = w & 1;
      float vc[8], vs[8];
#pragma unroll
      for (int e = 0; e < 8; ++e) {
        int k2 = 16 * s2 + 8 * (e >> 2) + 4 * h + (e & 3);
        float rev = (float)(((par + 2 * c31) * (k1 + 32 * k2)) & 2047) * (1.0f / 2048.0f);
        vc[e] = __builtin_amdgcn_cosf(rev); vs[e] = __builtin_amdgcn_sinf(rev);
      }
      unsigned a = L0 + 16384 + w * 3072 + l * 16;
      *(LAS u32x4*)(uintptr_t)(a) = u32x4{pk2(vc[0], vc[1]), pk2(vc[2], vc[3]), pk2(vc[4], vc[5]), pk2(vc[6], vc[7])};
      *(LAS u32x4*)(uintptr_t)(a + 1024) = u32x4{pk2(vs[0], vs[1]), pk2(vs[2], vs[3]), pk2(vs[4], vs[5]), pk2(vs[6], vs[7])};
      *(LAS u32x4*)(uintptr_t)(a + 2048) = u32x4{pk2(-vs[0], -vs[1]), pk2(-vs[2], -vs[3]), pk2(-vs[4], -vs[5]), pk2(-vs[6], -vs[7])};
    }
#pragma unroll
    for (int i = 0; i < 16; ++i) { PIN(wpr[i]); PIN(wpi[i]); PIN(wmr[i]); PIN(wmi[i]); }
    GET_TAG();
    BARRIER();
    STAMP(1);
    poll_flags(ws + WS_FLAGE, (unsigned)((w == 7 ? 0 : w) * 128 + c31 * 4), (l < 32) && (w < 7 || !(l & 1)), tag);
    STAMP(2);
    u32x4 ef[8];
    {
      const char* eb = ws + WS_E + (size_t)k1 * 65536 + w * 512;
      sfor<0, 8>([&](auto S) {
        constexpr int s = decltype(S)::v;
        const unsigned vo = ((s >> 2) + 2 * (2 * (s & 3) + h)) * 4096 + c31 * 16;
        LD128_SC1(ef[s], eb, vo, 0);
      });
    }
    f32x16 ac[4];
#pragma unroll
    for (int t = 0; t < 4; ++t)
#pragma unroll
      for (int i = 0; i < 16; ++i) ac[t][i] = 0.f;
    sfor<0, 8>([&](auto S) {
      constexpr int s = decltype(S)::v;
      u32x4 fre = lds128(L0 + (2 * s) * 1024 + l * 16), fim = lds128(L0 + (2 * s + 1) * 1024 + l * 16);
      WAITV(7 - s);
      PIN(ef[s]);
      u32x4 e = ef[s];
      if (!cok) e = u32x4{0, 0, 0, 0};
      ac[2 * (s >> 2)] = mfma32(fre, e, ac[2 * (s >> 2)]);
      ac[2 * (s >> 2) + 1] = mfma32(fim, e, ac[2 * (s >> 2) + 1]);
    });
    STAMP(3);
    u32x4 pf[2][2][2];
    {
      float sr[16], si[16], dr[16], di[16];
#pragma unroll
      for (int i = 0; i < 16; ++i) {
        float ar = ac[0][i], ai = ac[1][i], br = ac[2][i], bi_ = ac[3][i];
        sr[i] = ar * wpr[i] - ai * wpi[i] + br * wmr[i] - bi_ * wmi[i];
        si[i] = ar * wpi[i] + ai * wpr[i] + br * wmi[i] + bi_ * wmr[i];
        dr[i] = ar * wmr[i] - ai * wmi[i] + br * wpr[i] - bi_ * wpi[i];
        di[i] = ar * wmi[i] + ai * wmr[i] + br * wpi[i] + bi_ * wpr[i];
      }
#pragma unroll
      for (int s2 = 0; s2 < 2; ++s2) {
        const int o = 8 * s2;
        pf[0][0][s2] = u32x4{pk2(sr[o], sr[o + 1]), pk2(sr[o + 2], sr[o + 3]), pk2(sr[o + 4], sr[o + 5]), pk2(sr[o + 6], sr[o + 7])};
        pf[0][1][s2] = u32x4{pk2(si[o], si[o + 1]), pk2(si[o + 2], si[o + 3]), pk2(si[o + 4], si[o + 5]), pk2(si[o + 6], si[o + 7])};
        pf[1][0][s2] = u32x4{pk2(dr[o], dr[o + 1]), pk2(dr[o + 2], dr[o + 3]), pk2(dr[o + 4], dr[o + 5]), pk2(dr[o + 6], dr[o + 7])};
        pf[1][1][s2] = u32x4{pk2(di[o], di[o + 1]), pk2(di[o + 2], di[o + 3]), pk2(di[o + 4], di[o + 5]), pk2(di[o + 6], di[o + 7])};
      }
    }
    const unsigned zl = L0 + 32768 + w * 8192;
#pragma unroll
    for (int par = 0; par < 2; ++par) {
      f32x16 zre, zim;
#pragma unroll
      for (int i = 0; i < 16; ++i) { zre[i] = 0.f; zim[i] = 0.f; }
#pragma unroll
      for (int s2 = 0; s2 < 2; ++s2) {
        unsigned a = L0 + 16384 + (par * 2 + s2) * 3072 + l * 16;
        u32x4 mc = lds128(a), ms = lds128(a + 1024), mn = lds128(a + 2048);
        zre = mfma32(mc, pf[par][0][s2], zre);
        zim = mfma32(ms, pf[par][0][s2], zim);
        zre = mfma32(mn, pf[par][1][s2], zre);
        zim = mfma32(mc, pf[par][1][s2], zim);
      }
#pragma unroll
      for (int i = 0; i < 16; ++i) {
        int rr = (i & 3) + 8 * (i >> 2) + 4 * h;
        int g = 4 * (par + 2 * (rr >> 2)) + (rr & 3);
        *(LAS unsigned*)(uintptr_t)(zl + g * 128 + c31 * 4) = pk2(zre[i], zim[i]);
      }
    }
    STAMP(4);
    {
      char* zb = ws + WS_Z + (size_t)k1 * 1024 + w * 128;
#pragma unroll
      for (int i = 0; i < 8; ++i) {
        u32x4 v = lds128(zl + i * 1024 + l * 16);
        st128_sc1(zb, (unsigned)((8 * i + (l >> 3)) * 32768 + (l & 7) * 16), v);
      }
    }
    WAITV(0);
    STAMP(5);
    if (l == 0) st32_sc1(ws + WS_FLAGZ, (unsigned)((w >> 1) * 256 + k1 * 8 + (w & 1) * 4), tag);
  }
  asm volatile("s_waitcnt vmcnt(0)" : "+v"(code_touch)::"memory");
}

extern "C" void kernel_launch(void* const* d_in, const int* in_sizes, int n_in, void* d_out, int out_size, void* d_ws,
                              size_t ws_size, hipStream_t stream) {
  (void)in_sizes; (void)n_in; (void)out_size; (void)ws_size;
  (void)hipFuncSetAttribute((const void*)gf_glu_kernel, hipFuncAttributeMaxDynamicSharedMemorySize, L_TOTAL);
  gf_glu_kernel<<<256, 512, L_TOTAL, stream>>>((const char*)d_in[0], (const char*)d_in[1], (const char*)d_in[2],
                                               (const char*)d_in[4], (const char*)d_in[5], (const char*)d_in[6],
                                               (char*)d_out, (char*)d_ws);
}
```

```cpp
#define KDEBUG 1
#define PROBE 0
#include <hip/hip_runtime.h>
#include <hip/hip_bf16.h>
#include <stdint.h>

typedef __bf16 bf16x8 __attribute__((ext_vector_type(8)));
typedef __bf16 bf16x2 __attribute__((ext_vector_type(2)));
typedef short s16x4 __attribute__((ext_vector_type(4)));
typedef float f32x2 __attribute__((ext_vector_type(2)));
typedef float f32x4 __attribute__((ext_vector_type(4)));
typedef float f32x16 __attribute__((ext_vector_type(16)));
typedef unsigned u32x4 __attribute__((ext_vector_type(4)));
typedef unsigned u32x2 __attribute__((ext_vector_type(2)));
#define LAS __attribute__((address_space(3)))
#define DI __device__ __forceinline__

constexpr unsigned WS_EPOCH = 0;
constexpr unsigned WS_FLAGE = 4096;
constexpr unsigned WS_FLAGZ = 8192;
constexpr unsigned WS_FLAGH = 65536;
constexpr unsigned WS_E = 1u << 20;
constexpr unsigned WS_Z = 3u << 20;
constexpr unsigned WS_H = 5u << 20;
constexpr unsigned WS_DBG = 16u << 20;

constexpr unsigned L_RING = 0;
constexpr unsigned L_F = 114688;
constexpr unsigned L_EB = L_F + 28672;
constexpr unsigned L_ST = L_EB + 8192 + 512;
constexpr unsigned L_CS = L_ST + 1024;
constexpr unsigned L_TOTAL = 163840;

constexpr unsigned SPIN_CAP = 1u << 17;
constexpr unsigned CODE_BYTES = 16384;

template <int N> struct IC { static constexpr int v = N; };
template <int B, int E, class Fn> DI void sfor(Fn&& f) {
  if constexpr (B < E) { f(IC<B>{}); sfor<B + 1, E>(f); }
}

DI unsigned pk2(float a, float b) { f32x2 t = {a, b}; bf16x2 p = __builtin_convertvector(t, bf16x2); return __builtin_bit_cast(unsigned, p); }
DI bf16x8 asbf(u32x4 v) { return __builtin_bit_cast(bf16x8, v); }
DI f32x4 mfma16(u32x4 a, u32x4 b, f32x4 c) { return __builtin_amdgcn_mfma_f32_16x16x32_bf16(asbf(a), asbf(b), c, 0, 0, 0); }
DI f32x16 mfma32(u32x4 a, u32x4 b, f32x16 c) { return __builtin_amdgcn_mfma_f32_32x32x16_bf16(asbf(a), asbf(b), c, 0, 0, 0); }
DI float dot2(unsigned a, unsigned b, float c) {
  return __builtin_amdgcn_fdot2_f32_bf16(__builtin_bit_cast(bf16x2, a), __builtin_bit_cast(bf16x2, b), c, false);
}
DI float sum8(u32x4 v, float c) { unsigned a = v.x, b = v.y, d = v.z, e = v.w; const unsigned o = 0x3f803f80u;
  return dot2(e, o, dot2(d, o, dot2(b, o, dot2(a, o, c)))); }
DI float sq8(u32x4 v, float c) { unsigned a = v.x, b = v.y, d = v.z, e = v.w;
  return dot2(e, e, dot2(d, d, dot2(b, b, dot2(a, a, c)))); }
DI float cosr(float rev) { return __builtin_amdgcn_cosf(__builtin_amdgcn_fractf(rev)); }
DI float sinr(float rev) { return __builtin_amdgcn_sinf(__builtin_amdgcn_fractf(rev)); }
DI float qsum(float v) {
  unsigned u = __builtin_bit_cast(unsigned, v);
  auto a = __builtin_amdgcn_permlane16_swap(u, u, false, false);
  unsigned a0 = a[0], a1 = a[1];
  float s = __builtin_bit_cast(float, a0) + __builtin_bit_cast(float, a1);
  u = __builtin_bit_cast(unsigned, s);
  auto b = __builtin_amdgcn_permlane32_swap(u, u, false, false);
  unsigned b0 = b[0], b1 = b[1];
  return __builtin_bit_cast(float, b0) + __builtin_bit_cast(float, b1);
}
DI u32x4 lds128(unsigned a) { return *(LAS const u32x4*)(uintptr_t)a; }
DI f32x4 lds128f(unsigned a) { return *(LAS const f32x4*)(uintptr_t)a; }
DI u32x4 ldstr(unsigned a0, unsigned a1) {
  s16x4 lo = __builtin_amdgcn_ds_read_tr16_b64_v4i16((LAS s16x4*)(uintptr_t)a0);
  s16x4 hi = __builtin_amdgcn_ds_read_tr16_b64_v4i16((LAS s16x4*)(uintptr_t)a1);
  return __builtin_bit_cast(u32x4, __builtin_shufflevector(lo, hi, 0, 1, 2, 3, 4, 5, 6, 7));
}
DI void dma16(const char* base, unsigned voff, unsigned ldst) {
  asm volatile("s_mov_b32 m0, %2\n\ts_nop 0\n\tglobal_load_lds_dwordx4 %0, %1" ::"v"(voff), "s"(base), "s"(ldst) : "memory");
}
DI void st128_sc1(char* base, unsigned voff, u32x4 v) {
  asm volatile("global_store_dwordx4 %0, %1, %2 sc1\n\ts_nop 1" ::"v"(voff), "v"(v), "s"(base) : "memory");
}
DI void st32_sc1(char* base, unsigned voff, unsigned v) {
  asm volatile("global_store_dword %0, %1, %2 sc1" ::"v"(voff), "v"(v), "s"(base) : "memory");
}
template <int IMM> DI void ld128_sc1(u32x4& dst, const char* base, unsigned voff) {
  asm volatile("global_load_dwordx4 %0, %1, %2 offset:%3 sc1" : "=v"(dst) : "v"(voff), "s"(base), "i"(IMM) : "memory");
}
#define LD128_SC1(dst, base, voff, imm) ld128_sc1<imm>(dst, base, voff)
DI void dma16v(unsigned long long addr, unsigned ldst) {
  asm volatile("s_mov_b32 m0, %1\n\ts_nop 0\n\tglobal_load_lds_dwordx4 %0, off" ::"v"(addr), "s"(ldst) : "memory");
}
#define WAITV(n) asm volatile("s_waitcnt vmcnt(%0)" ::"i"(n) : "memory")
#define BARRIER() asm volatile("s_waitcnt lgkmcnt(0)\n\ts_barrier" ::: "memory")
template <class T> DI void PIN(T& x) { asm volatile("" : "+v"(x)); }

DI void poll_flags(const char* base, unsigned voff, bool active, unsigned tag) {
  for (unsigned spins = 0; spins < SPIN_CAP; ++spins) {
    unsigned got;
    asm volatile("global_load_dword %0, %1, %2 sc1\n\ts_waitcnt vmcnt(0)" : "=v"(got) : "v"(voff), "s"(base) : "memory");
    if (__all(!active || got == tag)) break;
    __builtin_amdgcn_s_sleep(2);
  }
}

DI unsigned rtime() { unsigned long long t_; asm volatile("s_memrealtime %0\n\ts_waitcnt lgkmcnt(0)" : "=s"(t_)::"memory"); return (unsigned)t_; }
#define STAMP(i) stamps[i] = rtime()
#define NSTAMP 16

__global__ __launch_bounds__(512, 2) void gf_glu_kernel(const char* __restrict__ x, const char* __restrict__ wr,
                                                        const char* __restrict__ wi, const char* __restrict__ wg,
                                                        const char* __restrict__ wu, const char* __restrict__ wd,
                                                        char* __restrict__ out, char* __restrict__ ws) {
  extern __shared__ __attribute__((aligned(16))) char smem[];
  unsigned stamps[NSTAMP];
  for (int i = 0; i < NSTAMP; ++i) stamps[i] = 0;
  STAMP(0);
  const int tid = threadIdx.x;
  const int w = __builtin_amdgcn_readfirstlane(tid >> 6);
  const int l = tid & 63, r = l & 15, q = l >> 4;
  const int b = blockIdx.x, xcd = b & 7, bi = b >> 3;
  const unsigned L0 = (unsigned)(uintptr_t)(LAS char*)smem;

  unsigned code_touch;
  {
    unsigned long long pc;
    asm volatile("s_getpc_b64 %0" : "=s"(pc));
    const char* pcb = (const char*)(pc & ~127ull);
    unsigned off = tid * 64;
    off = off < CODE_BYTES ? off : 0;
    asm volatile("global_load_dword %0, %1, %2" : "=v"(code_touch) : "v"(off), "s"(pcb) : "memory");
  }
  unsigned epoch;
  asm volatile("s_load_dword %0, %1, 0x0" : "=s"(epoch) : "s"(ws) : "memory");
#define GET_TAG() asm volatile("s_waitcnt lgkmcnt(0)" : "+s"(epoch)); const unsigned tag = epoch * 0x9E3779B1u + 0x7F4A7C15u

  if (bi < 28) {
    const int pn = 4 * (xcd & 3) + (bi & 3), tt = 7 * (xcd >> 2) + (bi >> 2);
    const int rot = ((bi >> 2) + 2 * (bi & 3)) % 7;
    const int m = w & 3, nh = w >> 2;
    const int rt = 4 * xcd + (bi & 3), ct = bi >> 2;
    auto rk = [&](int j) { int k = j + rot; return k >= 7 ? k - 7 : k; };

    unsigned xoff[2], woff[2], doff[2];
#pragma unroll
    for (int i = 0; i < 2; ++i) {
      int R = 16 * w + 8 * i + (l >> 3);
      int mm = R >> 5, n1 = R & 31;
      int n2 = (pn & 1) + 8 * (pn >> 1) + 2 * mm;
      xoff[i] = (unsigned)((64 * n1 + n2) * 896 + (((l & 7) ^ ((R >> 1) & 7)) << 4));
      int kgd = R >> 5, rr = R & 31;
      doff[i] = (unsigned)((128 * (kgd >> 1) + 32 * (kgd & 1) + rr) * 896 + ct * 128 + (((l & 7) ^ (((R >> 1) & 3) << 1)) << 4));
    }
    unsigned long long wlane[2];
#pragma unroll
    for (int i = 0; i < 2; ++i) {
      int row = 8 * w + 4 * i + q;
      int f = (row & 3) | (((row >> 3) & 1) << 2);
      int ch = r ^ (f << 1);
      woff[i] = (unsigned)(row * 3584 + tt * 256 + ((ch & 7) << 4));
      wlane[i] = (unsigned long long)((ch >> 3) ? wu : wg) + woff[i];
    }
    const unsigned ldw = L0 + L_RING + w * 2048;
    auto issue = [&](auto TI) {
      constexpr int T = decltype(TI)::v;
      constexpr int slot = T % 7, kind = T / 7;
      const int kt = rk(slot);
      if constexpr (kind == 0) {
        const char* bs = x + kt * 128;
        dma16(bs, xoff[0], ldw + slot * 16384);
        dma16(bs, xoff[1], ldw + slot * 16384 + 1024);
      } else if constexpr (kind <= 2) {
        const unsigned long long add = (unsigned long long)(kt * (64 * 3584) + (kind - 1) * 128);
        unsigned long long a0 = wlane[0] + add, a1 = wlane[1] + add;
        dma16v(a0, ldw + slot * 16384);
        dma16v(a1, ldw + slot * 16384 + 1024);
      } else {
        const char* bs = wd + (kt * 256 + (kind - 3) * 64) * 896;
        dma16(bs, doff[0], ldw + slot * 16384);
        dma16(bs, doff[1], ldw + slot * 16384 + 1024);
      }
    };
    sfor<0, 7>([&](auto T) { issue(T); });
    STAMP(1);

#pragma unroll
    for (int it = 0; it < 4; ++it) {
      const int item = w + 8 * it;
      if (item < 28) {
        const int unit = item / 14, ks = item % 14;
        int c = 16 * tt + 8 * unit + (r >> 1);
        float ph = (r & 1) ? 0.25f : 0.0f;
        if (c == 0 && (r & 1)) { c = 224; ph = 0.0f; }
        const int d0 = 64 * rk(ks >> 1) + 32 * (ks & 1) + 8 * q;
        const float cf = (float)c * (1.0f / 448.0f);
        float v[8];
#pragma unroll
        for (int e = 0; e < 8; ++e) v[e] = cosr(__builtin_fmaf((float)(d0 + e), cf, ph));
        u32x4 pk = {pk2(v[0], v[1]), pk2(v[2], v[3]), pk2(v[4], v[5]), pk2(v[6], v[7])};
        *(LAS u32x4*)(uintptr_t)(L0 + L_F + item * 1024 + l * 16) = pk;
      }
    }
    u32x4 p1c[2], p1s[2];
#pragma unroll
    for (int a = 0; a < 2; ++a) {
      float vc[8], vs[8];
#pragma unroll
      for (int e = 0; e < 8; ++e) {
        int n1 = 16 * ((e >> 2) ^ nh) + 4 * q + (e & 3);
        float rev = (float)(((16 * a + r) * n1) & 31) * (1.0f / 32.0f);
        vc[e] = __builtin_amdgcn_cosf(rev); vs[e] = __builtin_amdgcn_sinf(rev);
      }
      p1c[a] = u32x4{pk2(vc[0], vc[1]), pk2(vc[2], vc[3]), pk2(vc[4], vc[5]), pk2(vc[6], vc[7])};
      p1s[a] = u32x4{pk2(vs[0], vs[1]), pk2(vs[2], vs[3]), pk2(vs[4], vs[5]), pk2(vs[6], vs[7])};
    }
    GET_TAG();
    STAMP(2);

    u32x4 xf[14][2];
    f32x4 accD[2] = {{0, 0, 0, 0}, {0, 0, 0, 0}};
    float ssum = 0.f, ssq = 0.f, cF = 0.f;
    {
      unsigned xa[2][2];
#pragma unroll
      for (int i = 0; i < 2; ++i) {
        int R = 32 * m + 16 * (i ^ nh) + r;
#pragma unroll
        for (int kk = 0; kk < 2; ++kk) xa[i][kk] = L0 + L_RING + R * 128 + ((((4 * kk + q) ^ ((R >> 1) & 7))) << 4);
      }
      const unsigned fa = L0 + L_F + nh * 14336 + l * 16;
      sfor<0, 7>([&](auto IT) {
        constexpr int it = decltype(IT)::v;
        WAITV(it == 0 ? 12 : 10);
        BARRIER();
        if constexpr (it >= 1) issue(IC<it + 6>{});
#pragma unroll
        for (int kk = 0; kk < 2; ++kk) {
          const int ks = 2 * it + kk;
          xf[ks][0] = lds128(xa[0][kk] + it * 16384);
          xf[ks][1] = lds128(xa[1][kk] + it * 16384);
          u32x4 ff = lds128(fa + ks * 1024);
          accD[0] = mfma16(xf[ks][0], ff, accD[0]);
          accD[1] = mfma16(xf[ks][1], ff, accD[1]);
          ssum = sum8(xf[ks][0], ssum);
          ssq = sq8(xf[ks][0], ssq);
          cF = sum8(ff, cF);
        }
      });
      ssum = qsum(ssum); ssq = qsum(ssq); cF = qsum(cF);
      float mean = ssum * (1.0f / 448.0f);
      float var = __builtin_fmaf(-mean, mean, ssq * (1.0f / 448.0f));
      var = var > 0.f ? var : 0.f;
      float rstd = __builtin_amdgcn_rsqf(var + 1e-5f);
      if (q == 0) {
        int R = 32 * m + 16 * nh + r;
        *(LAS float*)(uintptr_t)(L0 + L_ST + R * 4) = mean;
        *(LAS float*)(uintptr_t)(L0 + L_ST + 512 + R * 4) = rstd;
      }
    }
    STAMP(3);

    unsigned wa[4];
    {
      const int qq = r >> 2, pp = r & 3;
      const int f = qq | ((q & 1) << 2);
#pragma unroll
      for (int i = 0; i < 4; ++i) {
        int lam = i ^ m;
        int u = 2 * nh + (lam & 1) + 4 * (lam >> 1);
        wa[i] = L0 + L_RING + (8 * q + qq) * 256 + ((u ^ f) << 5) + pp * 8;
      }
    }
    auto rdw = [&](u32x4* dst, int slot, int kk) {
#pragma unroll
      for (int i = 0; i < 4; ++i) dst[i] = ldstr(wa[i] + slot * 16384 + kk * 8192, wa[i] + slot * 16384 + kk * 8192 + 1024);
    };
    f32x4 acc[2][4][2];
#pragma unroll
    for (int a = 0; a < 2; ++a)
#pragma unroll
      for (int i = 0; i < 4; ++i) { acc[a][i][0] = f32x4{0, 0, 0, 0}; acc[a][i][1] = f32x4{0, 0, 0, 0}; }
    float mrow[2], r2row[2];
    const int g1 = 4 * pn + m;
    auto h_epilogue = [&](auto PS) {
      constexpr int ps = decltype(PS)::v;
      f32x4 cs[4];
#pragma unroll
      for (int i = 0; i < 4; ++i) {
        int lam = i ^ m;
        int u = 2 * nh + (lam & 1) + 4 * (lam >> 1);
        cs[i] = lds128f(L0 + L_CS + (ps * 8 + u) * 64 + q * 16);
      }
      const unsigned hoff = (unsigned)(((((g1 * 2 + ps) * 7 + (tt >> 1)) * 4 + 2 * (tt & 1) + nh) * 2) * 1024 + l * 16);
#pragma unroll
      for (int i2 = 0; i2 < 2; ++i2) {
        float hv[8];
#pragma unroll
        for (int i = 0; i < 2; ++i)
#pragma unroll
          for (int e = 0; e < 4; ++e) {
            float a = __builtin_fmaf(-mrow[i2], cs[i][e], acc[ps][i][i2][e]);
            float c = __builtin_fmaf(-mrow[i2], cs[i + 2][e], acc[ps][i + 2][i2][e]);
            hv[4 * i + e] = (a * r2row[i2]) * c;
          }
        u32x4 pk = {pk2(hv[0], hv[1]), pk2(hv[2], hv[3]), pk2(hv[4], hv[5]), pk2(hv[6], hv[7])};
        st128_sc1(ws + WS_H, hoff + ((i2 ^ nh) << 10), pk);
      }
    };
    auto h_flag = [&](int ps) {
      if (l == 0) st32_sc1(ws + WS_FLAGH, (unsigned)(((g1 * 2 + ps) * 4 + 2 * (tt & 1) + nh) * 128 + (tt >> 1) * 4), tag);
    };

    u32x4 fa_[4], fb_[4];
    float csum = 0.f;
    sfor<7, 21>([&](auto IT) {
      constexpr int it = decltype(IT)::v;
      constexpr int ps = (it - 7) / 7, j = (it - 7) % 7;
      WAITV(8);
      BARRIER();
      if constexpr (it == 8) {
        u32x4 v = lds128(L0 + L_EB + tid * 16);
        st128_sc1(ws + WS_E, (unsigned)((((tid >> 4) * 16 + pn) * 256 + 16 * tt + (tid & 15)) * 16), v);
        if (tt == 0 && tid < 32) {
          u32x4 v2 = lds128(L0 + L_EB + 8192 + tid * 16);
          st128_sc1(ws + WS_E, (unsigned)(((tid * 16 + pn) * 256 + 224) * 16), v2);
        }
      }
      if constexpr (it == 13) {
        if (tid == 0) st32_sc1(ws + WS_FLAGE, (unsigned)((tt >> 1) * 128 + pn * 8 + (tt & 1) * 4), tag);
      }
      if constexpr (it == 20) h_flag(0);
      issue(IC<it + 6>{});
      if constexpr (it == 7) {
        rdw(fa_, 0, 0);
        f32x4 mD[2], rD[2];
#pragma unroll
        for (int i = 0; i < 2; ++i) {
          int R0 = 32 * m + 16 * (i ^ nh);
          mrow[i] = *(LAS const float*)(uintptr_t)(L0 + L_ST + (R0 + r) * 4);
          float rs = *(LAS const float*)(uintptr_t)(L0 + L_ST + 512 + (R0 + r) * 4);
          r2row[i] = rs * rs;
          mD[i] = lds128f(L0 + L_ST + (R0 + 4 * q) * 4);
          rD[i] = lds128f(L0 + L_ST + 512 + (R0 + 4 * q) * 4);
        }
        float dv[8];
#pragma unroll
        for (int i = 0; i < 2; ++i)
#pragma unroll
          for (int e = 0; e < 4; ++e) dv[4 * i + e] = rD[i][e] * __builtin_fmaf(-mD[i][e], cF, accD[i][e]);
        u32x4 df = {pk2(dv[0], dv[1]), pk2(dv[2], dv[3]), pk2(dv[4], dv[5]), pk2(dv[6], dv[7])};
        const bool slot0 = (tt == 0) && (nh == 0) && (r < 2);
        const float kc = (slot0 && r == 1) ? 0.f : 1.f;
        const float ko = (r & 1) ? -1.f : (slot0 ? 0.f : 1.f);
#pragma unroll
        for (int a = 0; a < 2; ++a) {
          f32x4 z = {0, 0, 0, 0};
          f32x4 tc = mfma16(p1c[a], df, z), ts = mfma16(p1s[a], df, z);
#pragma unroll
          for (int e = 0; e < 4; ++e) {
            float tse = ts[e], tce = tc[e];
            float oth = __builtin_bit_cast(float, __builtin_amdgcn_update_dpp(0, __builtin_bit_cast(int, tse), 0xB1, 0xf, 0xf, true));
            float ev = __builtin_fmaf(ko, oth, kc * tce);
            int k1 = 16 * a + 4 * q + e;
            *(LAS unsigned short*)(uintptr_t)(L0 + L_EB + k1 * 256 + (16 * nh + r) * 8 + m * 2) = (unsigned short)pk2(ev, 0.f);
            if (slot0) {
              float nv = r ? tce : -oth;
              *(LAS unsigned short*)(uintptr_t)(L0 + L_EB + 8192 + k1 * 16 + (r ^ 1) * 8 + m * 2) = (unsigned short)pk2(nv, 0.f);
            }
          }
        }
      }
      rdw(fb_, j, 1);
#pragma unroll
      for (int i = 0; i < 4; ++i) {
        acc[ps][i][0] = mfma16(fa_[i], xf[2 * j][0], acc[ps][i][0]);
        acc[ps][i][1] = mfma16(fa_[i], xf[2 * j][1], acc[ps][i][1]);
      }
      csum = sum8(fa_[0], csum);
      if constexpr (it < 20) rdw(fa_, (j + 1) % 7, 0);
#pragma unroll
      for (int i = 0; i < 4; ++i) {
        acc[ps][i][0] = mfma16(fb_[i], xf[2 * j + 1][0], acc[ps][i][0]);
        acc[ps][i][1] = mfma16(fb_[i], xf[2 * j + 1][1], acc[ps][i][1]);
      }
      csum = sum8(fb_[0], csum);
      if constexpr (j == 6) {
        float cs = qsum(csum);
        csum = 0.f;
        int u = 2 * nh + (m & 1) + 4 * (m >> 1);
        if (q == 0) *(LAS float*)(uintptr_t)(L0 + L_CS + (ps * 8 + u) * 64 + r * 4) = cs;
      }
      if constexpr (it == 14) h_epilogue(IC<0>{});
      if constexpr (it == 13) STAMP(4);
    });
    STAMP(5);
    BARRIER();
    h_epilogue(IC<1>{});
    issue(IC<27>{});
    STAMP(6);

    const int kg = w >> 1, half = w & 1;
    const int g2 = 2 * rt + half;
    unsigned da[4][2];
    {
      const int qq = r >> 2, pp = r & 3;
#pragma unroll
      for (int t2 = 0; t2 < 2; ++t2) {
        int row = 32 * kg + 16 * (t2 ^ half) + 4 * q + qq;
#pragma unroll
        for (int dt = 0; dt < 4; ++dt) da[dt][t2] = L0 + L_RING + row * 128 + ((dt ^ ((row >> 1) & 3)) << 5) + pp * 8;
      }
    }
    f32x4 oacc[4][2];
#pragma unroll
    for (int dt = 0; dt < 4; ++dt) { oacc[dt][0] = f32x4{0, 0, 0, 0}; oacc[dt][1] = f32x4{0, 0, 0, 0}; }
    u32x4 hf[7][2];
    auto load_h = [&](int ps) {
      sfor<0, 7>([&](auto J) {
        constexpr int j = decltype(J)::v;
        const char* hb = ws + WS_H + (size_t)((((g2 * 2 + ps) * 7 + rk(j)) * 4 + kg) * 2048);
        const unsigned vo = l * 16;
        LD128_SC1(hf[j][0], hb, vo, 0);
        LD128_SC1(hf[j][1], hb, vo, 1024);
      });
    };
    const unsigned hfl = (unsigned)((g2 * 2 * 4 + kg) * 128 + (l & 7) * 4);
    {
      unsigned got;
      asm volatile("global_load_dword %0, %1, %2 sc1\n\ts_waitcnt vmcnt(0)" : "=v"(got) : "v"(hfl), "s"(ws + WS_FLAGH) : "memory");
      h_flag(1);
      if (!__all(l >= 7 || got == tag)) poll_flags(ws + WS_FLAGH, hfl, l < 7, tag);
    }
    STAMP(7);
    load_h(0);
    sfor<21, 35>([&](auto IT) {
      constexpr int it = decltype(IT)::v;
      constexpr int ps = (it - 21) / 7, j = (it - 21) % 7;
      if constexpr (it == 28) {
        STAMP(8);
        poll_flags(ws + WS_FLAGH, hfl + 512, l < 7, tag);
        STAMP(9);
        load_h(1);
      }
      if constexpr (ps == 0) {
        WAITV(2 * (6 - j) + (j >= 2 ? 2 * (j - 1) : 0));
      } else {
        WAITV(j == 6 ? 0 : 2 * (6 - j) + (j >= 1 ? 2 : 0));
      }
      PIN(hf[j][0]); PIN(hf[j][1]);
      BARRIER();
      if constexpr (it >= 22 && it <= 28) issue(IC<it + 6>{});
#pragma unroll
      for (int dt = 0; dt < 4; ++dt) {
        u32x4 af = ldstr(da[dt][0] + j * 16384, da[dt][1] + j * 16384);
        oacc[dt][0] = mfma16(af, hf[j][0], oacc[dt][0]);
        oacc[dt][1] = mfma16(af, hf[j][1], oacc[dt][1]);
      }
    });
    STAMP(10);

    poll_flags(ws + WS_FLAGZ, (unsigned)(kg * 256 + l * 4), true, tag);
    STAMP(11);
    u32x4 zr[8];
    u32x4 xres;
    {
      const char* zb = ws + WS_Z + (size_t)g2 * 32768 + kg * 256;
      sfor<0, 8>([&](auto I) {
        constexpr int i = decltype(I)::v;
        const unsigned vo = (4 * i + q) * 1024 + r * 16;
        LD128_SC1(zr[i], zb, vo, 0);
      });
      int row = tid >> 3;
      int gg = 2 * rt + (row >> 5);
      int n2 = ((gg >> 2) & 1) + 8 * (gg >> 3) + 2 * (gg & 3);
      unsigned xo = (unsigned)((64 * (row & 31) + n2) * 896 + ct * 128 + (tid & 7) * 16);
      asm volatile("global_load_dwordx4 %0, %1, %2" : "=v"(xres) : "v"(xo), "s"(x) : "memory");
    }
    u32x4 p2c[2], p2s[2];
#pragma unroll
    for (int i2 = 0; i2 < 2; ++i2) {
      float vc[8], vs[8];
#pragma unroll
      for (int e = 0; e < 8; ++e) {
        float rev = (float)(((16 * i2 + r) * (8 * q + e)) & 31) * (1.0f / 32.0f);
        vc[e] = __builtin_amdgcn_cosf(rev); vs[e] = __builtin_amdgcn_sinf(rev);
      }
      p2c[i2] = u32x4{pk2(vc[0], vc[1]), pk2(vc[2], vc[3]), pk2(vc[4], vc[5]), pk2(vc[6], vc[7])};
      p2s[i2] = u32x4{pk2(vs[0], vs[1]), pk2(vs[2], vs[3]), pk2(vs[4], vs[5]), pk2(vs[6], vs[7])};
    }
    BARRIER();
    const unsigned zl = L0 + L_RING + 65536 + w * 8192;
    WAITV(1);
#pragma unroll
    for (int i = 0; i < 8; ++i) {
      PIN(zr[i]);
      int row = 4 * i + q;
      int f = (row & 3) | (((row >> 3) & 1) << 2);
      *(LAS u32x4*)(uintptr_t)(zl + row * 256 + ((r ^ (f << 1)) << 4)) = zr[i];
    }
    {
      const int qq = r >> 2, pp = r & 3;
      const int f = qq | ((q & 1) << 2);
      const unsigned zbase = zl + (8 * q + qq) * 256 + pp * 8;
#pragma unroll
      for (int bk = 0; bk < 4; ++bk) {
        unsigned yy[2][4];
#pragma unroll
        for (int aa = 0; aa < 2; ++aa) {
          const int a = 2 * bk + aa;
          const unsigned ad = zbase + ((a ^ f) << 5);
          u32x4 zf = ldstr(ad, ad + 1024);
#pragma unroll
          for (int i2 = 0; i2 < 2; ++i2) {
            f32x4 z = {0, 0, 0, 0};
            f32x4 tc = mfma16(zf, p2c[i2], z), ts = mfma16(zf, p2s[i2], z);
            yy[i2][2 * aa] = pk2(tc[0] - ts[1], tc[1] + ts[0]);
            yy[i2][2 * aa + 1] = pk2(tc[2] - ts[3], tc[3] + ts[2]);
          }
        }
        u32x4 y0 = {yy[0][0], yy[0][1], yy[0][2], yy[0][3]}, y1 = {yy[1][0], yy[1][1], yy[1][2], yy[1][3]};
#pragma unroll
        for (int dt = 0; dt < 4; ++dt) {
          const float df = (float)(64 * ct + 16 * dt + r) * (1.0f / 448.0f);
          unsigned gg[4];
#pragma unroll
          for (int i = 0; i < 4; ++i) {
            int c = 64 * kg + 16 * bk + 8 * (i >> 1) + 2 * q + (i & 1);
            float rev = __builtin_amdgcn_fractf((float)c * df);
            gg[i] = pk2(__builtin_amdgcn_cosf(rev), -__builtin_amdgcn_sinf(rev));
          }
          u32x4 gf = {gg[0], gg[1], gg[2], gg[3]};
          oacc[dt][0] = mfma16(gf, y0, oacc[dt][0]);
          oacc[dt][1] = mfma16(gf, y1, oacc[dt][1]);
        }
      }
    }
    STAMP(12);
#pragma unroll
    for (int i2 = 0; i2 < 2; ++i2) {
      int row = 32 * half + 16 * i2 + r;
#pragma unroll
      for (int dt = 0; dt < 4; ++dt)
        *(LAS f32x4*)(uintptr_t)(L0 + L_RING + kg * 16384 + row * 256 + (((4 * dt + q) ^ (row & 7)) << 4)) = oacc[dt][i2];
    }
    BARRIER();
    {
      int row = tid >> 3, cp = tid & 7;
      f32x4 s0 = {0, 0, 0, 0}, s1 = {0, 0, 0, 0};
#pragma unroll
      for (int k = 0; k < 4; ++k) {
        s0 += lds128f(L0 + L_RING + k * 16384 + row * 256 + (((2 * cp) ^ (row & 7)) << 4));
        s1 += lds128f(L0 + L_RING + k * 16384 + row * 256 + (((2 * cp + 1) ^ (row & 7)) << 4));
      }
      WAITV(0);
      PIN(xres);
      unsigned x0 = xres.x, x1 = xres.y, x2 = xres.z, x3 = xres.w;
      auto lo = [](unsigned u) { return __builtin_bit_cast(float, u << 16); };
      auto hi = [](unsigned u) { return __builtin_bit_cast(float, u & 0xffff0000u); };
      u32x4 o = {pk2(s0[0] + lo(x0), s0[1] + hi(x0)), pk2(s0[2] + lo(x1), s0[3] + hi(x1)),
                 pk2(s1[0] + lo(x2), s1[1] + hi(x2)), pk2(s1[2] + lo(x3), s1[3] + hi(x3))};
      int gg = 2 * rt + (row >> 5);
      int n2 = ((gg >> 2) & 1) + 8 * (gg >> 3) + 2 * (gg & 3);
      *(u32x4*)(out + (size_t)((64 * (row & 31) + n2) * 896 + ct * 128 + cp * 16)) = o;
    }
    if (b == 0 && tid == 0) *(unsigned*)(ws + WS_EPOCH) = tag;
    STAMP(13);
  } else {
    const int k1 = (bi - 28) * 8 + xcd;
    const int c31 = l & 31, h = l >> 5;
    const int c = 32 * w + c31;
    const bool cok = c < 225;
    float wpr[16], wpi[16], wmr[16], wmi[16];
    {
      const unsigned short* pr = (const unsigned short*)wr;
      const unsigned short* pi = (const unsigned short*)wi;
      const int cc = cok ? c : 224;
      const float sc = cok ? ((c == 0 || c == 224) ? 1.0f : 2.0f) * (1.0f / (2048.0f * 448.0f)) : 0.f;
#pragma unroll
      for (int i = 0; i < 16; ++i) {
        int k2 = (i & 3) + 8 * (i >> 2) + 4 * h;
        int i0 = (k1 + 32 * k2) * 225 + cc, i1 = i0 + 1024 * 225;
        float a0 = __builtin_bit_cast(float, (unsigned)pr[i0] << 16), b0 = __builtin_bit_cast(float, (unsigned)pi[i0] << 16);
        float a1 = __builtin_bit_cast(float, (unsigned)pr[i1] << 16), b1 = __builtin_bit_cast(float, (unsigned)pi[i1] << 16);
        wpr[i] = (a0 + a1) * sc; wpi[i] = (b0 + b1) * sc;
        wmr[i] = (a0 - a1) * sc; wmi[i] = (b0 - b1) * sc;
      }
    }
    {
      const int s = w, par = s >> 2;
      float vc[4], vs[4];
#pragma unroll
      for (int mm = 0; mm < 4; ++mm) {
        int n2 = par + 8 * (2 * (s & 3) + h) + 2 * mm;
        float rev = (float)((n2 * (k1 + 32 * c31)) & 2047) * (1.0f / 2048.0f);
        vc[mm] = __builtin_amdgcn_cosf(rev); vs[mm] = __builtin_amdgcn_sinf(rev);
      }
      u32x4 fre = {pk2(vc[0], vc[1]), pk2(vc[2], vc[3]), pk2(vs[0], vs[1]), pk2(vs[2], vs[3])};
      u32x4 fim = {pk2(-vs[0], -vs[1]), pk2(-vs[2], -vs[3]), pk2(vc[0], vc[1]), pk2(vc[2], vc[3])};
      *(LAS u32x4*)(uintptr_t)(L0 + (2 * s) * 1024 + l * 16) = fre;
      *(LAS u32x4*)(uintptr_t)(L0 + (2 * s + 1) * 1024 + l * 16) = fim;
    }
    if (w < 4) {
      const int par = w >> 1, s2 = w & 1;
      float vc[8], vs[8];
#pragma unroll
      for (int e = 0; e < 8; ++e) {
        int k2 = 16 * s2 + 8 * (e >> 2) + 4 * h + (e & 3);
        float rev = (float)(((par + 2 * c31) * (k1 + 32 * k2)) & 2047) * (1.0f / 2048.0f);
        vc[e] = __builtin_amdgcn_cosf(rev); vs[e] = __builtin_amdgcn_sinf(rev);
      }
      unsigned a = L0 + 16384 + w * 3072 + l * 16;
      *(LAS u32x4*)(uintptr_t)(a) = u32x4{pk2(vc[0], vc[1]), pk2(vc[2], vc[3]), pk2(vc[4], vc[5]), pk2(vc[6], vc[7])};
      *(LAS u32x4*)(uintptr_t)(a + 1024) = u32x4{pk2(vs[0], vs[1]), pk2(vs[2], vs[3]), pk2(vs[4], vs[5]), pk2(vs[6], vs[7])};
      *(LAS u32x4*)(uintptr_t)(a + 2048) = u32x4{pk2(-vs[0], -vs[1]), pk2(-vs[2], -vs[3]), pk2(-vs[4], -vs[5]), pk2(-vs[6], -vs[7])};
    }
#pragma unroll
    for (int i = 0; i < 16; ++i) { PIN(wpr[i]); PIN(wpi[i]); PIN(wmr[i]); PIN(wmi[i]); }
    GET_TAG();
    BARRIER();
    STAMP(1);
    poll_flags(ws + WS_FLAGE, (unsigned)((w == 7 ? 0 : w) * 128 + c31 * 4), (l < 32) && (w < 7 || !(l & 1)), tag);
    STAMP(2);
    u32x4 ef[8];
    {
      const char* eb = ws + WS_E + (size_t)k1 * 65536 + w * 512;
      sfor<0, 8>([&](auto S) {
        constexpr int s = decltype(S)::v;
        const unsigned vo = ((s >> 2) + 2 * (2 * (s & 3) + h)) * 4096 + c31 * 16;
        LD128_SC1(ef[s], eb, vo, 0);
      });
    }
    f32x16 ac[4];
#pragma unroll
    for (int t = 0; t < 4; ++t)
#pragma unroll
      for (int i = 0; i < 16; ++i) ac[t][i] = 0.f;
    sfor<0, 8>([&](auto S) {
      constexpr int s = decltype(S)::v;
      u32x4 fre = lds128(L0 + (2 * s) * 1024 + l * 16), fim = lds128(L0 + (2 * s + 1) * 1024 + l * 16);
      WAITV(7 - s);
      PIN(ef[s]);
      u32x4 e = ef[s];
      if (!cok) e = u32x4{0, 0, 0, 0};
      ac[2 * (s >> 2)] = mfma32(fre, e, ac[2 * (s >> 2)]);
      ac[2 * (s >> 2) + 1] = mfma32(fim, e, ac[2 * (s >> 2) + 1]);
    });
    STAMP(3);
    u32x4 pf[2][2][2];
    {
      float sr[16], si[16], dr[16], di[16];
#pragma unroll
      for (int i = 0; i < 16; ++i) {
        float ar = ac[0][i], ai = ac[1][i], br = ac[2][i], bi_ = ac[3][i];
        sr[i] = ar * wpr[i] - ai * wpi[i] + br * wmr[i] - bi_ * wmi[i];
        si[i] = ar * wpi[i] + ai * wpr[i] + br * wmi[i] + bi_ * wmr[i];
        dr[i] = ar * wmr[i] - ai * wmi[i] + br * wpr[i] - bi_ * wpi[i];
        di[i] = ar * wmi[i] + ai * wmr[i] + br * wpi[i] + bi_ * wpr[i];
      }
#pragma unroll
      for (int s2 = 0; s2 < 2; ++s2) {
        const int o = 8 * s2;
        pf[0][0][s2] = u32x4{pk2(sr[o], sr[o + 1]), pk2(sr[o + 2], sr[o + 3]), pk2(sr[o + 4], sr[o + 5]), pk2(sr[o + 6], sr[o + 7])};
        pf[0][1][s2] = u32x4{pk2(si[o], si[o + 1]), pk2(si[o + 2], si[o + 3]), pk2(si[o + 4], si[o + 5]), pk2(si[o + 6], si[o + 7])};
        pf[1][0][s2] = u32x4{pk2(dr[o], dr[o + 1]), pk2(dr[o + 2], dr[o + 3]), pk2(dr[o + 4], dr[o + 5]), pk2(dr[o + 6], dr[o + 7])};
        pf[1][1][s2] = u32x4{pk2(di[o], di[o + 1]), pk2(di[o + 2], di[o + 3]), pk2(di[o + 4], di[o + 5]), pk2(di[o + 6], di[o + 7])};
      }
    }
    const unsigned zl = L0 + 32768 + w * 8192;
#pragma unroll
    for (int par = 0; par < 2; ++par) {
      f32x16 zre, zim;
#pragma unroll
      for (int i = 0; i < 16; ++i) { zre[i] = 0.f; zim[i] = 0.f; }
#pragma unroll
      for (int s2 = 0; s2 < 2; ++s2) {
        unsigned a = L0 + 16384 + (par * 2 + s2) * 3072 + l * 16;
        u32x4 mc = lds128(a), ms = lds128(a + 1024), mn = lds128(a + 2048);
        zre = mfma32(mc, pf[par][0][s2], zre);
        zim = mfma32(ms, pf[par][0][s2], zim);
        zre = mfma32(mn, pf[par][1][s2], zre);
        zim = mfma32(mc, pf[par][1][s2], zim);
      }
#pragma unroll
      for (int i = 0; i < 16; ++i) {
        int rr = (i & 3) + 8 * (i >> 2) + 4 * h;
        int g = 4 * (par + 2 * (rr >> 2)) + (rr & 3);
        *(LAS unsigned*)(uintptr_t)(zl + g * 128 + c31 * 4) = pk2(zre[i], zim[i]);
      }
    }
    STAMP(4);
    {
      char* zb = ws + WS_Z + (size_t)k1 * 1024 + w * 128;
#pragma unroll
      for (int i = 0; i < 8; ++i) {
        u32x4 v = lds128(zl + i * 1024 + l * 16);
        st128_sc1(zb, (unsigned)((8 * i + (l >> 3)) * 32768 + (l & 7) * 16), v);
      }
    }
    WAITV(0);
    STAMP(5);
    if (l == 0) st32_sc1(ws + WS_FLAGZ, (unsigned)((w >> 1) * 256 + k1 * 8 + (w & 1) * 4), tag);
  }
  asm volatile("s_waitcnt vmcnt(0)" : "+v"(code_touch)::"memory");
  if (b == 0 && w == 0) { unsigned tgt = 6000u + 4u * (stamps[PROBE] - stamps[0]); while (rtime() - stamps[0] < tgt) __builtin_amdgcn_s_sleep(1); }
  if (l == 0) {
    unsigned* dbg = (unsigned*)(ws + WS_DBG) + (b * 8 + w) * NSTAMP;
    for (int i = 0; i < NSTAMP; ++i) dbg[i] = stamps[i];
  }
}

extern "C" void kernel_launch(void* const* d_in, const int* in_sizes, int n_in, void* d_out, int out_size, void* d_ws,
                              size_t ws_size, hipStream_t stream) {
  (void)in_sizes; (void)n_in; (void)out_size; (void)ws_size;
  (void)hipFuncSetAttribute((const void*)gf_glu_kernel, hipFuncAttributeMaxDynamicSharedMemorySize, L_TOTAL);
  gf_glu_kernel<<<256, 512, L_TOTAL, stream>>>((const char*)d_in[0], (const char*)d_in[1], (const char*)d_in[2],
                                               (const char*)d_in[4], (const char*)d_in[5], (const char*)d_in[6],
                                               (char*)d_out, (char*)d_ws);
}
```

```cpp
#define KDEBUG 1
#define PROBE 0
#include <hip/hip_runtime.h>
#include <hip/hip_bf16.h>
#include <stdint.h>

typedef __bf16 bf16x8 __attribute__((ext_vector_type(8)));
typedef __bf16 bf16x2 __attribute__((ext_vector_type(2)));
typedef short s16x4 __attribute__((ext_vector_type(4)));
typedef float f32x2 __attribute__((ext_vector_type(2)));
typedef float f32x4 __attribute__((ext_vector_type(4)));
typedef float f32x16 __attribute__((ext_vector_type(16)));
typedef unsigned u32x4 __attribute__((ext_vector_type(4)));
typedef unsigned u32x2 __attribute__((ext_vector_type(2)));
#define LAS __attribute__((address_space(3)))
#define DI __device__ __forceinline__

constexpr unsigned WS_EPOCH = 0;
constexpr unsigned WS_FLAGE = 4096;
constexpr unsigned WS_FLAGZ = 8192;
constexpr unsigned WS_FLAGH = 65536;
constexpr unsigned WS_E = 1u << 20;
constexpr unsigned WS_Z = 3u << 20;
constexpr unsigned WS_H = 5u << 20;
constexpr unsigned WS_DBG = 16u << 20;

constexpr unsigned L_RING = 0;
constexpr unsigned L_F = 114688;
constexpr unsigned L_EB = L_F + 28672;
constexpr unsigned L_ST = L_EB + 8192 + 512;
constexpr unsigned L_CS = L_ST + 1024;
constexpr unsigned L_TOTAL = 163840;

constexpr unsigned SPIN_CAP = 1u << 17;
constexpr unsigned CODE_BYTES = 16384;

template <int N> struct IC { static constexpr int v = N; };
template <int B, int E, class Fn> DI void sfor(Fn&& f) {
  if constexpr (B < E) { f(IC<B>{}); sfor<B + 1, E>(f); }
}

DI unsigned pk2(float a, float b) { f32x2 t = {a, b}; bf16x2 p = __builtin_convertvector(t, bf16x2); return __builtin_bit_cast(unsigned, p); }
DI bf16x8 asbf(u32x4 v) { return __builtin_bit_cast(bf16x8, v); }
DI f32x4 mfma16(u32x4 a, u32x4 b, f32x4 c) { return __builtin_amdgcn_mfma_f32_16x16x32_bf16(asbf(a), asbf(b), c, 0, 0, 0); }
DI f32x16 mfma32(u32x4 a, u32x4 b, f32x16 c) { return __builtin_amdgcn_mfma_f32_32x32x16_bf16(asbf(a), asbf(b), c, 0, 0, 0); }
DI float dot2(unsigned a, unsigned b, float c) {
  return __builtin_amdgcn_fdot2_f32_bf16(__builtin_bit_cast(bf16x2, a), __builtin_bit_cast(bf16x2, b), c, false);
}
DI float sum8(u32x4 v, float c) { unsigned a = v.x, b = v.y, d = v.z, e = v.w; const unsigned o = 0x3f803f80u;
  return dot2(e, o, dot2(d, o, dot2(b, o, dot2(a, o, c)))); }
DI float sq8(u32x4 v, float c) { unsigned a = v.x, b = v.y, d = v.z, e = v.w;
  return dot2(e, e, dot2(d, d, dot2(b, b, dot2(a, a, c)))); }
DI float cosr(float rev) { return __builtin_amdgcn_cosf(__builtin_amdgcn_fractf(rev)); }
DI float sinr(float rev) { return __builtin_amdgcn_sinf(__builtin_amdgcn_fractf(rev)); }
DI float qsum(float v) {
  unsigned u = __builtin_bit_cast(unsigned, v);
  auto a = __builtin_amdgcn_permlane16_swap(u, u, false, false);
  unsigned a0 = a[0], a1 = a[1];
  float s = __builtin_bit_cast(float, a0) + __builtin_bit_cast(float, a1);
  u = __builtin_bit_cast(unsigned, s);
  auto b = __builtin_amdgcn_permlane32_swap(u, u, false, false);
  unsigned b0 = b[0], b1 = b[1];
  return __builtin_bit_cast(float, b0) + __builtin_bit_cast(float, b1);
}
DI u32x4 lds128(unsigned a) { return *(LAS const u32x4*)(uintptr_t)a; }
DI f32x4 lds128f(unsigned a) { return *(LAS const f32x4*)(uintptr_t)a; }
DI u32x4 ldstr(unsigned a0, unsigned a1) {
  s16x4 lo = __builtin_amdgcn_ds_read_tr16_b64_v4i16((LAS s16x4*)(uintptr_t)a0);
  s16x4 hi = __builtin_amdgcn_ds_read_tr16_b64_v4i16((LAS s16x4*)(uintptr_t)a1);
  return __builtin_bit_cast(u32x4, __builtin_shufflevector(lo, hi, 0, 1, 2, 3, 4, 5, 6, 7));
}
DI void dma16(const char* base, unsigned voff, unsigned ldst) {
  asm volatile("s_mov_b32 m0, %2\n\ts_nop 0\n\tglobal_load_lds_dwordx4 %0, %1" ::"v"(voff), "s"(base), "s"(ldst) : "memory");
}
DI void st128_sc1(char* base, unsigned voff, u32x4 v) {
  asm volatile("global_store_dwordx4 %0, %1, %2 sc1\n\ts_nop 1" ::"v"(voff), "v"(v), "s"(base) : "memory");
}
DI void st32_sc1(char* base, unsigned voff, unsigned v) {
  asm volatile("global_store_dword %0, %1, %2 sc1" ::"v"(voff), "v"(v), "s"(base) : "memory");
}
template <int IMM> DI void ld128_sc1(u32x4& dst, const char* base, unsigned voff) {
  asm volatile("global_load_dwordx4 %0, %1, %2 offset:%3 sc1" : "=v"(dst) : "v"(voff), "s"(base), "i"(IMM) : "memory");
}
#define LD128_SC1(dst, base, voff, imm) ld128_sc1<imm>(dst, base, voff)
DI void dma16v(unsigned long long addr, unsigned ldst) {
  asm volatile("s_mov_b32 m0, %1\n\ts_nop 0\n\tglobal_load_lds_dwordx4 %0, off" ::"v"(addr), "s"(ldst) : "memory");
}
#define WAITV(n) asm volatile("s_waitcnt vmcnt(%0)" ::"i"(n) : "memory")
#define BARRIER() asm volatile("s_waitcnt lgkmcnt(0)\n\ts_barrier" ::: "memory")
template <class T> DI void PIN(T& x) { asm volatile("" : "+v"(x)); }

DI void poll_flags(const char* base, unsigned voff, bool active, unsigned tag) {
  for (unsigned spins = 0; spins < SPIN_CAP; ++spins) {
    unsigned got;
    asm volatile("global_load_dword %0, %1, %2 sc1\n\ts_waitcnt vmcnt(0)" : "=v"(got) : "v"(voff), "s"(base) : "memory");
    if (__all(!active || got == tag)) break;
    __builtin_amdgcn_s_sleep(2);
  }
}

DI unsigned rtime() { unsigned long long t_; asm volatile("s_memrealtime %0\n\ts_waitcnt lgkmcnt(0)" : "=s"(t_)::"memory"); return (unsigned)t_; }
#define STAMP(i) stamps[i] = rtime()
#define NSTAMP 16

__global__ __launch_bounds__(512, 2) void gf_glu_kernel(const char* __restrict__ x, const char* __restrict__ wr,
                                                        const char* __restrict__ wi, const char* __restrict__ wg,
                                                        const char* __restrict__ wu, const char* __restrict__ wd,
                                                        char* __restrict__ out, char* __restrict__ ws) {
  extern __shared__ __attribute__((aligned(16))) char smem[];
  unsigned stamps[NSTAMP];
  for (int i = 0; i < NSTAMP; ++i) stamps[i] = 0;
  STAMP(0);
  const int tid = threadIdx.x;
  const int w = __builtin_amdgcn_readfirstlane(tid >> 6);
  const int l = tid & 63, r = l & 15, q = l >> 4;
  const int b = blockIdx.x, xcd = b & 7, bi = b >> 3;
  const unsigned L0 = (unsigned)(uintptr_t)(LAS char*)smem;

  unsigned code_touch;
  {
    unsigned long long pc;
    asm volatile("s_getpc_b64 %0" : "=s"(pc));
    const char* pcb = (const char*)(pc & ~127ull);
    unsigned off = tid * 64;
    off = off < CODE_BYTES ? off : 0;
    asm volatile("global_load_dword %0, %1, %2" : "=v"(code_touch) : "v"(off), "s"(pcb) : "memory");
  }
  unsigned epoch;
  asm volatile("s_load_dword %0, %1, 0x0" : "=s"(epoch) : "s"(ws) : "memory");
#define GET_TAG() asm volatile("s_waitcnt lgkmcnt(0)" : "+s"(epoch)); const unsigned tag = epoch * 0x9E3779B1u + 0x7F4A7C15u

  if (bi < 28) {
    const int pn = 4 * (xcd & 3) + (bi & 3), tt = 7 * (xcd >> 2) + (bi >> 2);
    const int rot = ((bi >> 2) + 2 * (bi & 3)) % 7;
    const int m = w & 3, nh = w >> 2;
    const int rt = 4 * xcd + (bi & 3), ct = bi >> 2;
    auto rk = [&](int j) { int k = j + rot; return k >= 7 ? k - 7 : k; };

    unsigned xoff[2], woff[2], doff[2];
#pragma unroll
    for (int i = 0; i < 2; ++i) {
      int R = 16 * w + 8 * i + (l >> 3);
      int mm = R >> 5, n1 = R & 31;
      int n2 = (pn & 1) + 8 * (pn >> 1) + 2 * mm;
      xoff[i] = (unsigned)((64 * n1 + n2) * 896 + (((l & 7) ^ ((R >> 1) & 7)) << 4));
      int kgd = R >> 5, rr = R & 31;
      doff[i] = (unsigned)((128 * (kgd >> 1) + 32 * (kgd & 1) + rr) * 896 + ct * 128 + (((l & 7) ^ (((R >> 1) & 3) << 1)) << 4));
    }
    unsigned long long wlane[2];
#pragma unroll
    for (int i = 0; i < 2; ++i) {
      int row = 8 * w + 4 * i + q;
      int f = (row & 3) | (((row >> 3) & 1) << 2);
      int ch = r ^ (f << 1);
      woff[i] = (unsigned)(row * 3584 + tt * 256 + ((ch & 7) << 4));
      wlane[i] = (unsigned long long)((ch >> 3) ? wu : wg) + woff[i];
    }
    const unsigned ldw = L0 + L_RING + w * 2048;
    auto issue = [&](auto TI) {
      constexpr int T = decltype(TI)::v;
      constexpr int slot = T % 7, kind = T / 7;
      const int kt = rk(slot);
      if constexpr (kind == 0) {
        const char* bs = x + kt * 128;
        dma16(bs, xoff[0], ldw + slot * 16384);
        dma16(bs, xoff[1], ldw + slot * 16384 + 1024);
      } else if constexpr (kind <= 2) {
        const unsigned long long add = (unsigned long long)(kt * (64 * 3584) + (kind - 1) * 128);
        unsigned long long a0 = wlane[0] + add, a1 = wlane[1] + add;
        dma16v(a0, ldw + slot * 16384);
        dma16v(a1, ldw + slot * 16384 + 1024);
      } else {
        const char* bs = wd + (kt * 256 + (kind - 3) * 64) * 896;
        dma16(bs, doff[0], ldw + slot * 16384);
        dma16(bs, doff[1], ldw + slot * 16384 + 1024);
      }
    };
    sfor<0, 7>([&](auto T) { issue(T); });
    STAMP(1);

#pragma unroll
    for (int it = 0; it < 4; ++it) {
      const int item = w + 8 * it;
      if (item < 28) {
        const int unit = item / 14, ks = item % 14;
        int c = 16 * tt + 8 * unit + (r >> 1);
        float ph = (r & 1) ? 0.25f : 0.0f;
        if (c == 0 && (r & 1)) { c = 224; ph = 0.0f; }
        const int d0 = 64 * rk(ks >> 1) + 32 * (ks & 1) + 8 * q;
        const float cf = (float)c * (1.0f / 448.0f);
        float v[8];
#pragma unroll
        for (int e = 0; e < 8; ++e) v[e] = cosr(__builtin_fmaf((float)(d0 + e), cf, ph));
        u32x4 pk = {pk2(v[0], v[1]), pk2(v[2], v[3]), pk2(v[4], v[5]), pk2(v[6], v[7])};
        *(LAS u32x4*)(uintptr_t)(L0 + L_F + item * 1024 + l * 16) = pk;
      }
    }
    u32x4 p1c[2], p1s[2];
#pragma unroll
    for (int a = 0; a < 2; ++a) {
      float vc[8], vs[8];
#pragma unroll
      for (int e = 0; e < 8; ++e) {
        int n1 = 16 * ((e >> 2) ^ nh) + 4 * q + (e & 3);
        float rev = (float)(((16 * a + r) * n1) & 31) * (1.0f / 32.0f);
        vc[e] = __builtin_amdgcn_cosf(rev); vs[e] = __builtin_amdgcn_sinf(rev);
      }
      p1c[a] = u32x4{pk2(vc[0], vc[1]), pk2(vc[2], vc[3]), pk2(vc[4], vc[5]), pk2(vc[6], vc[7])};
      p1s[a] = u32x4{pk2(vs[0], vs[1]), pk2(vs[2], vs[3]), pk2(vs[4], vs[5]), pk2(vs[6], vs[7])};
    }
    GET_TAG();
    STAMP(2);

    u32x4 xf[14][2];
    f32x4 accD[2] = {{0, 0, 0, 0}, {0, 0, 0, 0}};
    float ssum = 0.f, ssq = 0.f, cF = 0.f;
    {
      unsigned xa[2][2];
#pragma unroll
      for (int i = 0; i < 2; ++i) {
        int R = 32 * m + 16 * (i ^ nh) + r;
#pragma unroll
        for (int kk = 0; kk < 2; ++kk) xa[i][kk] = L0 + L_RING + R * 128 + ((((4 * kk + q) ^ ((R >> 1) & 7))) << 4);
      }
      const unsigned fa = L0 + L_F + nh * 14336 + l * 16;
      sfor<0, 7>([&](auto IT) {
        constexpr int it = decltype(IT)::v;
        WAITV(it == 0 ? 12 : 10);
        BARRIER();
        if constexpr (it >= 1) issue(IC<it + 6>{});
#pragma unroll
        for (int kk = 0; kk < 2; ++kk) {
          const int ks = 2 * it + kk;
          xf[ks][0] = lds128(xa[0][kk] + it * 16384);
          xf[ks][1] = lds128(xa[1][kk] + it * 16384);
          u32x4 ff = lds128(fa + ks * 1024);
          accD[0] = mfma16(xf[ks][0], ff, accD[0]);
          accD[1] = mfma16(xf[ks][1], ff, accD[1]);
          ssum = sum8(xf[ks][0], ssum);
          ssq = sq8(xf[ks][0], ssq);
          cF = sum8(ff, cF);
        }
      });
      ssum = qsum(ssum); ssq = qsum(ssq); cF = qsum(cF);
      float mean = ssum * (1.0f / 448.0f);
      float var = __builtin_fmaf(-mean, mean, ssq * (1.0f / 448.0f));
      var = var > 0.f ? var : 0.f;
      float rstd = __builtin_amdgcn_rsqf(var + 1e-5f);
      if (q == 0) {
        int R = 32 * m + 16 * nh + r;
        *(LAS float*)(uintptr_t)(L0 + L_ST + R * 4) = mean;
        *(LAS float*)(uintptr_t)(L0 + L_ST + 512 + R * 4) = rstd;
      }
    }
    STAMP(3);

    unsigned wa[4];
    {
      const int qq = r >> 2, pp = r & 3;
      const int f = qq | ((q & 1) << 2);
#pragma unroll
      for (int i = 0; i < 4; ++i) {
        int lam = i ^ m;
        int u = 2 * nh + (lam & 1) + 4 * (lam >> 1);
        wa[i] = L0 + L_RING + (8 * q + qq) * 256 + ((u ^ f) << 5) + pp * 8;
      }
    }
    auto rdw = [&](u32x4* dst, int slot, int kk) {
#pragma unroll
      for (int i = 0; i < 4; ++i) dst[i] = ldstr(wa[i] + slot * 16384 + kk * 8192, wa[i] + slot * 16384 + kk * 8192 + 1024);
    };
    f32x4 acc[2][4][2];
#pragma unroll
    for (int a = 0; a < 2; ++a)
#pragma unroll
      for (int i = 0; i < 4; ++i) { acc[a][i][0] = f32x4{0, 0, 0, 0}; acc[a][i][1] = f32x4{0, 0, 0, 0}; }
    float mrow[2], r2row[2];
    const int g1 = 4 * pn + m;
    auto h_epilogue = [&](auto PS) {
      constexpr int ps = decltype(PS)::v;
      f32x4 cs[4];
#pragma unroll
      for (int i = 0; i < 4; ++i) {
        int lam = i ^ m;
        int u = 2 * nh + (lam & 1) + 4 * (lam >> 1);
        cs[i] = lds128f(L0 + L_CS + (ps * 8 + u) * 64 + q * 16);
      }
      const unsigned hoff = (unsigned)(((((g1 * 2 + ps) * 7 + (tt >> 1)) * 4 + 2 * (tt & 1) + nh) * 2) * 1024 + l * 16);
#pragma unroll
      for (int i2 = 0; i2 < 2; ++i2) {
        float hv[8];
#pragma unroll
        for (int i = 0; i < 2; ++i)
#pragma unroll
          for (int e = 0; e < 4; ++e) {
            float a = __builtin_fmaf(-mrow[i2], cs[i][e], acc[ps][i][i2][e]);
            float c = __builtin_fmaf(-mrow[i2], cs[i + 2][e], acc[ps][i + 2][i2][e]);
            hv[4 * i + e] = (a * r2row[i2]) * c;
          }
        u32x4 pk = {pk2(hv[0], hv[1]), pk2(hv[2], hv[3]), pk2(hv[4], hv[5]), pk2(hv[6], hv[7])};
        st128_sc1(ws + WS_H, hoff + ((i2 ^ nh) << 10), pk);
      }
    };
    auto h_flag = [&](int ps) {
      if (l == 0) st32_sc1(ws + WS_FLAGH, (unsigned)(((g1 * 2 + ps) * 4 + 2 * (tt & 1) + nh) * 128 + (tt >> 1) * 4), tag);
    };

    u32x4 fa_[4], fb_[4];
    float csum = 0.f;
    sfor<7, 21>([&](auto IT) {
      constexpr int it = decltype(IT)::v;
      constexpr int ps = (it - 7) / 7, j = (it - 7) % 7;
      WAITV(8);
      BARRIER();
      if constexpr (it == 8) {
        u32x4 v = lds128(L0 + L_EB + tid * 16);
        st128_sc1(ws + WS_E, (unsigned)((((tid >> 4) * 16 + pn) * 256 + 16 * tt + (tid & 15)) * 16), v);
        if (tt == 0 && tid < 32) {
          u32x4 v2 = lds128(L0 + L_EB + 8192 + tid * 16);
          st128_sc1(ws + WS_E, (unsigned)(((tid * 16 + pn) * 256 + 224) * 16), v2);
        }
      }
      if constexpr (it == 13) {
        if (tid == 0) st32_sc1(ws + WS_FLAGE, (unsigned)((tt >> 1) * 128 + pn * 8 + (tt & 1) * 4), tag);
      }
      if constexpr (it == 20) h_flag(0);
      issue(IC<it + 6>{});
      if constexpr (it == 7) {
        rdw(fa_, 0, 0);
        f32x4 mD[2], rD[2];
#pragma unroll
        for (int i = 0; i < 2; ++i) {
          int R0 = 32 * m + 16 * (i ^ nh);
          mrow[i] = *(LAS const float*)(uintptr_t)(L0 + L_ST + (R0 + r) * 4);
          float rs = *(LAS const float*)(uintptr_t)(L0 + L_ST + 512 + (R0 + r) * 4);
          r2row[i] = rs * rs;
          mD[i] = lds128f(L0 + L_ST + (R0 + 4 * q) * 4);
          rD[i] = lds128f(L0 + L_ST + 512 + (R0 + 4 * q) * 4);
        }
        float dv[8];
#pragma unroll
        for (int i = 0; i < 2; ++i)
#pragma unroll
          for (int e = 0; e < 4; ++e) dv[4 * i + e] = rD[i][e] * __builtin_fmaf(-mD[i][e], cF, accD[i][e]);
        u32x4 df = {pk2(dv[0], dv[1]), pk2(dv[2], dv[3]), pk2(dv[4], dv[5]), pk2(dv[6], dv[7])};
        const bool slot0 = (tt == 0) && (nh == 0) && (r < 2);
        const float kc = (slot0 && r == 1) ? 0.f : 1.f;
        const float ko = (r & 1) ? -1.f : (slot0 ? 0.f : 1.f);
#pragma unroll
        for (int a = 0; a < 2; ++a) {
          f32x4 z = {0, 0, 0, 0};
          f32x4 tc = mfma16(p1c[a], df, z), ts = mfma16(p1s[a], df, z);
#pragma unroll
          for (int e = 0; e < 4; ++e) {
            float tse = ts[e], tce = tc[e];
            float oth = __builtin_bit_cast(float, __builtin_amdgcn_update_dpp(0, __builtin_bit_cast(int, tse), 0xB1, 0xf, 0xf, true));
            float ev = __builtin_fmaf(ko, oth, kc * tce);
            int k1 = 16 * a + 4 * q + e;
            *(LAS unsigned short*)(uintptr_t)(L0 + L_EB + k1 * 256 + (16 * nh + r) * 8 + m * 2) = (unsigned short)pk2(ev, 0.f);
            if (slot0) {
              float nv = r ? tce : -oth;
              *(LAS unsigned short*)(uintptr_t)(L0 + L_EB + 8192 + k1 * 16 + (r ^ 1) * 8 + m * 2) = (unsigned short)pk2(nv, 0.f);
            }
          }
        }
      }
      rdw(fb_, j, 1);
#pragma unroll
      for (int i = 0; i < 4; ++i) {
        acc[ps][i][0] = mfma16(fa_[i], xf[2 * j][0], acc[ps][i][0]);
        acc[ps][i][1] = mfma16(fa_[i], xf[2 * j][1], acc[ps][i][1]);
      }
      csum = sum8(fa_[0], csum);
      if constexpr (it < 20) rdw(fa_, (j + 1) % 7, 0);
#pragma unroll
      for (int i = 0; i < 4; ++i) {
        acc[ps][i][0] = mfma16(fb_[i], xf[2 * j + 1][0], acc[ps][i][0]);
        acc[ps][i][1] = mfma16(fb_[i], xf[2 * j + 1][1], acc[ps][i][1]);
      }
      csum = sum8(fb_[0], csum);
      if constexpr (j == 6) {
        float cs = qsum(csum);
        csum = 0.f;
        int u = 2 * nh + (m & 1) + 4 * (m >> 1);
        if (q == 0) *(LAS float*)(uintptr_t)(L0 + L_CS + (ps * 8 + u) * 64 + r * 4) = cs;
      }
      if constexpr (it == 14) h_epilogue(IC<0>{});
      if constexpr (it == 13) STAMP(4);
    });
    STAMP(5);
    BARRIER();
    h_epilogue(IC<1>{});
    issue(IC<27>{});
    STAMP(6);

    const int kg = w >> 1, half = w & 1;
    const int g2 = 2 * rt + half;
    unsigned da[4][2];
    {
      const int qq = r >> 2, pp = r & 3;
#pragma unroll
      for (int t2 = 0; t2 < 2; ++t2) {
        int row = 32 * kg + 16 * (t2 ^ half) + 4 * q + qq;
#pragma unroll
        for (int dt = 0; dt < 4; ++dt) da[dt][t2] = L0 + L_RING + row * 128 + ((dt ^ ((row >> 1) & 3)) << 5) + pp * 8;
      }
    }
    f32x4 oacc[4][2];
#pragma unroll
    for (int dt = 0; dt < 4; ++dt) { oacc[dt][0] = f32x4{0, 0, 0, 0}; oacc[dt][1] = f32x4{0, 0, 0, 0}; }
    u32x4 hf[7][2];
    auto load_h = [&](int ps) {
      sfor<0, 7>([&](auto J) {
        constexpr int j = decltype(J)::v;
        const char* hb = ws + WS_H + (size_t)((((g2 * 2 + ps) * 7 + rk(j)) * 4 + kg) * 2048);
        const unsigned vo = l * 16;
        LD128_SC1(hf[j][0], hb, vo, 0);
        LD128_SC1(hf[j][1], hb, vo, 1024);
      });
    };
    const unsigned hfl = (unsigned)((g2 * 2 * 4 + kg) * 128 + (l & 7) * 4);
    {
      unsigned got;
      asm volatile("global_load_dword %0, %1, %2 sc1\n\ts_waitcnt vmcnt(0)" : "=v"(got) : "v"(hfl), "s"(ws + WS_FLAGH) : "memory");
      h_flag(1);
      if (!__all(l >= 7 || got == tag)) poll_flags(ws + WS_FLAGH, hfl, l < 7, tag);
    }
    STAMP(7);
    load_h(0);
    sfor<21, 35>([&](auto IT) {
      constexpr int it = decltype(IT)::v;
      constexpr int ps = (it - 21) / 7, j = (it - 21) % 7;
      if constexpr (it == 28) {
        STAMP(8);
        poll_flags(ws + WS_FLAGH, hfl + 512, l < 7, tag);
        STAMP(9);
        load_h(1);
      }
      if constexpr (ps == 0) {
        WAITV(2 * (6 - j) + (j >= 2 ? 2 * (j - 1) : 0));
      } else {
        WAITV(j == 6 ? 0 : 2 * (6 - j) + (j >= 1 ? 2 : 0));
      }
      PIN(hf[j][0]); PIN(hf[j][1]);
      BARRIER();
      if constexpr (it >= 22 && it <= 28) issue(IC<it + 6>{});
#pragma unroll
      for (int dt = 0; dt < 4; ++dt) {
        u32x4 af = ldstr(da[dt][0] + j * 16384, da[dt][1] + j * 16384);
        oacc[dt][0] = mfma16(af, hf[j][0], oacc[dt][0]);
        oacc[dt][1] = mfma16(af, hf[j][1], oacc[dt][1]);
      }
    });
    STAMP(10);

    poll_flags(ws + WS_FLAGZ, (unsigned)(kg * 256 + l * 4), true, tag);
    STAMP(11);
    u32x4 zr[8];
    u32x4 xres;
    {
      const char* zb = ws + WS_Z + (size_t)g2 * 32768 + kg * 256;
      sfor<0, 8>([&](auto I) {
        constexpr int i = decltype(I)::v;
        const unsigned vo = (4 * i + q) * 1024 + r * 16;
        LD128_SC1(zr[i], zb, vo, 0);
      });
      int row = tid >> 3;
      int gg = 2 * rt + (row >> 5);
      int n2 = ((gg >> 2) & 1) + 8 * (gg >> 3) + 2 * (gg & 3);
      unsigned xo = (unsigned)((64 * (row & 31) + n2) * 896 + ct * 128 + (tid & 7) * 16);
      asm volatile("global_load_dwordx4 %0, %1, %2" : "=v"(xres) : "v"(xo), "s"(x) : "memory");
    }
    u32x4 p2c[2], p2s[2];
#pragma unroll
    for (int i2 = 0; i2 < 2; ++i2) {
      float vc[8], vs[8];
#pragma unroll
      for (int e = 0; e < 8; ++e) {
        float rev = (float)(((16 * i2 + r) * (8 * q + e)) & 31) * (1.0f / 32.0f);
        vc[e] = __builtin_amdgcn_cosf(rev); vs[e] = __builtin_amdgcn_sinf(rev);
      }
      p2c[i2] = u32x4{pk2(vc[0], vc[1]), pk2(vc[2], vc[3]), pk2(vc[4], vc[5]), pk2(vc[6], vc[7])};
      p2s[i2] = u32x4{pk2(vs[0], vs[1]), pk2(vs[2], vs[3]), pk2(vs[4], vs[5]), pk2(vs[6], vs[7])};
    }
    BARRIER();
    const unsigned zl = L0 + L_RING + 65536 + w * 8192;
    WAITV(1);
#pragma unroll
    for (int i = 0; i < 8; ++i) {
      PIN(zr[i]);
      int row = 4 * i + q;
      int f = (row & 3) | (((row >> 3) & 1) << 2);
      *(LAS u32x4*)(uintptr_t)(zl + row * 256 + ((r ^ (f << 1)) << 4)) = zr[i];
    }
    {
      const int qq = r >> 2, pp = r & 3;
      const int f = qq | ((q & 1) << 2);
      const unsigned zbase = zl + (8 * q + qq) * 256 + pp * 8;
#pragma unroll
      for (int bk = 0; bk < 4; ++bk) {
        unsigned yy[2][4];
#pragma unroll
        for (int aa = 0; aa < 2; ++aa) {
          const int a = 2 * bk + aa;
          const unsigned ad = zbase + ((a ^ f) << 5);
          u32x4 zf = ldstr(ad, ad + 1024);
#pragma unroll
          for (int i2 = 0; i2 < 2; ++i2) {
            f32x4 z = {0, 0, 0, 0};
            f32x4 tc = mfma16(zf, p2c[i2], z), ts = mfma16(zf, p2s[i2], z);
            yy[i2][2 * aa] = pk2(tc[0] - ts[1], tc[1] + ts[0]);
            yy[i2][2 * aa + 1] = pk2(tc[2] - ts[3], tc[3] + ts[2]);
          }
        }
        u32x4 y0 = {yy[0][0], yy[0][1], yy[0][2], yy[0][3]}, y1 = {yy[1][0], yy[1][1], yy[1][2], yy[1][3]};
#pragma unroll
        for (int dt = 0; dt < 4; ++dt) {
          const float df = (float)(64 * ct + 16 * dt + r) * (1.0f / 448.0f);
          unsigned gg[4];
#pragma unroll
          for (int i = 0; i < 4; ++i) {
            int c = 64 * kg + 16 * bk + 8 * (i >> 1) + 2 * q + (i & 1);
            float rev = __builtin_amdgcn_fractf((float)c * df);
            gg[i] = pk2(__builtin_amdgcn_cosf(rev), -__builtin_amdgcn_sinf(rev));
          }
          u32x4 gf = {gg[0], gg[1], gg[2], gg[3]};
          oacc[dt][0] = mfma16(gf, y0, oacc[dt][0]);
          oacc[dt][1] = mfma16(gf, y1, oacc[dt][1]);
        }
      }
    }
    STAMP(12);
#pragma unroll
    for (int i2 = 0; i2 < 2; ++i2) {
      int row = 32 * half + 16 * i2 + r;
#pragma unroll
      for (int dt = 0; dt < 4; ++dt)
        *(LAS f32x4*)(uintptr_t)(L0 + L_RING + kg * 16384 + row * 256 + (((4 * dt + q) ^ (row & 7)) << 4)) = oacc[dt][i2];
    }
    BARRIER();
    {
      int row = tid >> 3, cp = tid & 7;
      f32x4 s0 = {0, 0, 0, 0}, s1 = {0, 0, 0, 0};
#pragma unroll
      for (int k = 0; k < 4; ++k) {
        s0 += lds128f(L0 + L_RING + k * 16384 + row * 256 + (((2 * cp) ^ (row & 7)) << 4));
        s1 += lds128f(L0 + L_RING + k * 16384 + row * 256 + (((2 * cp + 1) ^ (row & 7)) << 4));
      }
      WAITV(0);
      PIN(xres);
      unsigned x0 = xres.x, x1 = xres.y, x2 = xres.z, x3 = xres.w;
      auto lo = [](unsigned u) { return __builtin_bit_cast(float, u << 16); };
      auto hi = [](unsigned u) { return __builtin_bit_cast(float, u & 0xffff0000u); };
      u32x4 o = {pk2(s0[0] + lo(x0), s0[1] + hi(x0)), pk2(s0[2] + lo(x1), s0[3] + hi(x1)),
                 pk2(s1[0] + lo(x2), s1[1] + hi(x2)), pk2(s1[2] + lo(x3), s1[3] + hi(x3))};
      int gg = 2 * rt + (row >> 5);
      int n2 = ((gg >> 2) & 1) + 8 * (gg >> 3) + 2 * (gg & 3);
      st128_sc1(out, (unsigned)((64 * (row & 31) + n2) * 896 + ct * 128 + cp * 16), o);
    }
    if (b == 0 && tid == 0) st32_sc1(ws, WS_EPOCH, tag);
    STAMP(13);
  } else {
    const int k1 = (bi - 28) * 8 + xcd;
    const int c31 = l & 31, h = l >> 5;
    const int c = 32 * w + c31;
    const bool cok = c < 225;
    float wpr[16], wpi[16], wmr[16], wmi[16];
    {
      const unsigned short* pr = (const unsigned short*)wr;
      const unsigned short* pi = (const unsigned short*)wi;
      const int cc = cok ? c : 224;
      const float sc = cok ? ((c == 0 || c == 224) ? 1.0f : 2.0f) * (1.0f / (2048.0f * 448.0f)) : 0.f;
#pragma unroll
      for (int i = 0; i < 16; ++i) {
        int k2 = (i & 3) + 8 * (i >> 2) + 4 * h;
        int i0 = (k1 + 32 * k2) * 225 + cc, i1 = i0 + 1024 * 225;
        float a0 = __builtin_bit_cast(float, (unsigned)pr[i0] << 16), b0 = __builtin_bit_cast(float, (unsigned)pi[i0] << 16);
        float a1 = __builtin_bit_cast(float, (unsigned)pr[i1] << 16), b1 = __builtin_bit_cast(float, (unsigned)pi[i1] << 16);
        wpr[i] = (a0 + a1) * sc; wpi[i] = (b0 + b1) * sc;
        wmr[i] = (a0 - a1) * sc; wmi[i] = (b0 - b1) * sc;
      }
    }
    {
      const int s = w, par = s >> 2;
      float vc[4], vs[4];
#pragma unroll
      for (int mm = 0; mm < 4; ++mm) {
        int n2 = par + 8 * (2 * (s & 3) + h) + 2 * mm;
        float rev = (float)((n2 * (k1 + 32 * c31)) & 2047) * (1.0f / 2048.0f);
        vc[mm] = __builtin_amdgcn_cosf(rev); vs[mm] = __builtin_amdgcn_sinf(rev);
      }
      u32x4 fre = {pk2(vc[0], vc[1]), pk2(vc[2], vc[3]), pk2(vs[0], vs[1]), pk2(vs[2], vs[3])};
      u32x4 fim = {pk2(-vs[0], -vs[1]), pk2(-vs[2], -vs[3]), pk2(vc[0], vc[1]), pk2(vc[2], vc[3])};
      *(LAS u32x4*)(uintptr_t)(L0 + (2 * s) * 1024 + l * 16) = fre;
      *(LAS u32x4*)(uintptr_t)(L0 + (2 * s + 1) * 1024 + l * 16) = fim;
    }
    if (w < 4) {
      const int par = w >> 1, s2 = w & 1;
      float vc[8], vs[8];
#pragma unroll
      for (int e = 0; e < 8; ++e) {
        int k2 = 16 * s2 + 8 * (e >> 2) + 4 * h + (e & 3);
        float rev = (float)(((par + 2 * c31) * (k1 + 32 * k2)) & 2047) * (1.0f / 2048.0f);
        vc[e] = __builtin_amdgcn_cosf(rev); vs[e] = __builtin_amdgcn_sinf(rev);
      }
      unsigned a = L0 + 16384 + w * 3072 + l * 16;
      *(LAS u32x4*)(uintptr_t)(a) = u32x4{pk2(vc[0], vc[1]), pk2(vc[2], vc[3]), pk2(vc[4], vc[5]), pk2(vc[6], vc[7])};
      *(LAS u32x4*)(uintptr_t)(a + 1024) = u32x4{pk2(vs[0], vs[1]), pk2(vs[2], vs[3]), pk2(vs[4], vs[5]), pk2(vs[6], vs[7])};
      *(LAS u32x4*)(uintptr_t)(a + 2048) = u32x4{pk2(-vs[0], -vs[1]), pk2(-vs[2], -vs[3]), pk2(-vs[4], -vs[5]), pk2(-vs[6], -vs[7])};
    }
#pragma unroll
    for (int i = 0; i < 16; ++i) { PIN(wpr[i]); PIN(wpi[i]); PIN(wmr[i]); PIN(wmi[i]); }
    GET_TAG();
    BARRIER();
    STAMP(1);
    poll_flags(ws + WS_FLAGE, (unsigned)((w == 7 ? 0 : w) * 128 + c31 * 4), (l < 32) && (w < 7 || !(l & 1)), tag);
    STAMP(2);
    u32x4 ef[8];
    {
      const char* eb = ws + WS_E + (size_t)k1 * 65536 + w * 512;
      sfor<0, 8>([&](auto S) {
        constexpr int s = decltype(S)::v;
        const unsigned vo = ((s >> 2) + 2 * (2 * (s & 3) + h)) * 4096 + c31 * 16;
        LD128_SC1(ef[s], eb, vo, 0);
      });
    }
    f32x16 ac[4];
#pragma unroll
    for (int t = 0; t < 4; ++t)
#pragma unroll
      for (int i = 0; i < 16; ++i) ac[t][i] = 0.f;
    sfor<0, 8>([&](auto S) {
      constexpr int s = decltype(S)::v;
      u32x4 fre = lds128(L0 + (2 * s) * 1024 + l * 16), fim = lds128(L0 + (2 * s + 1) * 1024 + l * 16);
      WAITV(7 - s);
      PIN(ef[s]);
      u32x4 e = ef[s];
      if (!cok) e = u32x4{0, 0, 0, 0};
      ac[2 * (s >> 2)] = mfma32(fre, e, ac[2 * (s >> 2)]);
      ac[2 * (s >> 2) + 1] = mfma32(fim, e, ac[2 * (s >> 2) + 1]);
    });
    STAMP(3);
    u32x4 pf[2][2][2];
    {
      float sr[16], si[16], dr[16], di[16];
#pragma unroll
      for (int i = 0; i < 16; ++i) {
        float ar = ac[0][i], ai = ac[1][i], br = ac[2][i], bi_ = ac[3][i];
        sr[i] = ar * wpr[i] - ai * wpi[i] + br * wmr[i] - bi_ * wmi[i];
        si[i] = ar * wpi[i] + ai * wpr[i] + br * wmi[i] + bi_ * wmr[i];
        dr[i] = ar * wmr[i] - ai * wmi[i] + br * wpr[i] - bi_ * wpi[i];
        di[i] = ar * wmi[i] + ai * wmr[i] + br * wpi[i] + bi_ * wpr[i];
      }
#pragma unroll
      for (int s2 = 0; s2 < 2; ++s2) {
        const int o = 8 * s2;
        pf[0][0][s2] = u32x4{pk2(sr[o], sr[o + 1]), pk2(sr[o + 2], sr[o + 3]), pk2(sr[o + 4], sr[o + 5]), pk2(sr[o + 6], sr[o + 7])};
        pf[0][1][s2] = u32x4{pk2(si[o], si[o + 1]), pk2(si[o + 2], si[o + 3]), pk2(si[o + 4], si[o + 5]), pk2(si[o + 6], si[o + 7])};
        pf[1][0][s2] = u32x4{pk2(dr[o], dr[o + 1]), pk2(dr[o + 2], dr[o + 3]), pk2(dr[o + 4], dr[o + 5]), pk2(dr[o + 6], dr[o + 7])};
        pf[1][1][s2] = u32x4{pk2(di[o], di[o + 1]), pk2(di[o + 2], di[o + 3]), pk2(di[o + 4], di[o + 5]), pk2(di[o + 6], di[o + 7])};
      }
    }
    const unsigned zl = L0 + 32768 + w * 8192;
#pragma unroll
    for (int par = 0; par < 2; ++par) {
      f32x16 zre, zim;
#pragma unroll
      for (int i = 0; i < 16; ++i) { zre[i] = 0.f; zim[i] = 0.f; }
#pragma unroll
      for (int s2 = 0; s2 < 2; ++s2) {
        unsigned a = L0 + 16384 + (par * 2 + s2) * 3072 + l * 16;
        u32x4 mc = lds128(a), ms = lds128(a + 1024), mn = lds128(a + 2048);
        zre = mfma32(mc, pf[par][0][s2], zre);
        zim = mfma32(ms, pf[par][0][s2], zim);
        zre = mfma32(mn, pf[par][1][s2], zre);
        zim = mfma32(mc, pf[par][1][s2], zim);
      }
#pragma unroll
      for (int i = 0; i < 16; ++i) {
        int rr = (i & 3) + 8 * (i >> 2) + 4 * h;
        int g = 4 * (par + 2 * (rr >> 2)) + (rr & 3);
        *(LAS unsigned*)(uintptr_t)(zl + g * 128 + c31 * 4) = pk2(zre[i], zim[i]);
      }
    }
    STAMP(4);
    {
      char* zb = ws + WS_Z + (size_t)k1 * 1024 + w * 128;
#pragma unroll
      for (int i = 0; i < 8; ++i) {
        u32x4 v = lds128(zl + i * 1024 + l * 16);
        st128_sc1(zb, (unsigned)((8 * i + (l >> 3)) * 32768 + (l & 7) * 16), v);
      }
    }
    WAITV(0);
    STAMP(5);
    if (l == 0) st32_sc1(ws + WS_FLAGZ, (unsigned)((w >> 1) * 256 + k1 * 8 + (w & 1) * 4), tag);
  }
  asm volatile("s_waitcnt vmcnt(0)" : "+v"(code_touch)::"memory");
  if (b == 0 && w == 0) { unsigned tgt = 3200u + 4u * (stamps[PROBE] - stamps[0]); while (rtime() - stamps[0] < tgt) __builtin_amdgcn_s_sleep(1); }
  if (l == 0 && stamps[1] == 12345u) {
    unsigned* dbg = (unsigned*)(ws + WS_DBG) + (b * 8 + w) * NSTAMP;
    for (int i = 0; i < NSTAMP; ++i) dbg[i] = stamps[i];
  }
}

extern "C" void kernel_launch(void* const* d_in, const int* in_sizes, int n_in, void* d_out, int out_size, void* d_ws,
                              size_t ws_size, hipStream_t stream) {
  (void)in_sizes; (void)n_in; (void)out_size; (void)ws_size;
  (void)hipFuncSetAttribute((const void*)gf_glu_kernel, hipFuncAttributeMaxDynamicSharedMemorySize, L_TOTAL);
  gf_glu_kernel<<<256, 512, L_TOTAL, stream>>>((const char*)d_in[0], (const char*)d_in[1], (const char*)d_in[2],
                                               (const char*)d_in[4], (const char*)d_in[5], (const char*)d_in[6],
                                               (char*)d_out, (char*)d_ws);
}
```

```cpp
#define KDEBUG 1
#define PROBE 0
#include <hip/hip_runtime.h>
#include <hip/hip_bf16.h>
#include <stdint.h>

typedef __bf16 bf16x8 __attribute__((ext_vector_type(8)));
typedef __bf16 bf16x2 __attribute__((ext_vector_type(2)));
typedef short s16x4 __attribute__((ext_vector_type(4)));
typedef float f32x2 __attribute__((ext_vector_type(2)));
typedef float f32x4 __attribute__((ext_vector_type(4)));
typedef float f32x16 __attribute__((ext_vector_type(16)));
typedef unsigned u32x4 __attribute__((ext_vector_type(4)));
typedef unsigned u32x2 __attribute__((ext_vector_type(2)));
#define LAS __attribute__((address_space(3)))
#define DI __device__ __forceinline__

constexpr unsigned WS_EPOCH = 0;
constexpr unsigned WS_FLAGE = 4096;
constexpr unsigned WS_FLAGZ = 8192;
constexpr unsigned WS_FLAGH = 65536;
constexpr unsigned WS_E = 1u << 20;
constexpr unsigned WS_Z = 3u << 20;
constexpr unsigned WS_H = 5u << 20;
constexpr unsigned WS_DBG = 16u << 20;

constexpr unsigned L_RING = 0;
constexpr unsigned L_F = 114688;
constexpr unsigned L_EB = L_F + 28672;
constexpr unsigned L_ST = L_EB + 8192 + 512;
constexpr unsigned L_CS = L_ST + 1024;
constexpr unsigned L_TOTAL = 163840;

constexpr unsigned SPIN_CAP = 1u << 17;
constexpr unsigned CODE_BYTES = 16384;

template <int N> struct IC { static constexpr int v = N; };
template <int B, int E, class Fn> DI void sfor(Fn&& f) {
  if constexpr (B < E) { f(IC<B>{}); sfor<B + 1, E>(f); }
}

DI unsigned pk2(float a, float b) { f32x2 t = {a, b}; bf16x2 p = __builtin_convertvector(t, bf16x2); return __builtin_bit_cast(unsigned, p); }
DI bf16x8 asbf(u32x4 v) { return __builtin_bit_cast(bf16x8, v); }
DI f32x4 mfma16(u32x4 a, u32x4 b, f32x4 c) { return __builtin_amdgcn_mfma_f32_16x16x32_bf16(asbf(a), asbf(b), c, 0, 0, 0); }
DI f32x16 mfma32(u32x4 a, u32x4 b, f32x16 c) { return __builtin_amdgcn_mfma_f32_32x32x16_bf16(asbf(a), asbf(b), c, 0, 0, 0); }
DI float dot2(unsigned a, unsigned b, float c) {
  return __builtin_amdgcn_fdot2_f32_bf16(__builtin_bit_cast(bf16x2, a), __builtin_bit_cast(bf16x2, b), c, false);
}
DI float sum8(u32x4 v, float c) { unsigned a = v.x, b = v.y, d = v.z, e = v.w; const unsigned o = 0x3f803f80u;
  return dot2(e, o, dot2(d, o, dot2(b, o, dot2(a, o, c)))); }
DI float sq8(u32x4 v, float c) { unsigned a = v.x, b = v.y, d = v.z, e = v.w;
  return dot2(e, e, dot2(d, d, dot2(b, b, dot2(a, a, c)))); }
DI float cosr(float rev) { return __builtin_amdgcn_cosf(__builtin_amdgcn_fractf(rev)); }
DI float sinr(float rev) { return __builtin_amdgcn_sinf(__builtin_amdgcn_fractf(rev)); }
DI float qsum(float v) {
  unsigned u = __builtin_bit_cast(unsigned, v);
  auto a = __builtin_amdgcn_permlane16_swap(u, u, false, false);
  unsigned a0 = a[0], a1 = a[1];
  float s = __builtin_bit_cast(float, a0) + __builtin_bit_cast(float, a1);
  u = __builtin_bit_cast(unsigned, s);
  auto b = __builtin_amdgcn_permlane32_swap(u, u, false, false);
  unsigned b0 = b[0], b1 = b[1];
  return __builtin_bit_cast(float, b0) + __builtin_bit_cast(float, b1);
}
DI u32x4 lds128(unsigned a) { return *(LAS const u32x4*)(uintptr_t)a; }
DI f32x4 lds128f(unsigned a) { return *(LAS const f32x4*)(uintptr_t)a; }
DI u32x4 ldstr(unsigned a0, unsigned a1) {
  s16x4 lo = __builtin_amdgcn_ds_read_tr16_b64_v4i16((LAS s16x4*)(uintptr_t)a0);
  s16x4 hi = __builtin_amdgcn_ds_read_tr16_b64_v4i16((LAS s16x4*)(uintptr_t)a1);
  return __builtin_bit_cast(u32x4, __builtin_shufflevector(lo, hi, 0, 1, 2, 3, 4, 5, 6, 7));
}
DI void dma16(const char* base, unsigned voff, unsigned ldst) {
  asm volatile("s_mov_b32 m0, %2\n\ts_nop 0\n\tglobal_load_lds_dwordx4 %0, %1" ::"v"(voff), "s"(base), "s"(ldst) : "memory");
}
DI void st128_sc1(char* base, unsigned voff, u32x4 v) {
  asm volatile("global_store_dwordx4 %0, %1, %2 sc1\n\ts_nop 1" ::"v"(voff), "v"(v), "s"(base) : "memory");
}
DI void st32_sc1(char* base, unsigned voff, unsigned v) {
  asm volatile("global_store_dword %0, %1, %2 sc1" ::"v"(voff), "v"(v), "s"(base) : "memory");
}
template <int IMM> DI void ld128_sc1(u32x4& dst, const char* base, unsigned voff) {
  asm volatile("global_load_dwordx4 %0, %1, %2 offset:%3 sc1" : "=v"(dst) : "v"(voff), "s"(base), "i"(IMM) : "memory");
}
#define LD128_SC1(dst, base, voff, imm) ld128_sc1<imm>(dst, base, voff)
DI void dma16v(unsigned long long addr, unsigned ldst) {
  asm volatile("s_mov_b32 m0, %1\n\ts_nop 0\n\tglobal_load_lds_dwordx4 %0, off" ::"v"(addr), "s"(ldst) : "memory");
}
#define WAITV(n) asm volatile("s_waitcnt vmcnt(%0)" ::"i"(n) : "memory")
#define BARRIER() asm volatile("s_waitcnt lgkmcnt(0)\n\ts_barrier" ::: "memory")
template <class T> DI void PIN(T& x) { asm volatile("" : "+v"(x)); }

DI void poll_flags(const char* base, unsigned voff, bool active, unsigned tag) {
  for (unsigned spins = 0; spins < SPIN_CAP; ++spins) {
    unsigned got;
    asm volatile("global_load_dword %0, %1, %2 sc1\n\ts_waitcnt vmcnt(0)" : "=v"(got) : "v"(voff), "s"(base) : "memory");
    if (__all(!active || got == tag)) break;
    __builtin_amdgcn_s_sleep(2);
  }
}

DI unsigned rtime() { unsigned long long t_; asm volatile("s_memrealtime %0\n\ts_waitcnt lgkmcnt(0)" : "=s"(t_)::"memory"); return (unsigned)t_; }
#define STAMP(i) stamps[i] = rtime()
#define NSTAMP 16

__global__ __launch_bounds__(512, 2) void gf_glu_kernel(const char* __restrict__ x, const char* __restrict__ wr,
                                                        const char* __restrict__ wi, const char* __restrict__ wg,
                                                        const char* __restrict__ wu, const char* __restrict__ wd,
                                                        char* __restrict__ out, char* __restrict__ ws) {
  extern __shared__ __attribute__((aligned(16))) char smem[];
  unsigned stamps[NSTAMP];
  for (int i = 0; i < NSTAMP; ++i) stamps[i] = 0;
  STAMP(0);
  const int tid = threadIdx.x;
  const int w = __builtin_amdgcn_readfirstlane(tid >> 6);
  const int l = tid & 63, r = l & 15, q = l >> 4;
  const int b = blockIdx.x, xcd = b & 7, bi = b >> 3;
  const unsigned L0 = (unsigned)(uintptr_t)(LAS char*)smem;

  unsigned code_touch;
  {
    unsigned long long pc;
    asm volatile("s_getpc_b64 %0" : "=s"(pc));
    const char* pcb = (const char*)(pc & ~127ull);
    unsigned off = tid * 64;
    off = off < CODE_BYTES ? off : 0;
    asm volatile("global_load_dword %0, %1, %2" : "=v"(code_touch) : "v"(off), "s"(pcb) : "memory");
  }
  unsigned epoch;
  asm volatile("s_load_dword %0, %1, 0x0" : "=s"(epoch) : "s"(ws) : "memory");
#define GET_TAG() asm volatile("s_waitcnt lgkmcnt(0)" : "+s"(epoch)); const unsigned tag = epoch * 0x9E3779B1u + 0x7F4A7C15u

  if (bi < 28) {
    const int pn = 4 * (xcd & 3) + (bi & 3), tt = 7 * (xcd >> 2) + (bi >> 2);
    const int rot = ((bi >> 2) + 2 * (bi & 3)) % 7;
    const int m = w & 3, nh = w >> 2;
    const int rt = 4 * xcd + (bi & 3), ct = bi >> 2;
    auto rk = [&](int j) { int k = j + rot; return k >= 7 ? k - 7 : k; };

    unsigned xoff[2], woff[2], doff[2];
#pragma unroll
    for (int i = 0; i < 2; ++i) {
      int R = 16 * w + 8 * i + (l >> 3);
      int mm = R >> 5, n1 = R & 31;
      int n2 = (pn & 1) + 8 * (pn >> 1) + 2 * mm;
      xoff[i] = (unsigned)((64 * n1 + n2) * 896 + (((l & 7) ^ ((R >> 1) & 7)) << 4));
      int kgd = R >> 5, rr = R & 31;
      doff[i] = (unsigned)((128 * (kgd >> 1) + 32 * (kgd & 1) + rr) * 896 + ct * 128 + (((l & 7) ^ (((R >> 1) & 3) << 1)) << 4));
    }
    unsigned long long wlane[2];
#pragma unroll
    for (int i = 0; i < 2; ++i) {
      int row = 8 * w + 4 * i + q;
      int f = (row & 3) | (((row >> 3) & 1) << 2);
      int ch = r ^ (f << 1);
      woff[i] = (unsigned)(row * 3584 + tt * 256 + ((ch & 7) << 4));
      wlane[i] = (unsigned long long)((ch >> 3) ? wu : wg) + woff[i];
    }
    const unsigned ldw = L0 + L_RING + w * 2048;
    auto issue = [&](auto TI) {
      constexpr int T = decltype(TI)::v;
      constexpr int slot = T % 7, kind = T / 7;
      const int kt = rk(slot);
      if constexpr (kind == 0) {
        const char* bs = x + kt * 128;
        dma16(bs, xoff[0], ldw + slot * 16384);
        dma16(bs, xoff[1], ldw + slot * 16384 + 1024);
      } else if constexpr (kind <= 2) {
        const unsigned long long add = (unsigned long long)(kt * (64 * 3584) + (kind - 1) * 128);
        unsigned long long a0 = wlane[0] + add, a1 = wlane[1] + add;
        dma16v(a0, ldw + slot * 16384);
        dma16v(a1, ldw + slot * 16384 + 1024);
      } else {
        const char* bs = wd + (kt * 256 + (kind - 3) * 64) * 896;
        dma16(bs, doff[0], ldw + slot * 16384);
        dma16(bs, doff[1], ldw + slot * 16384 + 1024);
      }
    };
    sfor<0, 7>([&](auto T) { issue(T); });
    STAMP(1);

#pragma unroll
    for (int it = 0; it < 4; ++it) {
      const int item = w + 8 * it;
      if (item < 28) {
        const int unit = item / 14, ks = item % 14;
        int c = 16 * tt + 8 * unit + (r >> 1);
        float ph = (r & 1) ? 0.25f : 0.0f;
        if (c == 0 && (r & 1)) { c = 224; ph = 0.0f; }
        const int d0 = 64 * rk(ks >> 1) + 32 * (ks & 1) + 8 * q;
        const float cf = (float)c * (1.0f / 448.0f);
        float v[8];
#pragma unroll
        for (int e = 0; e < 8; ++e) v[e] = cosr(__builtin_fmaf((float)(d0 + e), cf, ph));
        u32x4 pk = {pk2(v[0], v[1]), pk2(v[2], v[3]), pk2(v[4], v[5]), pk2(v[6], v[7])};
        *(LAS u32x4*)(uintptr_t)(L0 + L_F + item * 1024 + l * 16) = pk;
      }
    }
    u32x4 p1c[2], p1s[2];
#pragma unroll
    for (int a = 0; a < 2; ++a) {
      float vc[8], vs[8];
#pragma unroll
      for (int e = 0; e < 8; ++e) {
        int n1 = 16 * ((e >> 2) ^ nh) + 4 * q + (e & 3);
        float rev = (float)(((16 * a + r) * n1) & 31) * (1.0f / 32.0f);
        vc[e] = __builtin_amdgcn_cosf(rev); vs[e] = __builtin_amdgcn_sinf(rev);
      }
      p1c[a] = u32x4{pk2(vc[0], vc[1]), pk2(vc[2], vc[3]), pk2(vc[4], vc[5]), pk2(vc[6], vc[7])};
      p1s[a] = u32x4{pk2(vs[0], vs[1]), pk2(vs[2], vs[3]), pk2(vs[4], vs[5]), pk2(vs[6], vs[7])};
    }
    GET_TAG();
    STAMP(2);

    u32x4 xf[14][2];
    f32x4 accD[2] = {{0, 0, 0, 0}, {0, 0, 0, 0}};
    float ssum = 0.f, ssq = 0.f, cF = 0.f;
    {
      unsigned xa[2][2];
#pragma unroll
      for (int i = 0; i < 2; ++i) {
        int R = 32 * m + 16 * (i ^ nh) + r;
#pragma unroll
        for (int kk = 0; kk < 2; ++kk) xa[i][kk] = L0 + L_RING + R * 128 + ((((4 * kk + q) ^ ((R >> 1) & 7))) << 4);
      }
      const unsigned fa = L0 + L_F + nh * 14336 + l * 16;
      sfor<0, 7>([&](auto IT) {
        constexpr int it = decltype(IT)::v;
        WAITV(it == 0 ? 12 : 10);
        BARRIER();
        if constexpr (it >= 1) issue(IC<it + 6>{});
#pragma unroll
        for (int kk = 0; kk < 2; ++kk) {
          const int ks = 2 * it + kk;
          xf[ks][0] = lds128(xa[0][kk] + it * 16384);
          xf[ks][1] = lds128(xa[1][kk] + it * 16384);
          u32x4 ff = lds128(fa + ks * 1024);
          accD[0] = mfma16(xf[ks][0], ff, accD[0]);
          accD[1] = mfma16(xf[ks][1], ff, accD[1]);
          ssum = sum8(xf[ks][0], ssum);
          ssq = sq8(xf[ks][0], ssq);
          cF = sum8(ff, cF);
        }
      });
      ssum = qsum(ssum); ssq = qsum(ssq); cF = qsum(cF);
      float mean = ssum * (1.0f / 448.0f);
      float var = __builtin_fmaf(-mean, mean, ssq * (1.0f / 448.0f));
      var = var > 0.f ? var : 0.f;
      float rstd = __builtin_amdgcn_rsqf(var + 1e-5f);
      if (q == 0) {
        int R = 32 * m + 16 * nh + r;
        *(LAS float*)(uintptr_t)(L0 + L_ST + R * 4) = mean;
        *(LAS float*)(uintptr_t)(L0 + L_ST + 512 + R * 4) = rstd;
      }
    }
    STAMP(3);

    unsigned wa[4];
    {
      const int qq = r >> 2, pp = r & 3;
      const int f = qq | ((q & 1) << 2);
#pragma unroll
      for (int i = 0; i < 4; ++i) {
        int lam = i ^ m;
        int u = 2 * nh + (lam & 1) + 4 * (lam >> 1);
        wa[i] = L0 + L_RING + (8 * q + qq) * 256 + ((u ^ f) << 5) + pp * 8;
      }
    }
    auto rdw = [&](u32x4* dst, int slot, int kk) {
#pragma unroll
      for (int i = 0; i < 4; ++i) dst[i] = ldstr(wa[i] + slot * 16384 + kk * 8192, wa[i] + slot * 16384 + kk * 8192 + 1024);
    };
    f32x4 acc[2][4][2];
#pragma unroll
    for (int a = 0; a < 2; ++a)
#pragma unroll
      for (int i = 0; i < 4; ++i) { acc[a][i][0] = f32x4{0, 0, 0, 0}; acc[a][i][1] = f32x4{0, 0, 0, 0}; }
    float mrow[2], r2row[2];
    const int g1 = 4 * pn + m;
    auto h_epilogue = [&](auto PS) {
      constexpr int ps = decltype(PS)::v;
      f32x4 cs[4];
#pragma unroll
      for (int i = 0; i < 4; ++i) {
        int lam = i ^ m;
        int u = 2 * nh + (lam & 1) + 4 * (lam >> 1);
        cs[i] = lds128f(L0 + L_CS + (ps * 8 + u) * 64 + q * 16);
      }
      const unsigned hoff = (unsigned)(((((g1 * 2 + ps) * 7 + (tt >> 1)) * 4 + 2 * (tt & 1) + nh) * 2) * 1024 + l * 16);
#pragma unroll
      for (int i2 = 0; i2 < 2; ++i2) {
        float hv[8];
#pragma unroll
        for (int i = 0; i < 2; ++i)
#pragma unroll
          for (int e = 0; e < 4; ++e) {
            float a = __builtin_fmaf(-mrow[i2], cs[i][e], acc[ps][i][i2][e]);
            float c = __builtin_fmaf(-mrow[i2], cs[i + 2][e], acc[ps][i + 2][i2][e]);
            hv[4 * i + e] = (a * r2row[i2]) * c;
          }
        u32x4 pk = {pk2(hv[0], hv[1]), pk2(hv[2], hv[3]), pk2(hv[4], hv[5]), pk2(hv[6], hv[7])};
        st128_sc1(ws + WS_H, hoff + ((i2 ^ nh) << 10), pk);
      }
    };
    auto h_flag = [&](int ps) {
      if (l == 0) st32_sc1(ws + WS_FLAGH, (unsigned)(((g1 * 2 + ps) * 4 + 2 * (tt & 1) + nh) * 128 + (tt >> 1) * 4), tag);
    };

    u32x4 fa_[4], fb_[4];
    float csum = 0.f;
    sfor<7, 21>([&](auto IT) {
      constexpr int it = decltype(IT)::v;
      constexpr int ps = (it - 7) / 7, j = (it - 7) % 7;
      WAITV(8);
      BARRIER();
      if constexpr (it == 8) {
        u32x4 v = lds128(L0 + L_EB + tid * 16);
        st128_sc1(ws + WS_E, (unsigned)((((tid >> 4) * 16 + pn) * 256 + 16 * tt + (tid & 15)) * 16), v);
        if (tt == 0 && tid < 32) {
          u32x4 v2 = lds128(L0 + L_EB + 8192 + tid * 16);
          st128_sc1(ws + WS_E, (unsigned)(((tid * 16 + pn) * 256 + 224) * 16), v2);
        }
      }
      if constexpr (it == 13) {
        if (tid == 0) st32_sc1(ws + WS_FLAGE, (unsigned)((tt >> 1) * 128 + pn * 8 + (tt & 1) * 4), tag);
      }
      if constexpr (it == 20) h_flag(0);
      issue(IC<it + 6>{});
      if constexpr (it == 7) {
        rdw(fa_, 0, 0);
        f32x4 mD[2], rD[2];
#pragma unroll
        for (int i = 0; i < 2; ++i) {
          int R0 = 32 * m + 16 * (i ^ nh);
          mrow[i] = *(LAS const float*)(uintptr_t)(L0 + L_ST + (R0 + r) * 4);
          float rs = *(LAS const float*)(uintptr_t)(L0 + L_ST + 512 + (R0 + r) * 4);
          r2row[i] = rs * rs;
          mD[i] = lds128f(L0 + L_ST + (R0 + 4 * q) * 4);
          rD[i] = lds128f(L0 + L_ST + 512 + (R0 + 4 * q) * 4);
        }
        float dv[8];
#pragma unroll
        for (int i = 0; i < 2; ++i)
#pragma unroll
          for (int e = 0; e < 4; ++e) dv[4 * i + e] = rD[i][e] * __builtin_fmaf(-mD[i][e], cF, accD[i][e]);
        u32x4 df = {pk2(dv[0], dv[1]), pk2(dv[2], dv[3]), pk2(dv[4], dv[5]), pk2(dv[6], dv[7])};
        const bool slot0 = (tt == 0) && (nh == 0) && (r < 2);
        const float kc = (slot0 && r == 1) ? 0.f : 1.f;
        const float ko = (r & 1) ? -1.f : (slot0 ? 0.f : 1.f);
#pragma unroll
        for (int a = 0; a < 2; ++a) {
          f32x4 z = {0, 0, 0, 0};
          f32x4 tc = mfma16(p1c[a], df, z), ts = mfma16(p1s[a], df, z);
#pragma unroll
          for (int e = 0; e < 4; ++e) {
            float tse = ts[e], tce = tc[e];
            float oth = __builtin_bit_cast(float, __builtin_amdgcn_update_dpp(0, __builtin_bit_cast(int, tse), 0xB1, 0xf, 0xf, true));
            float ev = __builtin_fmaf(ko, oth, kc * tce);
            int k1 = 16 * a + 4 * q + e;
            *(LAS unsigned short*)(uintptr_t)(L0 + L_EB + k1 * 256 + (16 * nh + r) * 8 + m * 2) = (unsigned short)pk2(ev, 0.f);
            if (slot0) {
              float nv = r ? tce : -oth;
              *(LAS unsigned short*)(uintptr_t)(L0 + L_EB + 8192 + k1 * 16 + (r ^ 1) * 8 + m * 2) = (unsigned short)pk2(nv, 0.f);
            }
          }
        }
      }
      rdw(fb_, j, 1);
#pragma unroll
      for (int i = 0; i < 4; ++i) {
        acc[ps][i][0] = mfma16(fa_[i], xf[2 * j][0], acc[ps][i][0]);
        acc[ps][i][1] = mfma16(fa_[i], xf[2 * j][1], acc[ps][i][1]);
      }
      csum = sum8(fa_[0], csum);
      if constexpr (it < 20) rdw(fa_, (j + 1) % 7, 0);
#pragma unroll
      for (int i = 0; i < 4; ++i) {
        acc[ps][i][0] = mfma16(fb_[i], xf[2 * j + 1][0], acc[ps][i][0]);
        acc[ps][i][1] = mfma16(fb_[i], xf[2 * j + 1][1], acc[ps][i][1]);
      }
      csum = sum8(fb_[0], csum);
      if constexpr (j == 6) {
        float cs = qsum(csum);
        csum = 0.f;
        int u = 2 * nh + (m & 1) + 4 * (m >> 1);
        if (q == 0) *(LAS float*)(uintptr_t)(L0 + L_CS + (ps * 8 + u) * 64 + r * 4) = cs;
      }
      if constexpr (it == 14) h_epilogue(IC<0>{});
      if constexpr (it == 13) STAMP(4);
    });
    STAMP(5);
    BARRIER();
    h_epilogue(IC<1>{});
    issue(IC<27>{});
    STAMP(6);

    const int kg = w >> 1, half = w & 1;
    const int g2 = 2 * rt + half;
    unsigned da[4][2];
    {
      const int qq = r >> 2, pp = r & 3;
#pragma unroll
      for (int t2 = 0; t2 < 2; ++t2) {
        int row = 32 * kg + 16 * (t2 ^ half) + 4 * q + qq;
#pragma unroll
        for (int dt = 0; dt < 4; ++dt) da[dt][t2] = L0 + L_RING + row * 128 + ((dt ^ ((row >> 1) & 3)) << 5) + pp * 8;
      }
    }
    f32x4 oacc[4][2];
#pragma unroll
    for (int dt = 0; dt < 4; ++dt) { oacc[dt][0] = f32x4{0, 0, 0, 0}; oacc[dt][1] = f32x4{0, 0, 0, 0}; }
    u32x4 hf[7][2];
    auto load_h = [&](int ps) {
      sfor<0, 7>([&](auto J) {
        constexpr int j = decltype(J)::v;
        const char* hb = ws + WS_H + (size_t)((((g2 * 2 + ps) * 7 + rk(j)) * 4 + kg) * 2048);
        const unsigned vo = l * 16;
        LD128_SC1(hf[j][0], hb, vo, 0);
        LD128_SC1(hf[j][1], hb, vo, 1024);
      });
    };
    const unsigned hfl = (unsigned)((g2 * 2 * 4 + kg) * 128 + (l & 7) * 4);
    {
      unsigned got;
      asm volatile("global_load_dword %0, %1, %2 sc1\n\ts_waitcnt vmcnt(0)" : "=v"(got) : "v"(hfl), "s"(ws + WS_FLAGH) : "memory");
      h_flag(1);
      if (!__all(l >= 7 || got == tag)) poll_flags(ws + WS_FLAGH, hfl, l < 7, tag);
    }
    STAMP(7);
    load_h(0);
    sfor<21, 35>([&](auto IT) {
      constexpr int it = decltype(IT)::v;
      constexpr int ps = (it - 21) / 7, j = (it - 21) % 7;
      if constexpr (it == 28) {
        STAMP(8);
        poll_flags(ws + WS_FLAGH, hfl + 512, l < 7, tag);
        STAMP(9);
        load_h(1);
      }
      if constexpr (ps == 0) {
        WAITV(2 * (6 - j) + (j >= 2 ? 2 * (j - 1) : 0));
      } else {
        WAITV(j == 6 ? 0 : 2 * (6 - j) + (j >= 1 ? 2 : 0));
      }
      PIN(hf[j][0]); PIN(hf[j][1]);
      BARRIER();
      if constexpr (it >= 22 && it <= 28) issue(IC<it + 6>{});
#pragma unroll
      for (int dt = 0; dt < 4; ++dt) {
        u32x4 af = ldstr(da[dt][0] + j * 16384, da[dt][1] + j * 16384);
        oacc[dt][0] = mfma16(af, hf[j][0], oacc[dt][0]);
        oacc[dt][1] = mfma16(af, hf[j][1], oacc[dt][1]);
      }
    });
    STAMP(10);

    poll_flags(ws + WS_FLAGZ, (unsigned)(kg * 256 + l * 4), true, tag);
    STAMP(11);
    u32x4 zr[8];
    u32x4 xres;
    {
      const char* zb = ws + WS_Z + (size_t)g2 * 32768 + kg * 256;
      sfor<0, 8>([&](auto I) {
        constexpr int i = decltype(I)::v;
        const unsigned vo = (4 * i + q) * 1024 + r * 16;
        LD128_SC1(zr[i], zb, vo, 0);
      });
      int row = tid >> 3;
      int gg = 2 * rt + (row >> 5);
      int n2 = ((gg >> 2) & 1) + 8 * (gg >> 3) + 2 * (gg & 3);
      unsigned xo = (unsigned)((64 * (row & 31) + n2) * 896 + ct * 128 + (tid & 7) * 16);
      asm volatile("global_load_dwordx4 %0, %1, %2" : "=v"(xres) : "v"(xo), "s"(x) : "memory");
    }
    u32x4 p2c[2], p2s[2];
#pragma unroll
    for (int i2 = 0; i2 < 2; ++i2) {
      float vc[8], vs[8];
#pragma unroll
      for (int e = 0; e < 8; ++e) {
        float rev = (float)(((16 * i2 + r) * (8 * q + e)) & 31) * (1.0f / 32.0f);
        vc[e] = __builtin_amdgcn_cosf(rev); vs[e] = __builtin_amdgcn_sinf(rev);
      }
      p2c[i2] = u32x4{pk2(vc[0], vc[1]), pk2(vc[2], vc[3]), pk2(vc[4], vc[5]), pk2(vc[6], vc[7])};
      p2s[i2] = u32x4{pk2(vs[0], vs[1]), pk2(vs[2], vs[3]), pk2(vs[4], vs[5]), pk2(vs[6], vs[7])};
    }
    BARRIER();
    const unsigned zl = L0 + L_RING + 65536 + w * 8192;
    WAITV(1);
#pragma unroll
    for (int i = 0; i < 8; ++i) {
      PIN(zr[i]);
      int row = 4 * i + q;
      int f = (row & 3) | (((row >> 3) & 1) << 2);
      *(LAS u32x4*)(uintptr_t)(zl + row * 256 + ((r ^ (f << 1)) << 4)) = zr[i];
    }
    {
      const int qq = r >> 2, pp = r & 3;
      const int f = qq | ((q & 1) << 2);
      const unsigned zbase = zl + (8 * q + qq) * 256 + pp * 8;
#pragma unroll
      for (int bk = 0; bk < 4; ++bk) {
        unsigned yy[2][4];
#pragma unroll
        for (int aa = 0; aa < 2; ++aa) {
          const int a = 2 * bk + aa;
          const unsigned ad = zbase + ((a ^ f) << 5);
          u32x4 zf = ldstr(ad, ad + 1024);
#pragma unroll
          for (int i2 = 0; i2 < 2; ++i2) {
            f32x4 z = {0, 0, 0, 0};
            f32x4 tc = mfma16(zf, p2c[i2], z), ts = mfma16(zf, p2s[i2], z);
            yy[i2][2 * aa] = pk2(tc[0] - ts[1], tc[1] + ts[0]);
            yy[i2][2 * aa + 1] = pk2(tc[2] - ts[3], tc[3] + ts[2]);
          }
        }
        u32x4 y0 = {yy[0][0], yy[0][1], yy[0][2], yy[0][3]}, y1 = {yy[1][0], yy[1][1], yy[1][2], yy[1][3]};
#pragma unroll
        for (int dt = 0; dt < 4; ++dt) {
          const float df = (float)(64 * ct + 16 * dt + r) * (1.0f / 448.0f);
          unsigned gg[4];
#pragma unroll
          for (int i = 0; i < 4; ++i) {
            int c = 64 * kg + 16 * bk + 8 * (i >> 1) + 2 * q + (i & 1);
            float rev = __builtin_amdgcn_fractf((float)c * df);
            gg[i] = pk2(__builtin_amdgcn_cosf(rev), -__builtin_amdgcn_sinf(rev));
          }
          u32x4 gf = {gg[0], gg[1], gg[2], gg[3]};
          oacc[dt][0] = mfma16(gf, y0, oacc[dt][0]);
          oacc[dt][1] = mfma16(gf, y1, oacc[dt][1]);
        }
      }
    }
    STAMP(12);
#pragma unroll
    for (int i2 = 0; i2 < 2; ++i2) {
      int row = 32 * half + 16 * i2 + r;
#pragma unroll
      for (int dt = 0; dt < 4; ++dt)
        *(LAS f32x4*)(uintptr_t)(L0 + L_RING + kg * 16384 + row * 256 + (((4 * dt + q) ^ (row & 7)) << 4)) = oacc[dt][i2];
    }
    BARRIER();
    {
      int row = tid >> 3, cp = tid & 7;
      f32x4 s0 = {0, 0, 0, 0}, s1 = {0, 0, 0, 0};
#pragma unroll
      for (int k = 0; k < 4; ++k) {
        s0 += lds128f(L0 + L_RING + k * 16384 + row * 256 + (((2 * cp) ^ (row & 7)) << 4));
        s1 += lds128f(L0 + L_RING + k * 16384 + row * 256 + (((2 * cp + 1) ^ (row & 7)) << 4));
      }
      WAITV(0);
      PIN(xres);
      unsigned x0 = xres.x, x1 = xres.y, x2 = xres.z, x3 = xres.w;
      auto lo = [](unsigned u) { return __builtin_bit_cast(float, u << 16); };
      auto hi = [](unsigned u) { return __builtin_bit_cast(float, u & 0xffff0000u); };
      u32x4 o = {pk2(s0[0] + lo(x0), s0[1] + hi(x0)), pk2(s0[2] + lo(x1), s0[3] + hi(x1)),
                 pk2(s1[0] + lo(x2), s1[1] + hi(x2)), pk2(s1[2] + lo(x3), s1[3] + hi(x3))};
      int gg = 2 * rt + (row >> 5);
      int n2 = ((gg >> 2) & 1) + 8 * (gg >> 3) + 2 * (gg & 3);
      *(u32x4*)(out + (size_t)((64 * (row & 31) + n2) * 896 + ct * 128 + cp * 16)) = o;
    }
    if (b == 0 && tid == 0) *(unsigned*)(ws + WS_EPOCH) = tag;
    STAMP(13);
  } else {
    const int k1 = (bi - 28) * 8 + xcd;
    const int c31 = l & 31, h = l >> 5;
    const int c = 32 * w + c31;
    const bool cok = c < 225;
    float wpr[16], wpi[16], wmr[16], wmi[16];
    {
      const unsigned short* pr = (const unsigned short*)wr;
      const unsigned short* pi = (const unsigned short*)wi;
      const int cc = cok ? c : 224;
      const float sc = cok ? ((c == 0 || c == 224) ? 1.0f : 2.0f) * (1.0f / (2048.0f * 448.0f)) : 0.f;
#pragma unroll
      for (int i = 0; i < 16; ++i) {
        int k2 = (i & 3) + 8 * (i >> 2) + 4 * h;
        int i0 = (k1 + 32 * k2) * 225 + cc, i1 = i0 + 1024 * 225;
        float a0 = __builtin_bit_cast(float, (unsigned)pr[i0] << 16), b0 = __builtin_bit_cast(float, (unsigned)pi[i0] << 16);
        float a1 = __builtin_bit_cast(float, (unsigned)pr[i1] << 16), b1 = __builtin_bit_cast(float, (unsigned)pi[i1] << 16);
        wpr[i] = (a0 + a1) * sc; wpi[i] = (b0 + b1) * sc;
        wmr[i] = (a0 - a1) * sc; wmi[i] = (b0 - b1) * sc;
      }
    }
    {
      const int s = w, par = s >> 2;
      float vc[4], vs[4];
#pragma unroll
      for (int mm = 0; mm < 4; ++mm) {
        int n2 = par + 8 * (2 * (s & 3) + h) + 2 * mm;
        float rev = (float)((n2 * (k1 + 32 * c31)) & 2047) * (1.0f / 2048.0f);
        vc[mm] = __builtin_amdgcn_cosf(rev); vs[mm] = __builtin_amdgcn_sinf(rev);
      }
      u32x4 fre = {pk2(vc[0], vc[1]), pk2(vc[2], vc[3]), pk2(vs[0], vs[1]), pk2(vs[2], vs[3])};
      u32x4 fim = {pk2(-vs[0], -vs[1]), pk2(-vs[2], -vs[3]), pk2(vc[0], vc[1]), pk2(vc[2], vc[3])};
      *(LAS u32x4*)(uintptr_t)(L0 + (2 * s) * 1024 + l * 16) = fre;
      *(LAS u32x4*)(uintptr_t)(L0 + (2 * s + 1) * 1024 + l * 16) = fim;
    }
    if (w < 4) {
      const int par = w >> 1, s2 = w & 1;
      float vc[8], vs[8];
#pragma unroll
      for (int e = 0; e < 8; ++e) {
        int k2 = 16 * s2 + 8 * (e >> 2) + 4 * h + (e & 3);
        float rev = (float)(((par + 2 * c31) * (k1 + 32 * k2)) & 2047) * (1.0f / 2048.0f);
        vc[e] = __builtin_amdgcn_cosf(rev); vs[e] = __builtin_amdgcn_sinf(rev);
      }
      unsigned a = L0 + 16384 + w * 3072 + l * 16;
      *(LAS u32x4*)(uintptr_t)(a) = u32x4{pk2(vc[0], vc[1]), pk2(vc[2], vc[3]), pk2(vc[4], vc[5]), pk2(vc[6], vc[7])};
      *(LAS u32x4*)(uintptr_t)(a + 1024) = u32x4{pk2(vs[0], vs[1]), pk2(vs[2], vs[3]), pk2(vs[4], vs[5]), pk2(vs[6], vs[7])};
      *(LAS u32x4*)(uintptr_t)(a + 2048) = u32x4{pk2(-vs[0], -vs[1]), pk2(-vs[2], -vs[3]), pk2(-vs[4], -vs[5]), pk2(-vs[6], -vs[7])};
    }
#pragma unroll
    for (int i = 0; i < 16; ++i) { PIN(wpr[i]); PIN(wpi[i]); PIN(wmr[i]); PIN(wmi[i]); }
    GET_TAG();
    BARRIER();
    STAMP(1);
    poll_flags(ws + WS_FLAGE, (unsigned)((w == 7 ? 0 : w) * 128 + c31 * 4), (l < 32) && (w < 7 || !(l & 1)), tag);
    STAMP(2);
    u32x4 ef[8];
    {
      const char* eb = ws + WS_E + (size_t)k1 * 65536 + w * 512;
      sfor<0, 8>([&](auto S) {
        constexpr int s = decltype(S)::v;
        const unsigned vo = ((s >> 2) + 2 * (2 * (s & 3) + h)) * 4096 + c31 * 16;
        LD128_SC1(ef[s], eb, vo, 0);
      });
    }
    f32x16 ac[4];
#pragma unroll
    for (int t = 0; t < 4; ++t)
#pragma unroll
      for (int i = 0; i < 16; ++i) ac[t][i] = 0.f;
    sfor<0, 8>([&](auto S) {
      constexpr int s = decltype(S)::v;
      u32x4 fre = lds128(L0 + (2 * s) * 1024 + l * 16), fim = lds128(L0 + (2 * s + 1) * 1024 + l * 16);
      WAITV(7 - s);
      PIN(ef[s]);
      u32x4 e = ef[s];
      if (!cok) e = u32x4{0, 0, 0, 0};
      ac[2 * (s >> 2)] = mfma32(fre, e, ac[2 * (s >> 2)]);
      ac[2 * (s >> 2) + 1] = mfma32(fim, e, ac[2 * (s >> 2) + 1]);
    });
    STAMP(3);
    u32x4 pf[2][2][2];
    {
      float sr[16], si[16], dr[16], di[16];
#pragma unroll
      for (int i = 0; i < 16; ++i) {
        float ar = ac[0][i], ai = ac[1][i], br = ac[2][i], bi_ = ac[3][i];
        sr[i] = ar * wpr[i] - ai * wpi[i] + br * wmr[i] - bi_ * wmi[i];
        si[i] = ar * wpi[i] + ai * wpr[i] + br * wmi[i] + bi_ * wmr[i];
        dr[i] = ar * wmr[i] - ai * wmi[i] + br * wpr[i] - bi_ * wpi[i];
        di[i] = ar * wmi[i] + ai * wmr[i] + br * wpi[i] + bi_ * wpr[i];
      }
#pragma unroll
      for (int s2 = 0; s2 < 2; ++s2) {
        const int o = 8 * s2;
        pf[0][0][s2] = u32x4{pk2(sr[o], sr[o + 1]), pk2(sr[o + 2], sr[o + 3]), pk2(sr[o + 4], sr[o + 5]), pk2(sr[o + 6], sr[o + 7])};
        pf[0][1][s2] = u32x4{pk2(si[o], si[o + 1]), pk2(si[o + 2], si[o + 3]), pk2(si[o + 4], si[o + 5]), pk2(si[o + 6], si[o + 7])};
        pf[1][0][s2] = u32x4{pk2(dr[o], dr[o + 1]), pk2(dr[o + 2], dr[o + 3]), pk2(dr[o + 4], dr[o + 5]), pk2(dr[o + 6], dr[o + 7])};
        pf[1][1][s2] = u32x4{pk2(di[o], di[o + 1]), pk2(di[o + 2], di[o + 3]), pk2(di[o + 4], di[o + 5]), pk2(di[o + 6], di[o + 7])};
      }
    }
    const unsigned zl = L0 + 32768 + w * 8192;
#pragma unroll
    for (int par = 0; par < 2; ++par) {
      f32x16 zre, zim;
#pragma unroll
      for (int i = 0; i < 16; ++i) { zre[i] = 0.f; zim[i] = 0.f; }
#pragma unroll
      for (int s2 = 0; s2 < 2; ++s2) {
        unsigned a = L0 + 16384 + (par * 2 + s2) * 3072 + l * 16;
        u32x4 mc = lds128(a), ms = lds128(a + 1024), mn = lds128(a + 2048);
        zre = mfma32(mc, pf[par][0][s2], zre);
        zim = mfma32(ms, pf[par][0][s2], zim);
        zre = mfma32(mn, pf[par][1][s2], zre);
        zim = mfma32(mc, pf[par][1][s2], zim);
      }
#pragma unroll
      for (int i = 0; i < 16; ++i) {
        int rr = (i & 3) + 8 * (i >> 2) + 4 * h;
        int g = 4 * (par + 2 * (rr >> 2)) + (rr & 3);
        *(LAS unsigned*)(uintptr_t)(zl + g * 128 + c31 * 4) = pk2(zre[i], zim[i]);
      }
    }
    STAMP(4);
    {
      char* zb = ws + WS_Z + (size_t)k1 * 1024 + w * 128;
#pragma unroll
      for (int i = 0; i < 8; ++i) {
        u32x4 v = lds128(zl + i * 1024 + l * 16);
        st128_sc1(zb, (unsigned)((8 * i + (l >> 3)) * 32768 + (l & 7) * 16), v);
      }
    }
    WAITV(0);
    STAMP(5);
    if (l == 0) st32_sc1(ws + WS_FLAGZ, (unsigned)((w >> 1) * 256 + k1 * 8 + (w & 1) * 4), tag);
  }
  asm volatile("s_waitcnt vmcnt(0)" : "+v"(code_touch)::"memory");
  if (b == 0 && w == 0) { unsigned tgt = 3200u + 4u * (stamps[PROBE] - stamps[0]); while (rtime() - stamps[0] < tgt) __builtin_amdgcn_s_sleep(1); }
  if (l == 0) {
    unsigned* dbg = (unsigned*)(ws + WS_DBG) + (b * 8 + w) * NSTAMP;
    for (int i = 0; i < NSTAMP; ++i) dbg[i] = stamps[i];
  }
}

__global__ __launch_bounds__(64) void spin_small(char* ws) { unsigned t0 = rtime(); if (blockIdx.x == 0) while (rtime() - t0 < 2000u) __builtin_amdgcn_s_sleep(1); }
__global__ __launch_bounds__(512, 2) void spin_big(char* ws) { unsigned t0 = rtime(); asm volatile("v_mov_b32 v255, 0" ::: "v255"); if (blockIdx.x == 0 && threadIdx.x < 64) while (rtime() - t0 < 2000u) __builtin_amdgcn_s_sleep(1); }
extern "C" void kernel_launch(void* const* d_in, const int* in_sizes, int n_in, void* d_out, int out_size, void* d_ws,
                              size_t ws_size, hipStream_t stream) {
  (void)in_sizes; (void)n_in; (void)out_size; (void)ws_size;
  (void)hipFuncSetAttribute((const void*)gf_glu_kernel, hipFuncAttributeMaxDynamicSharedMemorySize, L_TOTAL);
  gf_glu_kernel<<<256, 512, L_TOTAL, stream>>>((const char*)d_in[0], (const char*)d_in[1], (const char*)d_in[2],
                                               (const char*)d_in[4], (const char*)d_in[5], (const char*)d_in[6],
                                               (char*)d_out, (char*)d_ws);
  spin_small<<<1, 64, 0, stream>>>((char*)d_ws);

}
```

```cpp
#include <hip/hip_runtime.h>
#include <hip/hip_bf16.h>
#include <stdint.h>

typedef __bf16 bf16x8 __attribute__((ext_vector_type(8)));
typedef __bf16 bf16x2 __attribute__((ext_vector_type(2)));
typedef short s16x4 __attribute__((ext_vector_type(4)));
typedef float f32x2 __attribute__((ext_vector_type(2)));
typedef float f32x4 __attribute__((ext_vector_type(4)));
typedef float f32x16 __attribute__((ext_vector_type(16)));
typedef unsigned u32x4 __attribute__((ext_vector_type(4)));
typedef unsigned u32x2 __attribute__((ext_vector_type(2)));
#define LAS __attribute__((address_space(3)))
#define DI __device__ __forceinline__

constexpr unsigned WS_EPOCH = 0;
constexpr unsigned WS_STAT = 65536;
constexpr unsigned WS_CS = 131072;
constexpr unsigned WS_FLAGZ = 196608;
constexpr unsigned WS_HINT = 229376;
constexpr unsigned WS_EC = 524288;
constexpr unsigned WS_E = 1u << 20;
constexpr unsigned WS_Z = 3u << 20;
constexpr unsigned WS_BYTES = 14u << 20;
constexpr unsigned WS_H = 5u << 20;
constexpr unsigned HBLK = 4608;

constexpr unsigned L_AUX = 131072;
constexpr unsigned L_PRIV = L_AUX;
constexpr unsigned L_ZOK = L_AUX + 3072;
constexpr unsigned L_EB = L_AUX + 3584;
constexpr unsigned L_PAUX = L_AUX + 12288;
constexpr unsigned L_P1 = L_AUX + 28672;
constexpr unsigned L_TOTAL = 163840;

constexpr unsigned SPIN_CAP = 1u << 17;
constexpr unsigned CODE_BYTES = 36864;

template <int N> struct IC { static constexpr int v = N; };
template <int B, int E, class Fn> DI void sfor(Fn&& f) {
  if constexpr (B < E) { f(IC<B>{}); sfor<B + 1, E>(f); }
}

DI unsigned pk2(float a, float b) { f32x2 t = {a, b}; bf16x2 p = __builtin_convertvector(t, bf16x2); return __builtin_bit_cast(unsigned, p); }
DI bf16x8 asbf(u32x4 v) { return __builtin_bit_cast(bf16x8, v); }
DI float asf(unsigned u) { return __builtin_bit_cast(float, u); }
DI unsigned asu(float f) { return __builtin_bit_cast(unsigned, f); }
DI float bflo(unsigned u) { return asf(u << 16); }
DI float bfhi(unsigned u) { return asf(u & 0xffff0000u); }
DI f32x4 mfma16(u32x4 a, u32x4 b, f32x4 c) { return __builtin_amdgcn_mfma_f32_16x16x32_bf16(asbf(a), asbf(b), c, 0, 0, 0); }
DI f32x16 mfma32(u32x4 a, u32x4 b, f32x16 c) { return __builtin_amdgcn_mfma_f32_32x32x16_bf16(asbf(a), asbf(b), c, 0, 0, 0); }
DI void mma(f32x4& c, const u32x4& a, const u32x4& b) { asm volatile("v_mfma_f32_16x16x32_bf16 %0, %1, %2, %0" : "+v"(c) : "v"(a), "v"(b)); }
DI void mma0(f32x4& c, const u32x4& a, const u32x4& b) { asm volatile("v_mfma_f32_16x16x32_bf16 %0, %1, %2, 0" : "=&v"(c) : "v"(a), "v"(b)); }
DI float dot2(unsigned a, unsigned b, float c) {
  return __builtin_amdgcn_fdot2_f32_bf16(__builtin_bit_cast(bf16x2, a), __builtin_bit_cast(bf16x2, b), c, false);
}
DI float sum8(u32x4 v, float c) { unsigned a = v.x, b = v.y, d = v.z, e = v.w; const unsigned o = 0x3f803f80u;
  return dot2(e, o, dot2(d, o, dot2(b, o, dot2(a, o, c)))); }
DI float sq8(u32x4 v, float c) { unsigned a = v.x, b = v.y, d = v.z, e = v.w;
  return dot2(e, e, dot2(d, d, dot2(b, b, dot2(a, a, c)))); }
template <int CTRL> DI float dpp(float v) { return asf((unsigned)__builtin_amdgcn_update_dpp(0, (int)asu(v), CTRL, 0xf, 0xf, true)); }
DI u32x4 lds128(unsigned a) { return *(LAS const u32x4*)(uintptr_t)a; }
DI f32x4 lds128f(unsigned a) { return *(LAS const f32x4*)(uintptr_t)a; }
DI u32x2 ldstr1(unsigned a) { return __builtin_bit_cast(u32x2, __builtin_amdgcn_ds_read_tr16_b64_v4i16((LAS s16x4*)(uintptr_t)a)); }
DI u32x4 cat(u32x2 lo, u32x2 hi) { return __builtin_shufflevector(lo, hi, 0, 1, 2, 3); }
DI u32x4 ldstr(unsigned a0, unsigned a1) {
  s16x4 lo = __builtin_amdgcn_ds_read_tr16_b64_v4i16((LAS s16x4*)(uintptr_t)a0);
  s16x4 hi = __builtin_amdgcn_ds_read_tr16_b64_v4i16((LAS s16x4*)(uintptr_t)a1);
  return __builtin_bit_cast(u32x4, __builtin_shufflevector(lo, hi, 0, 1, 2, 3, 4, 5, 6, 7));
}
typedef u32x4 rsrc_t;
DI rsrc_t mk_rsrc(const void* p, unsigned bytes) {
  const unsigned long long a = (unsigned long long)p;
  rsrc_t d = {(unsigned)a, (unsigned)(a >> 32) & 0xffffu, bytes, 0x00020000u};
  asm volatile("" : "+s"(d));
  return d;
}
template <int ID, int LIMM, int MUL> DI void dma_mul(rsrc_t rs, unsigned voff, unsigned lbase, unsigned k) {
  unsigned t;
  asm volatile("s_add_u32 m0, %3, %5\n\ts_mul_i32 %0, %4, %6\n\tbuffer_load_dwordx4 %1, %2, %0 offen lds ; OP %7"
               : "=&s"(t) : "v"(voff), "s"(rs), "s"(lbase), "s"(k), "i"(LIMM), "i"(MUL), "i"(ID) : "memory", "scc");
}
template <int ID, int LIMM, int ADD> DI void dma_add(rsrc_t rs, unsigned voff, unsigned lbase, unsigned k) {
  unsigned t;
  asm volatile("s_add_u32 m0, %3, %5\n\ts_add_u32 %0, %4, %6\n\tbuffer_load_dwordx4 %1, %2, %0 offen lds ; OP %7"
               : "=&s"(t) : "v"(voff), "s"(rs), "s"(lbase), "s"(k), "i"(LIMM), "i"(ADD), "i"(ID) : "memory", "scc");
}
template <int ID, int LIMM, int ADD> DI void dma_add_sc1(rsrc_t rs, unsigned voff, unsigned lbase, unsigned k) {
  unsigned t;
  asm volatile("s_add_u32 m0, %3, %5\n\ts_add_u32 %0, %4, %6\n\tbuffer_load_dwordx4 %1, %2, %0 offen sc1 lds ; OP %7"
               : "=&s"(t) : "v"(voff), "s"(rs), "s"(lbase), "s"(k), "i"(LIMM), "i"(ADD), "i"(ID) : "memory", "scc");
}
template <int ID, int IMM> DI void st128_sc1(rsrc_t rs, unsigned voff, unsigned soff, u32x4 v) {
  asm volatile("buffer_store_dwordx4 %0, %1, %2, %3 offen offset:%4 sc1 ; OP %5\n\ts_nop 1" ::"v"(v), "v"(voff), "s"(rs), "Is"(soff), "i"(IMM), "i"(ID) : "memory");
}
template <int ID> DI void st64_sc1(rsrc_t rs, unsigned voff, unsigned soff, u32x2 v) {
  asm volatile("buffer_store_dwordx2 %0, %1, %2, %3 offen sc1 ; OP %4" ::"v"(v), "v"(voff), "s"(rs), "Is"(soff), "i"(ID) : "memory");
}
template <int ID> DI void st32_sc1(rsrc_t rs, unsigned voff, unsigned soff, unsigned v) {
  asm volatile("buffer_store_dword %0, %1, %2, %3 offen sc1 ; OP %4" ::"v"(v), "v"(voff), "s"(rs), "Is"(soff), "i"(ID) : "memory");
}
template <int ID, int IMM> DI void ld128_sc1(u32x4& dst, rsrc_t rs, unsigned voff, unsigned soff) {
  asm volatile("buffer_load_dwordx4 %0, %1, %2, %3 offen offset:%4 sc1 ; OP %5" : "=v"(dst) : "v"(voff), "s"(rs), "Is"(soff), "i"(IMM), "i"(ID) : "memory");
}
template <int ID, int IMM> DI void ld128(u32x4& dst, rsrc_t rs, unsigned voff, unsigned soff) {
  asm volatile("buffer_load_dwordx4 %0, %1, %2, %3 offen offset:%4 ; OP %5" : "=v"(dst) : "v"(voff), "s"(rs), "Is"(soff), "i"(IMM), "i"(ID) : "memory");
}
template <int ID, int IMM> DI void ld32s_sc1(unsigned& dst, rsrc_t rs, unsigned voff, unsigned soff) {
  asm volatile("buffer_load_dword %0, %1, %2, %3 offen offset:%4 sc1 ; OP %5" : "=v"(dst) : "v"(voff), "s"(rs), "s"(soff), "i"(IMM), "i"(ID) : "memory");
}
template <int ID> DI void ld32_plain(unsigned& dst, rsrc_t rs, unsigned voff) {
  asm volatile("buffer_load_dword %0, %1, %2, 0 offen ; OP %3" : "=v"(dst) : "v"(voff), "s"(rs), "i"(ID) : "memory");
}
template <int ID> DI void ld32_sc1(unsigned& dst, rsrc_t rs, unsigned voff) {
  asm volatile("buffer_load_dword %0, %1, %2, 0 offen sc1 ; OP %3" : "=v"(dst) : "v"(voff), "s"(rs), "i"(ID) : "memory");
}
template <int N, int NEED> DI void waitv() { asm volatile("s_waitcnt vmcnt(%0) ; NEED %1" ::"i"(N), "i"(NEED) : "memory"); }
template <int NEED> DI void need() { asm volatile("; NEED %0" ::"i"(NEED) : "memory"); }
#define BARRIER() asm volatile("s_waitcnt lgkmcnt(0)\n\ts_barrier" ::: "memory")
#define SB() __builtin_amdgcn_sched_barrier(0)
#define ONLY(g) asm volatile("; ONLY " #g ::: "memory")
DI void keep(const f32x4& a, const f32x4& b) { asm volatile("" ::"v"(a), "v"(b)); }
template <class T> DI void PINS(T& x) { asm volatile("" : "+s"(x)); }
DI float opq(float x) { asm volatile("" : "+v"(x)); return x; }
template <class T> DI void PIN(T& x) { asm volatile("" : "+v"(x)); }

DI void poll_flags(rsrc_t rs, unsigned voff, unsigned tag) {
#pragma nounroll
  for (unsigned spins = 0; spins < SPIN_CAP; ++spins) {
    unsigned got;
    asm volatile("buffer_load_dword %0, %1, %2, 0 offen sc1\n\ts_waitcnt vmcnt(0)" : "=v"(got) : "v"(voff), "s"(rs) : "memory");
    if (__all(got == tag)) break;
    __builtin_amdgcn_s_sleep(3);
  }
}
DI void poll_granules(u32x4& g, rsrc_t rs, unsigned voff, unsigned tag) {
#pragma nounroll
  for (unsigned spins = 0; spins < SPIN_CAP; ++spins) {
    __builtin_amdgcn_s_sleep(4);
    asm volatile("buffer_load_dwordx4 %0, %1, %2, 0 offen sc1\n\ts_waitcnt vmcnt(0)" : "=&v"(g) : "v"(voff), "s"(rs) : "memory");
    if (__all(g.y == tag && g.w == tag)) break;
  }
}
DI unsigned csum8(unsigned t, const u32x4& a, const u32x4& b) { return ((t + a.x + a.y) + a.z + a.w) + ((b.x + b.y + b.z) + b.w); }
DI void reload_h(u32x4* d, unsigned* ck, rsrc_t rs, unsigned vo, unsigned vc, unsigned so, unsigned tag) {
#pragma nounroll
  for (unsigned spins = 0; spins < SPIN_CAP; ++spins) {
    __builtin_amdgcn_s_sleep(8);
    asm volatile("buffer_load_dwordx4 %0, %6, %8, %9 offen sc1\n\tbuffer_load_dwordx4 %1, %6, %8, %9 offen offset:1024 sc1\n\t"
                 "buffer_load_dwordx4 %2, %6, %8, %9 offen offset:2048 sc1\n\tbuffer_load_dwordx4 %3, %6, %8, %9 offen offset:3072 sc1\n\t"
                 "buffer_load_dword %4, %7, %8, %9 offen sc1\n\tbuffer_load_dword %5, %7, %8, %9 offen offset:256 sc1\n\ts_waitcnt vmcnt(0)"
                 : "=&v"(d[0]), "=&v"(d[1]), "=&v"(d[2]), "=&v"(d[3]), "=&v"(ck[0]), "=&v"(ck[1])
                 : "v"(vo), "v"(vc), "s"(rs), "s"(so) : "memory");
    if (__all(csum8(tag, d[0], d[1]) == ck[0] && csum8(tag, d[2], d[3]) == ck[1])) break;
  }
}
DI void reload_e(u32x4& d, unsigned& ck, rsrc_t rs, unsigned vo, unsigned vc, unsigned so, unsigned tag) {
#pragma nounroll
  for (unsigned spins = 0; spins < SPIN_CAP; ++spins) {
    __builtin_amdgcn_s_sleep(10);
    asm volatile("buffer_load_dwordx4 %0, %2, %4, %5 offen sc1\n\tbuffer_load_dword %1, %3, %4, %6 offen sc1\n\ts_waitcnt vmcnt(0)"
                 : "=&v"(d), "=&v"(ck) : "v"(vo), "v"(vc), "s"(rs), "s"(so), "s"(so * 7) : "memory");
    if (__all((tag + d.x + d.y) + d.z + d.w == ck)) break;
  }
}
DI unsigned long long mism(unsigned v, unsigned tag) { unsigned long long m_ = __builtin_amdgcn_ballot_w64(v != tag); asm volatile("" : "+s"(m_)); return m_; }

#define STAMP(i)

constexpr int kTileOfX[7] = {0, 2, 3, 5, 6, 8, 9};
constexpr int kTileOfW[14] = {1, 4, 7, 10, 11, 12, 13, 14, 15, 16, 17, 18, 19, 20};
constexpr int kTileKind[21] = {0, 1, 0, 0, 1, 0, 0, 1, 0, 0, 1, 1, 1, 1, 2, 2, 2, 2, 2, 2, 2};
constexpr int kTileIdx[21] = {0, 0, 1, 2, 1, 3, 4, 2, 5, 6, 3, 4, 5, 6, 0, 1, 2, 3, 4, 5, 6};
constexpr int kSegX0[4] = {0, 1, 3, 5}, kSegNX[4] = {1, 2, 2, 2};
constexpr int kIssue0[5] = {5, 8, 10, 13, 16};
constexpr int tile_of_x(int j) { return kTileOfX[j]; }
constexpr int tile_of_w(int s) { return kTileOfW[s]; }
constexpr int tile_kind(int T) { return kTileKind[T]; }
constexpr int tile_idx(int T) { return kTileIdx[T]; }
enum { OP_EP = 29, OP_P = 30, OP_GS = 50, OP_GC1, OP_GC2, OP_E, OP_H1, OP_H2, OP_POLL, OP_XRES, OP_FL, OP_OPT = 59, OP_HE = 60, OP_HL = 70, OP_Z = 80 };

__global__ __launch_bounds__(512, 2) void gf_glu_kernel(const char* x, const char* wr, const char* wi, const char* wg,
                                                        const char* wu, const char* wd, char* out, char* ws) {
  extern __shared__ __attribute__((aligned(16))) char smem[];
  STAMP(0);
  const int tid = threadIdx.x;
  const int w = __builtin_amdgcn_readfirstlane(tid >> 6);
  const int l = tid & 63, r = l & 15, q = l >> 4;
  const int b = blockIdx.x, xcd = b & 7, bi = b >> 3;
  const unsigned L0 = (unsigned)(uintptr_t)(LAS char*)smem;

  unsigned code_touch;
  {
    unsigned long long pc;
    asm volatile("s_getpc_b64 %0" : "=s"(pc));
    const char* pcb = (const char*)(pc & ~127ull);
    unsigned off = (unsigned)((bi * 16 + w * 2 + (l & 1)) * 128);
    off = off < CODE_BYTES ? off : 0;
    asm volatile("global_load_dword %0, %1, %2" : "=v"(code_touch) : "v"(off), "s"(pcb) : "memory");
  }
  unsigned epoch;
#define LOAD_EPOCH() asm volatile("global_load_dword %0, %1, %2 ; OP %3" : "=v"(epoch) : "v"(0u), "s"(ws), "i"(OP_EP) : "memory")
#define GET_TAG() PIN(epoch); PIN(code_touch); const unsigned tag = (unsigned)__builtin_amdgcn_readfirstlane(epoch) * 0x9E3779B1u + 0x7F4A7C15u
  constexpr unsigned OOB = 0x40000000u;

  if (__builtin_expect(bi >= 4, 1)) {
    const int bj = bi - 4;
    const int pn = 4 * (xcd & 3) + (bj & 3), tt = 7 * (xcd >> 2) + (bj >> 2);
    int rot = (bj >> 2) + 2 * (bj & 3) + (xcd & 1);
    rot = rot >= 7 ? rot - 7 : rot;
    const int m = w & 3, nh = w >> 2;
    const int rt = 4 * xcd + (bj & 3), ct = bj >> 2;
    unsigned kx[7];
    kx[0] = (unsigned)(rot * 128);
    unsigned xoff[2], woff, doff;
    const int lr = l >> 3, lc = l & 7;
    {
      const int R = 16 * (w & 1) + lr;
      const int n2 = (pn & 1) + 8 * (pn >> 1) + 2 * (w >> 1);
      const unsigned rb = (unsigned)((64 * R + n2) * 896), cx = (unsigned)((lc ^ (lr >> 1)) << 4);
      xoff[0] = rb + cx;
      xoff[1] = rb + 8 * 64 * 896 + (cx ^ 64);
      const int swz = ((lr >> 1) & 1) | ((w & 1) << 1);
      woff = (unsigned)((8 * w + lr) * 3584 + tt * 256 + ((lc ^ (swz << 1)) << 4));
    }
    unsigned lw1 = L0 + w * 1024, lw2 = L0 + w * 2048;
    PINS(lw1); PINS(lw2); PINS(kx[0]); PIN(xoff[0]); PIN(xoff[1]); PIN(woff);
    SB();
    const rsrc_t rx = mk_rsrc(x, 2048 * 896), rg = mk_rsrc(wg, 448 * 3584), ru = mk_rsrc(wu, 448 * 3584);
    auto issue1 = [&](auto TI, auto HI) {
      constexpr int T = decltype(TI)::v, h = decltype(HI)::v;
      constexpr int kind = tile_kind(T), sl = (T & 7) * 16384;
      if constexpr (T == 14 && h == 0) { woff += 128; PIN(woff); }
      if constexpr (kind == 0) dma_mul<T, sl + h * 1024, 1>(rx, xoff[h], lw2, kx[tile_idx(T)]);
      else dma_mul<T, sl + h * 8192, 1792>(h ? ru : rg, woff, lw1, kx[tile_idx(T)]);
    };
    auto issue = [&](auto TI) { issue1(TI, IC<0>{}); issue1(TI, IC<1>{}); };
    issue(IC<0>{});
    issue(IC<1>{});
    LOAD_EPOCH();
    STAMP(1);
    SB();
#pragma unroll
    for (int j = 1; j < 7; ++j) { kx[j] = kx[j - 1] == 768 ? 0 : kx[j - 1] + 128; PINS(kx[j]); }
    SB();

    {
      int c = 16 * tt + 8 * nh + (r >> 1);
      const float ph = (r & 1) ? 0.25f : 0.0f;
      c = c ? c : 224;
      const float cfl = (float)c, cf = cfl * (1.0f / 448.0f);
      const float k2 = 2.0f * __builtin_amdgcn_cosf(cf);
#pragma unroll
      for (int it = 0; it < 4; ++it) {
        const int ks = m + 4 * it;
        if (it < 3 || m < 2) {
          const unsigned kk = (m & 2) ? kx[it < 3 ? 2 * it + 1 : 0] : kx[2 * it];
          const float t = cfl * (float)(int)((kk >> 1) + 32 * (m & 1) + 8 * q);
          const float rem = __builtin_fmaf(-448.0f, __builtin_floorf(t * (1.0f / 448.0f)), t);
          const float rev = __builtin_fmaf(rem, 1.0f / 448.0f, ph);
          float v[8];
          v[0] = __builtin_amdgcn_cosf(rev);
          v[1] = __builtin_amdgcn_cosf(rev + cf);
#pragma unroll
          for (int e = 2; e < 8; ++e) v[e] = __builtin_fmaf(k2, v[e - 1], -v[e - 2]);
          *(LAS u32x4*)(uintptr_t)(L0 + L_AUX + (nh * 14 + ks) * 1024 + l * 16) =
              u32x4{pk2(v[0], v[1]), pk2(v[2], v[3]), pk2(v[4], v[5]), pk2(v[6], v[7])};
        } else {
          const int k1 = 16 * (m & 1) + r;
          float v[4];
#pragma unroll
          for (int e = 0; e < 4; ++e)
            v[e] = __builtin_amdgcn_cosf((float)((k1 * (4 * q + e)) & 31) * (1.0f / 32.0f) - (nh ? 0.25f : 0.0f));
          const unsigned a0 = pk2(v[0], v[1]), a1 = pk2(v[2], v[3]), sg = (r & 1) ? 0x80008000u : 0u;
          *(LAS u32x4*)(uintptr_t)(L0 + L_P1 + (2 * nh + (m & 1)) * 1024 + l * 16) = u32x4{a0, a1, a0 ^ sg, a1 ^ sg};
        }
      }
    }
    unsigned xa[2][2], wa[2][2];
    {
      const int R = 32 * m + r;
      xa[0][0] = L0 + R * 128 + ((q ^ ((R >> 1) & 7)) << 4);
      xa[0][1] = xa[0][0] ^ 64;
      const int qq = r >> 2, pp = r & 3;
      const int swz = ((qq >> 1) & 1) | ((q & 1) << 1);
      wa[0][0] = L0 + (8 * q + qq) * 128 + (((2 * nh) ^ swz) << 5) + pp * 8;
      wa[0][1] = wa[0][0] ^ 32;
#pragma unroll
      for (int i = 0; i < 2; ++i) { xa[1][i] = xa[0][i] + 65536; wa[1][i] = wa[0][i] + 65536; PIN(xa[1][i]); PIN(wa[1][i]); PIN(xa[0][i]); PIN(wa[0][i]); }
    }
    unsigned l16 = l * 16, aux16 = L0 + L_AUX + l * 16, fad = aux16 + nh * 14336;
    PIN(l16); PIN(aux16); PIN(fad);
    STAMP(2);
    SB();
    const rsrc_t rd = mk_rsrc(wd, 1792 * 896), rw = mk_rsrc(ws, WS_BYTES);
    const rsrc_t rtch = mk_rsrc(nh ? wd : ((m & 1) ? wi : wr), nh ? 1792 * 896 : 921600);
    const int g1 = 4 * pn + m;
    unsigned pso[7], hso[7];
    unsigned hst_d, hst_c, ecs, gs_off, gc_off, pq;
    const unsigned priv = L0 + L_PRIV + w * 256;
    auto step_setup = [&](int s) {
      const int ro = 4 * rot + w;
      int v = s < 3 ? 8 * s + ro : (s == 3 ? 24 + ro - (w & 4) : 8 * (s - 4) + ro);
      v = v >= 28 ? v - 28 : v;
      const int ps = s < 3 ? 0 : (s == 3 ? nh : 1);
      pso[s] = (unsigned)((128 * (v >> 1) + 64 * ps + 32 * (v & 1)) * 896);
      hso[s] = (unsigned)(WS_H + (rt * 56 + 28 * ps + v) * HBLK);
      PINS(pso[s]); PINS(hso[s]);
    };
    constexpr unsigned S_ = 16384;
    constexpr unsigned ploc[18] = {L_PAUX, 5 * S_, 6 * S_, 7 * S_, 0, S_, 2 * S_, 3 * S_, L_PAUX, 5 * S_, 6 * S_, 7 * S_, L_PAUX, 5 * S_,
                                   0, S_, 2 * S_, 3 * S_};
    auto issue_p1 = [&](auto PI, auto HI) {
      constexpr int P = decltype(PI)::v, h = decltype(HI)::v;
      dma_add<OP_P + P, ploc[P] + h * 1024, (16 * (P & 1) + 8 * h) * 896>(rd, doff, lw2, pso[P >> 1]);
    };
    auto issue_p = [&](auto PI) { issue_p1(PI, IC<0>{}); issue_p1(PI, IC<1>{}); };

    u32x4 xf[14][2], ff[4];
    u32x2 fa_[4][2], fb_[4][2];
    f32x4 accD[2], acc[2][4][2];
    float mrow[2], r2row[2];
    u32x4 gran, ebv;
    unsigned pollv;
    u32x4 he[3][4], hl[4][4];
#pragma unroll
    for (int s = 0; s < 7; ++s) step_setup(s);
    {
      const unsigned hst = WS_H + (unsigned)((g1 >> 1) * 56 + 2 * tt + nh) * HBLK;
      hst_d = hst + (m & 1) * 2048; hst_c = hst + (m & 1) * 256;
      ecs = (unsigned)(WS_EC + (pn * 14 + tt) * 2048);
      PINS(hst_d); PINS(hst_c); PINS(ecs);
    }
    SB();
    issue(IC<2>{});
    unsigned tag, tch, tch2, tco;
    f32x4 cs[4];
    unsigned hpk[2][4];
    auto h_part = [&](auto PS, int i2, int sub) {
      constexpr int ps = decltype(PS)::v;
      f32x4 h0 = ((acc[ps][sub][i2] - mrow[i2] * cs[sub]) * r2row[i2]) * (acc[ps][sub + 2][i2] - mrow[i2] * cs[sub + 2]);
      hpk[i2][2 * sub] = pk2(h0[0], h0[1]); hpk[i2][2 * sub + 1] = pk2(h0[2], h0[3]);
      PIN(hpk[i2][2 * sub]); PIN(hpk[i2][2 * sub + 1]);
    };
    auto h_store = [&](auto PS, auto I2) {
      constexpr int ps = decltype(PS)::v, i2 = decltype(I2)::v;
      st128_sc1<OP_H1 + ps, i2 * 1024>(rw, l16, hst_d + ps * (28 * HBLK), u32x4{hpk[i2][0], hpk[i2][1], hpk[i2][2], hpk[i2][3]});
    };
    unsigned hck[7][2], l4c, hsum;
    auto h_check_store = [&](auto PS) { st32_sc1<OP_H1 + decltype(PS)::v>(rw, l4c, hst_c + decltype(PS)::v * (28 * HBLK), hsum); };
    auto load_h1 = [&](auto SI, auto II) {
      constexpr int s = decltype(SI)::v, i = decltype(II)::v;
      u32x4* dst = s < 3 ? he[s < 3 ? s : 0] : hl[s >= 3 ? s - 3 : 0];
      if constexpr (i < 4) ld128_sc1<OP_HE + s, (i & 3) * 1024>(dst[i & 3], rw, l16, hso[s]);
      else ld32s_sc1<OP_HE + s, (i & 1) * 256>(hck[s][i & 1], rw, l4c, hso[s]);
    };
    auto load_h = [&](auto SI) { sfor<0, 6>([&](auto II) { load_h1(SI, II); }); };
    u32x4 p1c[2], p1s[2], df;
    f32x4 rD[2], tc[2], ts[2];
    float rsr[1], dv[8], ev[4];
    unsigned dpk[4], ea, eo, ebo;
    auto hop = [&](auto OI) {
      constexpr int o = decltype(OI)::v, sub = o / 36, i2 = (o / 18) & 1, i = o % 18, e = i & 3;
      if constexpr (i < 4) dv[e] = opq(__builtin_fmaf(-mrow[i2], cs[sub][e], acc[0][sub][i2][e]));
      else if constexpr (i < 8) ev[e] = opq(__builtin_fmaf(-mrow[i2], cs[sub + 2][e], acc[0][sub + 2][i2][e]));
      else if constexpr (i < 12) dv[e] = opq(dv[e] * r2row[i2]);
      else if constexpr (i < 16) dv[e] = opq(dv[e] * ev[e]);
      else { hpk[i2][2 * sub + (i & 1)] = pk2(dv[2 * (i & 1)], dv[2 * (i & 1) + 1]); PIN(hpk[i2][2 * sub + (i & 1)]); }
    };
    u32x4 gf[2][4];
    float g_d[4], g_f[4], g_k[4], g_t[4], g_u[4], g_c[4][3], g_s[4][3], g_cq;
    unsigned g_mk;
    unsigned g_ny;
    auto gop = [&](auto OI) {
      constexpr int o = decltype(OI)::v, a = o & 3, dt = a, i = o >> 2;
      if constexpr (i == 0) g_d[a] = opq((float)(64 * ct + 16 * dt + r));
      else if constexpr (i == 1) g_t[a] = opq(g_d[a] * g_cq);
      else if constexpr (i == 2) g_f[a] = opq(g_d[a] * (1.0f / 448.0f));
      else if constexpr (i == 3) g_u[a] = opq(g_t[a] * (1.0f / 448.0f));
      else if constexpr (i == 4) g_k[a] = opq(__builtin_amdgcn_cosf(g_f[a]));
      else if constexpr (i == 5) g_u[a] = opq(__builtin_floorf(g_u[a]));
      else if constexpr (i == 6) g_t[a] = opq(__builtin_fmaf(-448.0f, g_u[a], g_t[a]));
      else if constexpr (i == 7) g_t[a] = opq(g_t[a] * (-1.0f / 448.0f));
      else if constexpr (i == 8) g_u[a] = opq(g_t[a] - g_f[a]);
      else if constexpr (i == 9) g_c[a][0] = opq(__builtin_amdgcn_cosf(g_t[a]));
      else if constexpr (i == 10) g_k[a] = opq(g_k[a] + g_k[a]);
      else if constexpr (i == 11) g_s[a][0] = opq(__builtin_amdgcn_sinf(g_t[a]));
      else if constexpr (i == 12) { unsigned p_ = pk2(g_c[a][0], g_s[a][0]) ^ g_ny; PIN(p_); gf[0][dt][0] = p_; }
      else if constexpr (i == 13) g_c[a][1] = opq(__builtin_amdgcn_cosf(g_u[a]));
      else if constexpr (i == 14) g_s[a][1] = opq(__builtin_amdgcn_sinf(g_u[a]));
      else if constexpr (i == 15) { unsigned p_ = pk2(g_c[a][1], g_s[a][1]); PIN(p_); gf[0][dt][1] = p_; }
      else {
        constexpr int e = 2 + (i - 16) / 3, j = (i - 16) % 3;
        if constexpr (j == 0) g_c[a][e % 3] = opq(__builtin_fmaf(g_k[a], g_c[a][(e - 1) % 3], -g_c[a][(e - 2) % 3]));
        else if constexpr (j == 1) g_s[a][e % 3] = opq(__builtin_fmaf(g_k[a], g_s[a][(e - 1) % 3], -g_s[a][(e - 2) % 3]));
        else { unsigned p_ = pk2(g_c[a][e % 3], g_s[a][e % 3]); if constexpr (e >= 4) p_ &= g_mk; PIN(p_); gf[e >> 2][dt][e & 3] = p_; }
      }
    };

    unsigned long long bad0, bad1;
    unsigned da[3][4];
    u32x2 af[2][4][2];
    auto rda1 = [&](int s, int dt, int h) { af[s & 1][dt][h] = ldstr1(da[ploc[2 * s + h] >> 16][dt] + (ploc[2 * s + h] & 0xffff)); };
    auto pin_af = [&](int s) {
#pragma unroll
      for (int dt = 0; dt < 4; ++dt) { PIN(af[s & 1][dt][0]); PIN(af[s & 1][dt][1]); }
    };

    u32x4 ph[3][2], pc, psn;
    unsigned pz_off, zo, za[4], zva, ooff, hs[2];
    const int orow = tid >> 3;
    auto prep = [&](auto SI, auto JI) {
      constexpr int s = decltype(SI)::v, j = decltype(JI)::v;
      u32x4* hfr = s < 3 ? he[s < 3 ? s : 0] : hl[s >= 3 ? s - 3 : 0];
      if constexpr (j == 0) { PIN(hfr[0]); PIN(hfr[1]); PIN(hfr[2]); PIN(hfr[3]); PIN(hck[s][0]); PIN(hck[s][1]); }
      if constexpr (j < 4) { rda1(s, j, 0); rda1(s, j, 1); }
      if constexpr (j >= 1 && j < 5) {
        constexpr int f = (j - 1) >> 1, o = 2 * ((j - 1) & 1);
        hs[0] = (j == 1 ? tag : hs[0]) + hfr[f][o] + hfr[f][o + 1];
        hs[1] = (j == 1 ? tag : hs[1]) + hfr[2 + f][o] + hfr[2 + f][o + 1];
        PIN(hs[0]); PIN(hs[1]);
      }
      if constexpr (j == 5) { bad0 = mism(hs[0], hck[s][0]); bad1 = mism(hs[1], hck[s][1]); }
      if constexpr (j == 7) { if (__builtin_expect((bad0 | bad1) != 0, 0)) reload_h(hfr, hck[s], rw, l16, l4c, hso[s], tag); }
    };

    unsigned pw0, pw1;
    auto side = [&](auto SI, auto KI) {
      constexpr int s = decltype(SI)::v, k = decltype(KI)::v;
      constexpr int G = 32 * s + k;
#define AT(ss, kk) (G == 32 * (ss) + (kk))
#define TILE(ss, kk, T, h) if constexpr (AT(ss, kk)) issue1(IC<T>{}, IC<h>{})
#define PIECE(ss, kk, P, h) if constexpr (AT(ss, kk)) issue_p1(IC<P>{}, IC<h>{})
      if constexpr (AT(0, 7)) issue(IC<4>{});
      TILE(0, 10, 3, 0); TILE(0, 13, 3, 1); TILE(0, 16, 5, 0); TILE(0, 19, 5, 1);
      if constexpr (s == 1 && k % 3 == 0) issue1(IC<6 + k / 6>{}, IC<(k / 3) & 1>{});
      if constexpr (s == 2 && k % 4 == 1) issue1(IC<10 + k / 8>{}, IC<(k >> 2) & 1>{});
      TILE(3, 5, 13, 0); TILE(3, 11, 13, 1); TILE(3, 17, 14, 0); TILE(3, 22, 14, 1);
      TILE(4, 4, 15, 0); TILE(4, 9, 15, 1); TILE(4, 15, 16, 0); TILE(5, 3, 16, 1);
      TILE(5, 11, 17, 0); TILE(5, 15, 17, 1); TILE(6, 4, 18, 0);
      TILE(6, 13, 18, 1); TILE(7, 1, 19, 0); TILE(7, 5, 19, 1); TILE(7, 9, 20, 0); TILE(7, 13, 20, 1);
      if constexpr (AT(1, 13)) {
        const unsigned gw = (unsigned)((bj * 8 + xcd) * 4 + m);
        tco = l < (nh ? 14 : 17) ? ((nh ? gw * 14 : (gw >> 1) * 17) + l) * 128 : OOB; PIN(tco);
      }
      if constexpr (AT(2, 23)) ld32_plain<OP_EP>(tch, rtch, tco);
      PIECE(8, 1, 0, 0); PIECE(8, 4, 0, 1); PIECE(9, 0, 1, 0); PIECE(9, 9, 1, 1);
      PIECE(11, 1, 2, 0); PIECE(11, 5, 2, 1); PIECE(11, 9, 3, 0); PIECE(11, 13, 3, 1);
      if constexpr (s == 12 && k % 4 == 1) load_h1(IC<0>{}, IC<k / 4>{});
      if constexpr (AT(12, 15)) { load_h1(IC<0>{}, IC<4>{}); load_h1(IC<0>{}, IC<5>{}); }
      if constexpr (s == 13 && k % 4 == 2) load_h1(IC<1>{}, IC<k / 4>{});
      if constexpr (AT(13, 15)) { load_h1(IC<1>{}, IC<4>{}); load_h1(IC<1>{}, IC<5>{}); }
      if constexpr (AT(1, 1)) { need<OP_EP>(); PIN(epoch); PIN(code_touch); tag = (unsigned)__builtin_amdgcn_readfirstlane(epoch); PINS(tag); }
      if constexpr (AT(1, 5)) { tag = tag * 0x9E3779B1u + 0x7F4A7C15u; PINS(tag); }
      if constexpr (AT(1, 7)) { gs_off = (unsigned)(WS_STAT + (g1 * 32 + (l & 31)) * 16); PIN(gs_off); }
      if constexpr (AT(1, 10)) { pq = priv + q * 16; pw0 = priv + l * 4; PIN(pq); PIN(pw0); }
      if constexpr (AT(1, 16)) { ea = L0 + L_EB + r * 264 + m * 64 + nh * 32 + q * 8; PIN(ea); }
      if constexpr (AT(1, 19)) { eo = (unsigned)((((tid >> 4) * 16 + pn) * 256 + 16 * tt + r) * 16); PIN(eo); }
      if constexpr (AT(1, 22)) { ebo = L0 + L_EB + (tid >> 4) * 264 + r * 4; PIN(ebo); }
      if constexpr (AT(2, 18)) { gc_off = (unsigned)(WS_CS + (tt * 4 + (q & 1)) * 512 + (32 * nh + 2 * r) * 8); PIN(gc_off); }
      if constexpr (AT(2, 15)) { pw1 = priv + (2 * (l & 31) + (l >> 5)) * 4; PIN(pw1); }
      if constexpr (AT(5, 13)) *(LAS unsigned*)(uintptr_t)(L0 + L_ZOK) = 0u;
      if constexpr (AT(6, 0)) { doff = (unsigned)(lr * 896 + ct * 128 + ((lc ^ (((lr >> 1) & 3) << 1)) << 4)); PIN(doff); }
      if constexpr (AT(6, 2)) { l4c = l * 4 + 4096; PIN(l4c); }
      if constexpr (AT(9, 2)) { const int row = 4 * q + (r >> 2); da[0][0] = L0 + w * 2048 + row * 128 + (((row >> 1) & 3) << 5) + (r & 3) * 8; PIN(da[0][0]); }
      if constexpr (AT(9, 5) || AT(9, 6) || AT(9, 8)) { constexpr int dt = k == 8 ? 3 : k - 4; da[0][dt] = da[0][0] ^ (dt << 5); PIN(da[0][dt]); }
      if constexpr (s == 9 && k >= 10 && k < 14) { da[1][k - 10] = da[0][k - 10] + 65536; da[2][k - 10] = da[0][k - 10] + 131072; PIN(da[1][k - 10]); PIN(da[2][k - 10]); }
      if constexpr (AT(9, 14)) pc = lds128(aux16 + (L_P1 - L_AUX));
      if constexpr (AT(9, 15)) psn = lds128(aux16 + (L_P1 - L_AUX) + 2048);
      if constexpr ((s == 10 && k >= 4) || (s == 11 && k < 4)) {
        constexpr int n = 16 * (s - 10) + k - 4, i = n >> 2, v = n & 3;
        const unsigned c_ = pc[i], s_ = psn[i];
        unsigned u0 = (v & 1) ? __builtin_amdgcn_perm(c_, s_, (v & 2) ? 0x07060302u : 0x05040100u) : __builtin_amdgcn_perm(s_, c_, (v & 2) ? 0x07060302u : 0x05040100u);
        if constexpr (v == 0) { u0 ^= 0x80000000u; PIN(u0); ph[0][0][i] = u0; }
        if constexpr (v == 1) { PIN(u0); ph[0][1][i] = u0; }
        if constexpr (v == 2) { unsigned u1 = u0 ^ 0x8000u; u0 ^= 0x80000000u; PIN(u0); PIN(u1); ph[1][0][i] = u0; ph[2][0][i] = u1; }
        if constexpr (v == 3) { unsigned u1 = u0 ^ 0x80008000u; PIN(u0); PIN(u1); ph[1][1][i] = u0; ph[2][1][i] = u1; }
      }
      if constexpr (AT(11, 4)) { pz_off = w ? OOB : (unsigned)(WS_FLAGZ + (l & 31) * 128); PIN(pz_off); }
      if constexpr (AT(11, 6)) { zo = (unsigned)(((l >> 1) & 7) ^ ((q & 1) | ((q & 2) << 1))); PIN(zo); }
      if constexpr (AT(11, 8)) { zo = q * 256 + (zo << 5) + (l & 1) * 16; PIN(zo); }
      if constexpr (AT(11, 10)) { const int P = 2 * q + (r >> 3); za[0] = (unsigned)(((r >> 1) & 2) ^ ((P & 1) | ((P & 2) << 1))); za[1] = P * 256 + (r & 3) * 8; PIN(za[0]); PIN(za[1]); }
      if constexpr (AT(11, 12)) { za[0] = lw2 + za[1] + (za[0] << 5); zva = lw2 + l16; PIN(za[0]); PIN(zva); }
      if constexpr (AT(11, 14)) { za[1] = za[0] ^ 128; za[2] = za[0] ^ 32; za[3] = za[0] ^ 160; PIN(za[1]); PIN(za[2]); PIN(za[3]); }
      if constexpr (AT(12, 0)) {
        const int gg = 2 * rt + (w >> 2);
        const int n2 = ((gg >> 2) & 1) + 8 * (gg >> 3) + 2 * (gg & 3);
        ooff = (unsigned)((64 * (orow & 31) + n2) * 896 + ct * 128 + lc * 16); PIN(ooff);
      }
      if constexpr (AT(12, 3)) { g_cq = opq((float)(28 * w + 8 * q)); }
      if constexpr (AT(12, 6)) { g_mk = q == 3 ? 0u : ~0u; PIN(g_mk); }
      if constexpr (AT(12, 8)) { g_ny = (tid < 16 && (tid & 1)) ? 0x8000u : 0u; PIN(g_ny); }
      if constexpr (AT(2, 22)) ld128_sc1<OP_GS, 0>(gran, rw, gs_off, 0u);
      if constexpr (AT(3, 10)) { waitv<2, OP_GS>(); PIN(gran); bad0 = mism(gran.y, tag); bad1 = mism(gran.w, tag); }
      if constexpr (AT(3, 13)) {
        if (__builtin_expect((bad0 | bad1) != 0, 0)) poll_granules(gran, rw, gs_off, tag);
        *(LAS unsigned*)(uintptr_t)(pw0) = gran.z;
      }
      if constexpr (AT(3, 15)) {
        auto sw = __builtin_amdgcn_permlane16_swap(gran.x, gran.x, false, false);
        const unsigned a0 = sw[0], a1 = sw[1];
        mrow[0] = opq(asf(a0)); mrow[1] = opq(asf(a1));
      }
      if constexpr (AT(3, 17)) { rsr[0] = opq(asf(gran.z) * asf(gran.z)); }
      if constexpr (AT(3, 21)) {
        auto sw = __builtin_amdgcn_permlane16_swap(asu(rsr[0]), asu(rsr[0]), false, false);
        const unsigned a0 = sw[0], a1 = sw[1];
        r2row[0] = opq(asf(a0)); r2row[1] = opq(asf(a1));
      }
      if constexpr (AT(3, 16) || AT(3, 18)) rD[(k - 16) >> 1] = lds128f(pq + ((k - 16) >> 1) * 64);
      if constexpr (AT(3, 19) || AT(3, 20)) p1s[k - 19] = lds128(aux16 + (L_P1 - L_AUX) + 2048 + (k - 19) * 1024);
      if constexpr (AT(4, 0) || AT(4, 2)) p1c[k >> 1] = lds128(aux16 + (L_P1 - L_AUX) + (k >> 1) * 1024);
      if constexpr (s == 4 && k >= 1 && k < 5) {
        constexpr int e = 2 * (k - 1);
        dv[e] = opq(rD[e >> 2][e & 3] * accD[e >> 2][e & 3]);
        dv[e + 1] = opq(rD[e >> 2][(e & 3) + 1] * accD[e >> 2][(e & 3) + 1]);
      }
      if constexpr (AT(4, 5) || AT(4, 6)) {
        constexpr int e = 4 * (k - 5);
        dpk[e >> 1] = pk2(dv[e], dv[e + 1]); dpk[(e >> 1) + 1] = pk2(dv[e + 2], dv[e + 3]);
        PIN(dpk[e >> 1]); PIN(dpk[(e >> 1) + 1]);
      }
      if constexpr (AT(4, 7)) {
        df = u32x4{dpk[0], dpk[1], dpk[2], dpk[3]};
        mma0(ts[0], df, p1s[0]); mma0(tc[0], df, p1c[0]); mma0(ts[1], df, p1s[1]); mma0(tc[1], df, p1c[1]);
      }
      if constexpr (s == 4 && k >= 10 && k < 14) {
        constexpr int a = (k - 10) >> 1, e = 2 * (k & 1);
        ev[0] = opq(tc[a][e] + ts[a][e + 1]);
        ev[1] = opq(tc[a][e + 1] - ts[a][e]);
        dpk[k & 1] = pk2(ev[0], ev[1]); PIN(dpk[k & 1]);
      }
      if constexpr (AT(4, 11) || AT(4, 13)) *(LAS u32x2*)(uintptr_t)(ea + ((k - 10) >> 1) * (16 * 264)) = u32x2{dpk[0], dpk[1]};
      if constexpr (AT(5, 0)) {
        waitv<5, 13>();
        asm volatile("s_waitcnt lgkmcnt(3)\n\ts_barrier" ::: "memory");
#pragma unroll
        for (int i = 0; i < 4; ++i) ebv[i] = *(LAS const unsigned*)(uintptr_t)(ebo + i * 64);
      }
      if constexpr (AT(5, 5)) ld128_sc1<OP_GC1, 0>(gran, rw, gc_off, 0u);
      if constexpr (AT(5, 4)) { hsum = (tag + ebv.x + ebv.y) + ebv.z + ebv.w; PIN(hsum); tch2 = tid * 4; PIN(tch2); }
      if constexpr (AT(5, 7)) { st128_sc1<OP_E, 0>(rw, eo, WS_E, ebv); st32_sc1<OP_E>(rw, tch2, ecs, hsum); }
      if constexpr (AT(5, 2)) { tco = (tid >= 480 && bj >= 24) ? (unsigned)(WS_HINT + ((bj - 24) * 8 + (xcd ^ 4)) * 128 + (tid - 480) * 4) : OOB; dpk[2] = tag; PIN(tco); PIN(dpk[2]); }
      if constexpr (AT(5, 9)) st32_sc1<OP_E>(rw, tco, 0u, dpk[2]);
      if constexpr (AT(6, 6)) { waitv<6, OP_GC1>(); need<16>(); PIN(gran); bad0 = mism(gran.y, tag); bad1 = mism(gran.w, tag); }
      if constexpr (AT(6, 7)) asm volatile("s_barrier" ::: "memory");
      if constexpr (AT(12, 11)) { waitv<7, OP_GC2>(); PIN(tch); PIN(gran); bad0 = mism(gran.y, tag); bad1 = mism(gran.w, tag); }
      if constexpr (AT(6, 9) || AT(12, 14)) {
        if (__builtin_expect((bad0 | bad1) != 0, 0)) poll_granules(gran, rw, gc_off + (s == 12 ? 1024 : 0), tag);
        *(LAS unsigned*)(uintptr_t)(pw1) = l < 32 ? gran.x : gran.z;
      }
      if constexpr (AT(6, 12) || AT(6, 14) || AT(7, 1) || AT(7, 3)) { constexpr int i = s == 6 ? k - 12 : k; cs[i] = lds128f(pq + i * 64); }
      if constexpr (s == 13 && k >= 3 && k < 11 && (k & 1)) cs[(k - 3) >> 1] = lds128f(pq + ((k - 3) >> 1) * 64);
      if constexpr ((s == 7 && k >= 8) || (s == 8 && k < 10)) {
        constexpr int o = 4 * (16 * (s - 7) + k - 8);
        hop(IC<o>{}); hop(IC<o + 1>{}); hop(IC<o + 2>{}); hop(IC<o + 3>{});
      }
      if constexpr (AT(10, 8)) ld128_sc1<OP_GC2, 1024>(gran, rw, gc_off, 0u);
      if constexpr (AT(8, 7)) h_store(IC<0>{}, IC<0>{});
      if constexpr (AT(8, 10)) { h_store(IC<0>{}, IC<1>{}); hsum = (tag + hpk[0][0] + hpk[0][1]) + hpk[0][2] + hpk[0][3]; PIN(hsum); }
      if constexpr (AT(8, 11)) { hsum = (hsum + hpk[1][0] + hpk[1][1]) + hpk[1][2] + hpk[1][3]; PIN(hsum); }
      if constexpr (AT(8, 12)) h_check_store(IC<0>{});
      if constexpr (AT(8, 14)) { waitv<11, 17>(); asm volatile("s_barrier" ::: "memory"); }
      if constexpr (AT(10, 7)) { waitv<7, 20>(); asm volatile("s_barrier" ::: "memory"); }
      if constexpr (AT(13, 7)) asm volatile("s_barrier" ::: "memory");
    };

    sfor<0, 14>([&](auto SI) {
      constexpr int s = decltype(SI)::v;
      constexpr int T = tile_of_w(s), sl = T & 7;
      constexpr int ps = s / 7, j = s % 7, pps = (s - 1) / 7, pj = (s + 6) % 7;
      constexpr int x0 = s < 4 ? kSegX0[s < 4 ? s : 0] : 0, nx = s < 4 ? kSegNX[s < 4 ? s : 0] : 0;
      constexpr int waitB[5] = {3, 2, 4, 6, 6}, lastT[5] = {1, 3, 7, 10, 12};
      if constexpr (s <= 4) { waitv<waitB[s <= 4 ? s : 0], lastT[s <= 4 ? s : 0]>(); BARRIER(); }
      SB();
      sfor<0, 16 + 4 * nx>([&](auto KI) {
        constexpr int k = decltype(KI)::v, i = (k & 7) >> 1, h = k & 1;
        if constexpr (k < 8) {
          if constexpr (s > 0) mma(acc[pps][i][h], cat(fb_[i][0], fb_[i][1]), xf[2 * pj + 1][h]);
        } else if constexpr (k < 16) {
          if constexpr (j == 0) mma0(acc[ps][i][h], cat(fa_[i][0], fa_[i][1]), xf[0][h]);
          else mma(acc[ps][i][h], cat(fa_[i][0], fa_[i][1]), xf[2 * j][h]);
        } else {
          if constexpr (s == 0 && k < 18) mma0(accD[h], xf[0][h], ff[0]);
          else mma(accD[h], xf[2 * x0 + i][h], ff[i]);
        }
        SB();
        constexpr int xr = s == 0 ? k : (k < 2 ? k : ((k & 1) ? -1 : 1 + k / 2));
        if constexpr (xr >= 0 && xr < 4 * nx) {
          constexpr int xt = x0 + (xr >> 2), kk = (xr >> 1) & 1, xs = tile_of_x(xt) & 7;
          xf[2 * xt + kk][xr & 1] = lds128(xa[xs >> 2][kk] + (xs & 3) * 16384 + (xr & 1) * 2048);
        }
        if constexpr (k < 16) {
          const unsigned a = wa[sl >> 2][i & 1] + (sl & 3) * 16384 + (i >> 1) * 8192 + (k >> 3) * 4096 + h * 512;
          if constexpr (k < 8) fa_[i][h] = ldstr1(a); else fb_[i][h] = ldstr1(a);
        }
        if constexpr (k >= 9 && (k & 1) && (k - 9) / 2 < 2 * nx) ff[(k - 9) / 2] = lds128(fad + (2 * x0 + (k - 9) / 2) * 1024);
        SB();
        side(SI, KI);
        SB();
      });
      if constexpr (s == 0) STAMP(3);
      if constexpr (s == 3) STAMP(4);
      if constexpr (s == 5) STAMP(5);
      if constexpr (s == 7) STAMP(6);
      if constexpr (s == 9) STAMP(7);
      if constexpr (s == 11) STAMP(8);
    });
    waitv<6, OP_HE + 0>();
    need<OP_P + 3>();
    sfor<0, 8>([&](auto KI) {
      constexpr int k = decltype(KI)::v, i = ((k >> 1) & 1) * 2 + (k >> 2), h = k & 1;
      mma(acc[1][i][h], cat(fb_[i][0], fb_[i][1]), xf[13][h]);
      SB();
      prep(IC<0>{}, KI);
      SB();
    });
    STAMP(9);
    h_part(IC<1>{}, 0, 0); h_part(IC<1>{}, 1, 0);
    h_part(IC<1>{}, 0, 1); h_store(IC<1>{}, IC<0>{});
    h_part(IC<1>{}, 1, 1); h_store(IC<1>{}, IC<1>{});
    hsum = csum8(tag, u32x4{hpk[0][0], hpk[0][1], hpk[0][2], hpk[0][3]}, u32x4{hpk[1][0], hpk[1][1], hpk[1][2], hpk[1][3]});
    h_check_store(IC<1>{});
    SB();
    STAMP(10);

    f32x4 oacc[4][4];
    auto gstep = [&](auto SI, u32x4* hfr, auto&& fn) {
      constexpr int s = decltype(SI)::v;
      sfor<0, 16>([&](auto KI) {
        constexpr int k = decltype(KI)::v, dt = k >> 2, rf = k & 3;
        if constexpr (s == 0) mma0(oacc[dt][rf], cat(af[0][dt][0], af[0][dt][1]), hfr[rf]);
        else mma(oacc[dt][rf], cat(af[s & 1][dt][0], af[s & 1][dt][1]), hfr[rf]);
        SB();
        fn(KI);
        SB();
      });
    };
    auto gops = [&](auto B0, auto N) { sfor<decltype(B0)::v, (decltype(B0)::v + decltype(N)::v < 136 ? decltype(B0)::v + decltype(N)::v : 136)>([&](auto O) { gop(O); }); };
    auto step_ld = [&](auto SI, auto NI) {
      constexpr int s = decltype(SI)::v, n = decltype(NI)::v;
      if constexpr (n < 4) issue_p1(IC<2 * s + n / 2>{}, IC<n & 1>{});
      else load_h1(SI, IC<n - 4>{});
      if constexpr (n == 7) { load_h1(SI, IC<4>{}); load_h1(SI, IC<5>{}); }
    };
    gstep(IC<0>{}, he[0], [&](auto KI) {
      constexpr int k = decltype(KI)::v;
      if constexpr (k == 0 || k == 3 || k == 6) step_ld(IC<2>{}, IC<k / 3>{});
      if constexpr (k == 8) waitv<6, OP_HE + 1>();
      if constexpr (k >= 8) prep(IC<1>{}, IC<k - 8>{});
      if constexpr (k < 8) gops(IC<4 * k>{}, IC<4>{}); else gops(IC<32 + 3 * (k - 8)>{}, IC<3>{});
    });
    gstep(IC<1>{}, he[1], [&](auto KI) {
      constexpr int k = decltype(KI)::v;
      gops(IC<56 + 5 * k>{}, IC<5>{});
      if constexpr (k >= 11) step_ld(IC<2>{}, IC<k - 8>{});
    });
    STAMP(11);
    sfor<0, 8>([&](auto NI) { step_ld(IC<3>{}, NI); });
    ld32_sc1<OP_POLL>(pollv, rw, pz_off);
    waitv<11, OP_HE + 2>();
    sfor<0, 8>([&](auto JI) { prep(IC<2>{}, JI); });
    STAMP(12);
    gstep(IC<2>{}, he[2], [&](auto KI) {
      constexpr int k = decltype(KI)::v;
      if constexpr (k < 8) step_ld(IC<4>{}, KI);
      if constexpr (k == 8) { waitv<10, OP_POLL>(); need<OP_HE + 3>(); }
      if constexpr (k >= 8) prep(IC<3>{}, IC<k - 8>{});
    });
    STAMP(13);
    PIN(pollv);
    pin_af(3);
    u32x4 xres;
    const unsigned zso = (unsigned)(WS_Z + 2 * rt * 32768 + w * 4096);
    if (w == 0) {
      if (mism(pollv, tag) != 0) poll_flags(rw, pz_off, tag);
      *(volatile LAS unsigned*)(uintptr_t)(L0 + L_ZOK) = 1u;
    } else {
#pragma nounroll
      for (unsigned spins = 0; spins < 64 * SPIN_CAP; ++spins) {
        if (__builtin_amdgcn_readfirstlane(*(volatile LAS unsigned*)(uintptr_t)(L0 + L_ZOK))) break;
        __builtin_amdgcn_s_sleep(1);
      }
    }
    asm volatile("" ::: "memory");
    STAMP(14);
    auto issue_z1 = [&](auto NI, auto IDI) {
      constexpr int zi = decltype(NI)::v >> 1, zh = decltype(NI)::v & 1;
      dma_add_sc1<decltype(IDI)::v, ploc[14 + zi] + zh * 1024, (zi >> 1) * 32768 + (zi & 1) * 2048 + zh * 1024>(rw, zo, lw2, zso);
    };
    gstep(IC<3>{}, hl[0], [&](auto KI) {
      constexpr int k = decltype(KI)::v;
      if constexpr (k < 8) issue_z1(KI, IC<OP_Z>{});
      if constexpr (k == 7) ld128<OP_XRES, 0>(xres, rx, ooff, 0u);
      if constexpr (k == 8) waitv<9, OP_HE + 4>();
      if constexpr (k >= 8) prep(IC<4>{}, IC<k - 8>{});
    });
    gstep(IC<4>{}, hl[1], [&](auto KI) {
      constexpr int k = decltype(KI)::v;
      if constexpr (!(k & 1)) step_ld(IC<5>{}, IC<k / 2>{});
    });
    STAMP(15);
    waitv<10, OP_XRES>();
    need<OP_Z>();
    {
      u32x4 z0[2], z1[2], zv[4];
      f32x4 t[2][6];
      unsigned yy[2][2][4], zs;
      auto rdz = [&](int u, int par) {
        const unsigned a_ = za[2 * (u & 1) + par] + (u >> 1) * 2 * S_;
        (par ? z1 : z0)[u & 1] = ldstr(a_, a_ + S_);
      };
      auto amma = [&](int u, int i) {
        f32x4* tt_ = t[u & 1];
        if (i < 2) mma0(tt_[i], z0[u & 1], ph[0][i]);
        else asm volatile("v_mfma_f32_16x16x32_bf16 %0, %1, %2, %3" : "=&v"(tt_[i]) : "v"(z1[u & 1]), "v"(ph[i >> 1][i & 1]), "v"(tt_[i & 1]));
      };
      auto ypk = [&](int u, int k) { yy[u & 1][k >> 2][k & 3] = pk2(t[u & 1][2 + 2 * (k >> 2)][k & 3], t[u & 1][3 + 2 * (k >> 2)][k & 3]); PIN(yy[u & 1][k >> 2][k & 3]); };
#pragma nounroll
      for (unsigned spins = 0;; ++spins) {
        rdz(0, 0); rdz(0, 1);
#pragma unroll
        for (int i = 0; i < 4; ++i) zv[i] = lds128(zva + (i >> 1) * S_ + (i & 1) * 1024);
        rdz(1, 0); rdz(1, 1);
        SB();
#pragma unroll
        for (int i = 0; i < 12; ++i) {
          amma(i / 6, i % 6);
          SB();
          if (i >= 1 && i < 5) { zs = ((i == 1 ? 0u : zs) + zv[i - 1].x + zv[i - 1].y) + zv[i - 1].z + zv[i - 1].w; PIN(zs); zv[i - 1] = lds128(zva + (2 + ((i - 1) >> 1)) * S_ + ((i - 1) & 1) * 1024); }
          if (i >= 8) { zs = (zs + zv[i - 8].x + zv[i - 8].y) + zv[i - 8].z + zv[i - 8].w; PIN(zs); }
          if (i == 6) rdz(2, 0);
          if (i == 7) rdz(2, 1);
          SB();
        }
        rdz(3, 0); rdz(3, 1);
        SB();
#pragma unroll
        for (int k = 0; k < 8; ++k) {
          ypk(0, k);
          if (k == 1) { zs += (unsigned)__builtin_amdgcn_update_dpp(0, (int)zs, 0x128, 0xf, 0xf, true); PIN(zs); }
          if (k == 3) { zs += (unsigned)__builtin_amdgcn_update_dpp(0, (int)zs, 0x124, 0xf, 0xf, true); PIN(zs); }
          if (k == 5) { zs += (unsigned)__builtin_amdgcn_update_dpp(0, (int)zs, 0x122, 0xf, 0xf, true); PIN(zs); }
          if (k == 7) { zs += (unsigned)__builtin_amdgcn_update_dpp(0, (int)zs, 0x121, 0xf, 0xf, true); PIN(zs); }
          if (k == 2) keep(t[1][0], t[1][1]);
          SB();
        }
        bad0 = mism(zs, tag * 16u);
#pragma unroll
        for (int i = 0; i < 6; ++i) amma(2, i);
        if (__builtin_expect(bad0 == 0 || spins >= SPIN_CAP, 1)) break;
        __builtin_amdgcn_s_sleep(10);
        asm volatile("s_waitcnt lgkmcnt(0)" ::: "memory");
        sfor<0, 8>([&](auto NI) { issue_z1(NI, IC<OP_OPT>{}); });
        asm volatile("s_waitcnt vmcnt(0)" ::: "memory");
      }
      sfor<0, 4>([&](auto UI) {
        constexpr int u = decltype(UI)::v;
        const u32x4 y0 = {yy[u & 1][0][0], yy[u & 1][0][1], yy[u & 1][0][2], yy[u & 1][0][3]};
        const u32x4 y1 = {yy[u & 1][1][0], yy[u & 1][1][1], yy[u & 1][1][2], yy[u & 1][1][3]};
        if (u == 1) {
#pragma unroll
          for (int i = 0; i < 6; ++i) amma(3, i);
        } else if (u > 1) asm volatile("s_nop 1");
        sfor<0, 8>([&](auto KI) {
          constexpr int k = decltype(KI)::v;
          mma(oacc[k >> 1][2 * (u >> 1) + (k & 1)], gf[u & 1][k >> 1], (k & 1) ? y1 : y0);
          SB();
          if (u < 3) ypk(u + 1, k);
          if (u < 2 && k == 3) keep(t[u & 1][0], t[u & 1][1]);
          if constexpr (u < 2 && (k & 1)) step_ld(IC<6>{}, IC<4 * u + k / 2>{});
          if constexpr (u == 3 && k == 0) waitv<10, OP_HE + 5>();
          if constexpr (u == 3) prep(IC<5>{}, KI);
          SB();
        });
      });
    }
    STAMP(17);
    f32x4 fin[2];
    u32x4 sel[4], xd;
    unsigned pw[4], ra[8], bp;
    gstep(IC<5>{}, hl[2], [&](auto KI) {
      constexpr int k = decltype(KI)::v;
      if constexpr (k == 0) { PIN(xres); bp = (unsigned)((8 * (r & 7) + 4 * (r >> 3) + q) * 4); PIN(bp); }
      if constexpr (k >= 1 && k < 5) {
        const unsigned t_ = xres[k - 1]; xd[k - 1] = (unsigned)__builtin_amdgcn_ds_bpermute((int)bp, (int)t_);
        const unsigned o_ = r == 4 * (k - 1) + q ? 0x3f803f80u : 0u; sel[k - 1] = u32x4{o_, o_, o_, o_}; PIN(sel[k - 1]);
      }
      if constexpr (k == 5) { pw[0] = lw2 + r * 128 + ((q ^ r ^ (w & 1) ^ ((w & 2) << 2)) << 3); PIN(pw[0]); }
      if constexpr (k == 6) { pw[1] = pw[0] ^ 32; pw[2] = pw[0] ^ 64; pw[3] = pw[0] ^ 96; PIN(pw[1]); PIN(pw[2]); PIN(pw[3]); }
      if constexpr (k == 7) { bp = (unsigned)((lr + 8 * (lc >> 2) + 16 * (lc & 3)) * 4); PIN(bp); }
      if constexpr (k == 8) waitv<0, OP_HE + 6>();
      if constexpr (k >= 8) prep(IC<6>{}, IC<k - 8>{});
    });
    STAMP(18);
    auto put = [&](int j) {
      const f32x4 o = oacc[j >> 2][j & 3];
      *(LAS u32x2*)(uintptr_t)(pw[j >> 2] + (j & 3) * 16384) = u32x2{pk2(o[0], o[1]), pk2(o[2], o[3])};
    };
    gstep(IC<6>{}, hl[3], [&](auto KI) {
      constexpr int k = decltype(KI)::v;
      if constexpr (k == 0) {
        const int qq = r >> 2, row = 8 * (w & 1) + q;
        ra[0] = L0 + (w >> 1) * 16384 + qq * 2048 + row * 128 + (((2 * (r & 3)) ^ row ^ (qq & 1) ^ ((qq & 2) << 2)) << 3); PIN(ra[0]);
      }
      if constexpr (k == 1) {
#pragma unroll
        for (int i = 1; i < 8; ++i) { ra[i] = ra[0] ^ (((i >> 1) & 1) << 5) ^ ((i >> 2) << 6) ^ ((i & 1) << 3); PIN(ra[i]); }
      }
      if constexpr (k == 2) {
#pragma unroll
        for (int i = 0; i < 4; ++i) { const unsigned t_ = xd[i]; fin[i >> 1][2 * (i & 1)] = opq(bflo(t_)); fin[i >> 1][2 * (i & 1) + 1] = opq(bfhi(t_)); }
      }
      if constexpr (k >= 3) put(k - 3);
    });
    STAMP(19);
    put(13); SB(); put(14); SB(); put(15);
    BARRIER();
    STAMP(20);
    {
      u32x4 pa[8];
#pragma unroll
      for (int i = 0; i < 8; ++i) pa[i] = ldstr(ra[i] + ((i >> 1) & 1) * 512, ra[i] + ((i >> 1) & 1) * 512 + 8192);
#pragma unroll
      for (int i = 0; i < 8; ++i) fin[i & 1] = mfma16(pa[i], sel[i >> 1], fin[i & 1]);
      const u32x4 o = {pk2(fin[0][0], fin[0][1]), pk2(fin[0][2], fin[0][3]), pk2(fin[1][0], fin[1][1]), pk2(fin[1][2], fin[1][3])};
      u32x4 oc;
#pragma unroll
      for (int i = 0; i < 4; ++i) { const unsigned t_ = o[i]; oc[i] = (unsigned)__builtin_amdgcn_ds_bpermute((int)bp, (int)t_); }
      *(u32x4*)(out + ooff) = oc;
    }
    if (b == 32 && tid == 0) *(unsigned*)(ws + WS_EPOCH) = tag;
    STAMP(21);
  } else {
    const int k1 = 8 * bi + xcd;
    const int lr = l >> 3, lc = l & 7;
    const int gst = 2 * (8 * (xcd & 3) + 4 * (xcd >> 2) + bi) + (w >> 2), n1s = 8 * (w & 3) + lr;
    const int cid = 4 * (xcd & 3) + bi;
    const int ctt = 7 * (xcd >> 2) + ((cid < 14 ? cid : 0) >> 1), cmat = cid & 1;
    const rsrc_t rx = mk_rsrc(x, 2048 * 896), rc = mk_rsrc(cmat ? wu : wg, 448 * 3584), rw = mk_rsrc(ws, WS_BYTES);
    const rsrc_t rfr = mk_rsrc(wr, 921600), rfi = mk_rsrc(wi, 921600);
    LOAD_EPOCH();
    u32x4 xr[7];
    {
      const int n2 = ((gst >> 2) & 1) + 8 * (gst >> 3) + 2 * (gst & 3);
      const unsigned xo = (unsigned)((64 * n1s + n2) * 896 + lc * 16);
      sfor<0, 7>([&](auto J) { constexpr int j = decltype(J)::v; ld128<0, j * 128>(xr[j], rx, xo, 0u); });
    }
    constexpr unsigned L_RED = 28672, L_WT = 32768, L_FILT = 32768;
    const unsigned wo = (unsigned)((56 * w + lr) * 3584 + ctt * 256 + ((lc ^ (((lr >> 1) & 3) << 1)) << 4));
    const unsigned lwt = L0 + L_WT + w * 7168;
    const int c31 = l & 31, h = l >> 5;
    {
      const int s = w, par = s >> 2;
      float vc[4], vs[4];
#pragma unroll
      for (int mm = 0; mm < 4; ++mm) {
        int n2 = par + 8 * (2 * (s & 3) + h) + 2 * mm;
        float rev = (float)((n2 * (k1 + 32 * c31)) & 2047) * (1.0f / 2048.0f);
        vc[mm] = __builtin_amdgcn_cosf(rev); vs[mm] = __builtin_amdgcn_sinf(rev);
      }
      *(LAS u32x4*)(uintptr_t)(L0 + (2 * s) * 1024 + l * 16) = u32x4{pk2(vc[0], vs[0]), pk2(vc[1], vs[1]), pk2(vc[2], vs[2]), pk2(vc[3], vs[3])};
      *(LAS u32x4*)(uintptr_t)(L0 + (2 * s + 1) * 1024 + l * 16) = u32x4{pk2(-vs[0], vc[0]), pk2(-vs[1], vc[1]), pk2(-vs[2], vc[2]), pk2(-vs[3], vc[3])};
    }
    {
      const int par = (w >> 1) & 1, s2 = w & 1;
      float v[8];
#pragma unroll
      for (int e = 0; e < 8; ++e) {
        int k2 = 16 * s2 + 8 * (e >> 2) + 4 * h + (e & 3);
        v[e] = __builtin_amdgcn_cosf((float)(((par + 2 * c31) * (k1 + 32 * k2)) & 2047) * (1.0f / 2048.0f) - (w >= 4 ? 0.25f : 0.0f)) * (2.0f / (2048.0f * 448.0f));
      }
      const u32x4 pk = {pk2(v[0], v[1]), pk2(v[2], v[3]), pk2(v[4], v[5]), pk2(v[6], v[7])};
      const unsigned a = L0 + 16384 + (w & 3) * 3072 + (w >> 2) * 1024 + l * 16;
      *(LAS u32x4*)(uintptr_t)(a) = pk;
      if (w >= 4) *(LAS u32x4*)(uintptr_t)(a + 1024) = pk ^ 0x80008000u;
    }
    SB();
    waitv<0, 0>();
    sfor<0, 3>([&](auto J) { constexpr int j = decltype(J)::v; dma_add<0, j * 1024, 0>(rc, wo, lwt, (unsigned)(j * (8 * 3584))); });
    GET_TAG();
    {
      float s1 = 0.f, s2 = 0.f;
#pragma unroll
      for (int j = 0; j < 7; ++j) { PIN(xr[j]); s1 = sum8(xr[j], s1); s2 = sq8(xr[j], s2); }
      s1 += dpp<0xB1>(s1); s2 += dpp<0xB1>(s2);
      s1 += dpp<0x4E>(s1); s2 += dpp<0x4E>(s2);
      s1 += dpp<0x141>(s1); s2 += dpp<0x141>(s2);
      const float mean = s1 * (1.0f / 448.0f);
      float var = __builtin_fmaf(-mean, mean, s2 * (1.0f / 448.0f));
      var = var > 0.f ? var : 0.f;
      const float rstd = __builtin_amdgcn_rsqf(var + 1e-5f);
      st128_sc1<0, 0>(rw, lc ? OOB : (unsigned)(WS_STAT + (gst * 32 + n1s) * 16), 0u, u32x4{asu(mean), tag, asu(rstd), tag});
    }
    STAMP(1);
    sfor<3, 7>([&](auto J) { constexpr int j = decltype(J)::v; dma_add<0, j * 1024, 0>(rc, wo, lwt, (unsigned)(j * (8 * 3584))); });
    const unsigned fo = (((unsigned)((k1 + 32 * (8 * w + h)) * 450)) & ~15u) + c31 * 16, lfb = L0 + L_FILT + w * 4096;
    const u32x4 ones = {0x3f803f80u, 0x3f803f80u, 0x3f803f80u, 0x3f803f80u};
    const unsigned csa = L0 + L_WT + (4 * q + (r >> 2)) * 128 + ((((4 * q + (r >> 2)) >> 1) & 3) << 5) + (r & 3) * 8;
    const unsigned cso = cid < 14 ? (unsigned)(WS_CS + (ctt * 4 + cmat) * 512) : OOB;
    auto colsum = [&](unsigned base, int ps) {
      const unsigned a = (csa ^ ((w & 3) << 5)) + (w >> 2) * 28672 + base;
      f32x4 sm = {0, 0, 0, 0};
#pragma unroll
      for (int j = 0; j < 7; ++j) sm = mfma16(ldstr(a + j * 4096, a + j * 4096 + 2048), ones, sm);
      if (r == 0) *(LAS f32x4*)(uintptr_t)(L0 + L_RED + ps * 512 + w * 64 + q * 16) = sm;
      BARRIER();
      if (w == 0) {
        const float t = *(LAS const float*)(uintptr_t)(L0 + L_RED + ps * 512 + l * 4) + *(LAS const float*)(uintptr_t)(L0 + L_RED + ps * 512 + 256 + l * 4);
        st64_sc1<0>(rw, cso + ps * 1024 + l * 8, 0u, u32x2{asu(t), tag});
      }
    };
    waitv<0, 0>();
    sfor<0, 4>([&](auto J) { constexpr int j = decltype(J)::v; dma_add<0, 65536 + j * 1024, 128>(rc, wo, lwt, (unsigned)(j * (8 * 3584))); });
    BARRIER();
    colsum(0, 0);
    STAMP(2);
    sfor<4, 7>([&](auto J) { constexpr int j = decltype(J)::v; dma_add<0, 65536 + j * 1024, 128>(rc, wo, lwt, (unsigned)(j * (8 * 3584))); });
    u32x4 fm[16];
#pragma unroll
    for (int i = 0; i < 16; ++i) fm[i] = lds128(L0 + i * 1024 + l * 16);
#pragma unroll
    for (int i = 0; i < 16; ++i) PIN(fm[i]);
    waitv<0, 0>();
    sfor<0, 4>([&](auto I) { constexpr int i = decltype(I)::v; dma_add<0, i * 1024, 0>(rfr, fo, lfb, (unsigned)(i * 28800)); });
    BARRIER();
    colsum(65536, 1);
    sfor<0, 4>([&](auto I) { constexpr int i = decltype(I)::v; dma_add<0, 32768 + i * 1024, 0>(rfi, fo, lfb, (unsigned)(i * 28800)); });
    waitv<0, 0>();
    BARRIER();
    STAMP(3);
    const int c = 28 * w + (c31 < 28 ? c31 : 27);
    f32x2 wpr[8], wpi[8], wmr[8], wmi[8];
    {
      const unsigned fa0 = L0 + L_FILT + 4 * h * 512 + ((2 * k1) & 15) + (c ? c : 224) * 2;
      unsigned short t[4][16];
#pragma unroll
      for (int i = 0; i < 16; ++i)
#pragma unroll
        for (int j = 0; j < 4; ++j) t[j][i] = *(LAS const unsigned short*)(uintptr_t)(fa0 + ((i & 3) + 8 * (i >> 2)) * 512 + j * 16384);
#pragma unroll
      for (int i = 0; i < 8; ++i) {
        const f32x2 p0 = {bflo(t[0][2 * i]), bflo(t[0][2 * i + 1])}, p1 = {bflo(t[1][2 * i]), bflo(t[1][2 * i + 1])};
        const f32x2 q0 = {bflo(t[2][2 * i]), bflo(t[2][2 * i + 1])}, q1 = {bflo(t[3][2 * i]), bflo(t[3][2 * i + 1])};
        wpr[i] = p0 + p1; wpi[i] = q0 + q1; wmr[i] = p0 - p1; wmi[i] = q0 - q1;
      }
      if (w == 0) {
        const float sc = c ? 1.0f : 0.5f;
#pragma unroll
        for (int i = 0; i < 8; ++i) { wpr[i] *= sc; wpi[i] *= sc; wmr[i] *= sc; wmi[i] *= sc; }
      }
#pragma unroll
      for (int i = 0; i < 8; ++i) { PIN(wpr[i]); PIN(wpi[i]); PIN(wmr[i]); PIN(wmi[i]); }
    }
    const unsigned zst = L0 + 98304 + w * 4096;
    unsigned zw[4], zr = zst + lr * 128 + ((lc ^ lr) << 4);
    unsigned zg = (unsigned)((8 * (lr >> 2) + (lr & 3)) * 32768 + lc * 16), ma = L0 + 16384 + l * 16;
    unsigned evo = (unsigned)(WS_E + k1 * 65536 + h * 8192 + c * 16), eco = (unsigned)(WS_EC + (28 * h + (c >> 4)) * 2048 + k1 * 64 + (c & 15) * 4);
#pragma unroll
    for (int i = 0; i < 4; ++i) {
      zw[i] = zst + c31 * 128 + (((4 * (i >> 1) + 2 * h + (i & 1)) ^ (c31 & 7)) << 4);
      PIN(zw[i]);
    }
    PIN(zr); PIN(zg); PIN(ma); PIN(evo); PIN(eco);
    STAMP(4);
    if (w == 0) poll_flags(rw, (unsigned)(WS_HINT + k1 * 128), tag);
    asm volatile("s_barrier" ::: "memory");
    {
      u32x4 ef[8];
      unsigned ec[8];
      STAMP(5);
      sfor<0, 8>([&](auto S) {
        constexpr int s = decltype(S)::v;
        ld128_sc1<0, 0>(ef[s], rw, evo, (unsigned)(((s >> 2) + 4 * (s & 3)) * 4096));
        ld32s_sc1<0, 0>(ec[s], rw, eco, (unsigned)(((s >> 2) + 4 * (s & 3)) * 28672));
      });
      f32x16 ac[2];
      f32x2 pr[2][8], pi[2][8];
      u32x4 mi[12];
      auto pair = [&](const f32x16& v, int i) { return f32x2{v[2 * i], v[2 * i + 1]}; };
      sfor<0, 2>([&](auto HF) {
        constexpr int hf = decltype(HF)::v;
#pragma unroll
        for (int i = 0; i < 16; ++i) { ac[0][i] = 0.f; ac[1][i] = 0.f; }
        sfor<0, 4>([&](auto S) {
          constexpr int s = 4 * hf + decltype(S)::v;
          waitv<14 - 2 * s, 0>();
          PIN(ef[s]); PIN(ec[s]);
          {
            if (__builtin_expect(mism((tag + ef[s].x + ef[s].y) + ef[s].z + ef[s].w, ec[s]) != 0, 0))
              reload_e(ef[s], ec[s], rw, evo, eco, (unsigned)(((s >> 2) + 4 * (s & 3)) * 4096), tag);
          }
          ac[0] = mfma32(fm[2 * s], ef[s], ac[0]);
          ac[1] = mfma32(fm[2 * s + 1], ef[s], ac[1]);
          SB();
        });
        if constexpr (hf == 1) {
#pragma unroll
          for (int i = 0; i < 12; ++i) mi[i] = lds128(ma + i * 1024);
          st32_sc1<0>(rw, tid >= 480 ? (unsigned)(WS_FLAGZ + k1 * 128 + (tid - 480) * 4) : OOB, 0u, tag);
          STAMP(6);
        }
#pragma unroll
        for (int par = 0; par < 2; ++par) {
          const f32x2* ur = (par ^ hf) ? wmr : wpr;
          const f32x2* ui = (par ^ hf) ? wmi : wpi;
#pragma unroll
          for (int i = 0; i < 8; ++i) {
            if (hf == 0) { pr[par][i] = pair(ac[0], i) * ur[i]; pi[par][i] = pair(ac[0], i) * ui[i]; }
            else { pr[par][i] += pair(ac[0], i) * ur[i]; pi[par][i] += pair(ac[0], i) * ui[i]; }
          }
          SB();
#pragma unroll
          for (int i = 0; i < 8; ++i) { pr[par][i] -= pair(ac[1], i) * ui[i]; pi[par][i] += pair(ac[1], i) * ur[i]; }
          SB();
          if constexpr (hf == 1) {
            f32x16 zre, zim;
#pragma unroll
            for (int i = 0; i < 16; ++i) { zre[i] = 0.f; zim[i] = 0.f; }
#pragma unroll
            for (int s2 = 0; s2 < 2; ++s2) {
              const f32x2 *a_ = pr[par] + 4 * s2, *b_ = pi[par] + 4 * s2;
              const u32x4 fr = {pk2(a_[0][0], a_[0][1]), pk2(a_[1][0], a_[1][1]), pk2(a_[2][0], a_[2][1]), pk2(a_[3][0], a_[3][1])};
              const u32x4 fi = {pk2(b_[0][0], b_[0][1]), pk2(b_[1][0], b_[1][1]), pk2(b_[2][0], b_[2][1]), pk2(b_[3][0], b_[3][1])};
              const u32x4* mm = mi + (par * 2 + s2) * 3;
              zre = mfma32(fr, mm[0], zre);
              zim = mfma32(fr, mm[1], zim);
              zre = mfma32(fi, mm[2], zre);
              zim = mfma32(fi, mm[0], zim);
            }
            SB();
            u32x2 zp[2][4];
#pragma unroll
            for (int g4 = 0; g4 < 4; ++g4) {
              zp[0][g4] = u32x2{pk2(zre[4 * g4], zre[4 * g4 + 1]), pk2(zre[4 * g4 + 2], zre[4 * g4 + 3])};
              zp[1][g4] = u32x2{pk2(zim[4 * g4], zim[4 * g4 + 1]), pk2(zim[4 * g4 + 2], zim[4 * g4 + 3])};
              if (g4 < 3) {
                *(LAS u32x2*)(uintptr_t)(zw[g4 >> 1] + (g4 & 1) * 8) = zp[0][g4];
                *(LAS u32x2*)(uintptr_t)(zw[2 + (g4 >> 1)] + (g4 & 1) * 8) = zp[1][g4];
              }
            }
            {
              unsigned t3 = (zp[0][3].x + zp[0][3].y + zp[1][3].x) + zp[1][3].y;
              unsigned sm = csum8(h ? 0u : t3, cat(zp[0][0], zp[0][1]), cat(zp[1][0], zp[1][1]));
              sm = (sm + zp[0][2].x + zp[0][2].y) + zp[1][2].x + zp[1][2].y;
              auto sw = __builtin_amdgcn_permlane32_swap(sm, sm, false, false);
              const unsigned a0 = sw[0], a1 = sw[1], V = tag - a0 - a1, V2 = (V & 0x40004000u) >> 1;
              *(LAS u32x2*)(uintptr_t)(zw[1] + 8) = h ? u32x2{V & ~0x40004000u, V2} : zp[0][3];
              *(LAS u32x2*)(uintptr_t)(zw[3] + 8) = h ? u32x2{V2, 0u} : zp[1][3];
            }
            u32x4 v[4];
#pragma unroll
            for (int i = 0; i < 4; ++i) v[i] = lds128(zr + i * 1024);
            const unsigned zb = (unsigned)(WS_Z + par * 4 * 32768 + w * 4096 + k1 * 128);
#pragma unroll
            for (int i = 0; i < 4; ++i) st128_sc1<0, 0>(rw, zg, zb + i * (16 * 32768), v[i]);
            SB();
          }
        }
      });
      STAMP(7);
    }
  }
}

extern "C" void kernel_launch(void* const* d_in, const int* in_sizes, int n_in, void* d_out, int out_size, void* d_ws,
                              size_t ws_size, hipStream_t stream) {
  (void)in_sizes; (void)n_in; (void)out_size; (void)ws_size;
  (void)hipFuncSetAttribute((const void*)gf_glu_kernel, hipFuncAttributeMaxDynamicSharedMemorySize, L_TOTAL);
  gf_glu_kernel<<<256, 512, L_TOTAL, stream>>>((const char*)d_in[0], (const char*)d_in[1], (const char*)d_in[2],
                                               (const char*)d_in[4], (const char*)d_in[5], (const char*)d_in[6],
                                               (char*)d_out, (char*)d_ws);
}
```

```cpp
#include <hip/hip_runtime.h>
#include <hip/hip_bf16.h>
#include <stdint.h>

typedef __bf16 bf16x8 __attribute__((ext_vector_type(8)));
typedef __bf16 bf16x2 __attribute__((ext_vector_type(2)));
typedef short s16x4 __attribute__((ext_vector_type(4)));
typedef float f32x2 __attribute__((ext_vector_type(2)));
typedef float f32x4 __attribute__((ext_vector_type(4)));
typedef float f32x16 __attribute__((ext_vector_type(16)));
typedef unsigned u32x4 __attribute__((ext_vector_type(4)));
typedef unsigned u32x2 __attribute__((ext_vector_type(2)));
#define LAS __attribute__((address_space(3)))
#define DI __device__ __forceinline__

constexpr unsigned WS_EPOCH = 0;
constexpr unsigned WS_STAT = 65536;
constexpr unsigned WS_CS = 131072;
constexpr unsigned WS_FLAGZ = 196608;
constexpr unsigned WS_HINT = 229376;
constexpr unsigned WS_EC = 524288;
constexpr unsigned WS_E = 1u << 20;
constexpr unsigned WS_Z = 3u << 20;
constexpr unsigned WS_BYTES = 14u << 20;
constexpr unsigned WS_H = 0x520000;
constexpr unsigned HBLK = 4608;
constexpr unsigned E_PN = 3584, E_K1 = 16 * 3584 + 256, Z_G = 32768 + 640;

constexpr unsigned L_AUX = 131072;
constexpr unsigned L_PRIV = L_AUX;
constexpr unsigned L_ZOK = L_AUX + 3072;
constexpr unsigned L_EB = L_AUX + 3584;
constexpr unsigned L_PAUX = L_AUX + 12288;
constexpr unsigned L_P1 = L_AUX + 28672;
constexpr unsigned L_TOTAL = 163840;

constexpr unsigned SPIN_CAP = 1u << 17;

template <int N> struct IC { static constexpr int v = N; };
template <int B, int E, class Fn> DI void sfor(Fn&& f) {
  if constexpr (B < E) { f(IC<B>{}); sfor<B + 1, E>(f); }
}

DI unsigned pk2(float a, float b) { f32x2 t = {a, b}; bf16x2 p = __builtin_convertvector(t, bf16x2); return __builtin_bit_cast(unsigned, p); }
DI bf16x8 asbf(u32x4 v) { return __builtin_bit_cast(bf16x8, v); }
DI float asf(unsigned u) { return __builtin_bit_cast(float, u); }
DI unsigned asu(float f) { return __builtin_bit_cast(unsigned, f); }
DI float bflo(unsigned u) { return asf(u << 16); }
DI float bfhi(unsigned u) { return asf(u & 0xffff0000u); }
DI f32x4 mfma16(u32x4 a, u32x4 b, f32x4 c) { return __builtin_amdgcn_mfma_f32_16x16x32_bf16(asbf(a), asbf(b), c, 0, 0, 0); }
DI f32x16 mfma32(u32x4 a, u32x4 b, f32x16 c) { return __builtin_amdgcn_mfma_f32_32x32x16_bf16(asbf(a), asbf(b), c, 0, 0, 0); }
DI void mma(f32x4& c, const u32x4& a, const u32x4& b) { asm volatile("v_mfma_f32_16x16x32_bf16 %0, %1, %2, %0" : "+v"(c) : "v"(a), "v"(b)); }
DI void mma0(f32x4& c, const u32x4& a, const u32x4& b) { asm volatile("v_mfma_f32_16x16x32_bf16 %0, %1, %2, 0" : "=&v"(c) : "v"(a), "v"(b)); }
DI void mmac(f32x4& d, const u32x4& a, const u32x4& b, const f32x4& c) { asm volatile("v_mfma_f32_16x16x32_bf16 %0, %1, %2, %3" : "=&v"(d) : "v"(a), "v"(b), "v"(c)); }
DI float dot2(unsigned a, unsigned b, float c) {
  return __builtin_amdgcn_fdot2_f32_bf16(__builtin_bit_cast(bf16x2, a), __builtin_bit_cast(bf16x2, b), c, false);
}
DI float sum8(u32x4 v, float c) { unsigned a = v.x, b = v.y, d = v.z, e = v.w; const unsigned o = 0x3f803f80u;
  return dot2(e, o, dot2(d, o, dot2(b, o, dot2(a, o, c)))); }
DI float sq8(u32x4 v, float c) { unsigned a = v.x, b = v.y, d = v.z, e = v.w;
  return dot2(e, e, dot2(d, d, dot2(b, b, dot2(a, a, c)))); }
template <int CTRL> DI float dpp(float v) { return asf((unsigned)__builtin_amdgcn_update_dpp(0, (int)asu(v), CTRL, 0xf, 0xf, true)); }
DI u32x4 lds128(unsigned a) { return *(LAS const u32x4*)(uintptr_t)a; }
DI f32x4 lds128f(unsigned a) { return *(LAS const f32x4*)(uintptr_t)a; }
DI u32x2 ldstr1(unsigned a) { return __builtin_bit_cast(u32x2, __builtin_amdgcn_ds_read_tr16_b64_v4i16((LAS s16x4*)(uintptr_t)a)); }
DI u32x4 cat(u32x2 lo, u32x2 hi) { return __builtin_shufflevector(lo, hi, 0, 1, 2, 3); }
DI u32x4 ldstr(unsigned a0, unsigned a1) {
  s16x4 lo = __builtin_amdgcn_ds_read_tr16_b64_v4i16((LAS s16x4*)(uintptr_t)a0);
  s16x4 hi = __builtin_amdgcn_ds_read_tr16_b64_v4i16((LAS s16x4*)(uintptr_t)a1);
  return __builtin_bit_cast(u32x4, __builtin_shufflevector(lo, hi, 0, 1, 2, 3, 4, 5, 6, 7));
}
typedef u32x4 rsrc_t;
DI rsrc_t mk_rsrc(const void* p, unsigned bytes) {
  const unsigned long long a = (unsigned long long)p;
  rsrc_t d = {(unsigned)a, (unsigned)(a >> 32) & 0xffffu, bytes, 0x00020000u};
  asm volatile("" : "+s"(d));
  return d;
}
template <int ID, int LIMM, int MUL> DI void dma_mul(rsrc_t rs, unsigned voff, unsigned lbase, unsigned k) {
  unsigned t;
  asm volatile("s_add_u32 m0, %3, %5\n\ts_mul_i32 %0, %4, %6\n\tbuffer_load_dwordx4 %1, %2, %0 offen lds ; OP %7"
               : "=&s"(t) : "v"(voff), "s"(rs), "s"(lbase), "s"(k), "i"(LIMM), "i"(MUL), "i"(ID) : "memory", "scc");
}
template <int ID, int LIMM, int ADD> DI void dma_add(rsrc_t rs, unsigned voff, unsigned lbase, unsigned k) {
  unsigned t;
  asm volatile("s_add_u32 m0, %3, %5\n\ts_add_u32 %0, %4, %6\n\tbuffer_load_dwordx4 %1, %2, %0 offen lds ; OP %7"
               : "=&s"(t) : "v"(voff), "s"(rs), "s"(lbase), "s"(k), "i"(LIMM), "i"(ADD), "i"(ID) : "memory", "scc");
}
template <int ID, int LIMM, int ADD> DI void dma_add_sc1(rsrc_t rs, unsigned voff, unsigned lbase, unsigned k) {
  unsigned t;
  asm volatile("s_add_u32 m0, %3, %5\n\ts_add_u32 %0, %4, %6\n\tbuffer_load_dwordx4 %1, %2, %0 offen sc1 lds ; OP %7"
               : "=&s"(t) : "v"(voff), "s"(rs), "s"(lbase), "s"(k), "i"(LIMM), "i"(ADD), "i"(ID) : "memory", "scc");
}
template <int ID, int IMM> DI void st128_sc1(rsrc_t rs, unsigned voff, unsigned soff, u32x4 v) {
  asm volatile("buffer_store_dwordx4 %0, %1, %2, %3 offen offset:%4 sc1 ; OP %5\n\ts_nop 1" ::"v"(v), "v"(voff), "s"(rs), "Is"(soff), "i"(IMM), "i"(ID) : "memory");
}
template <int ID> DI void st64_sc1(rsrc_t rs, unsigned voff, unsigned soff, u32x2 v) {
  asm volatile("buffer_store_dwordx2 %0, %1, %2, %3 offen sc1 ; OP %4" ::"v"(v), "v"(voff), "s"(rs), "Is"(soff), "i"(ID) : "memory");
}
template <int ID> DI void st32_sc1(rsrc_t rs, unsigned voff, unsigned soff, unsigned v) {
  asm volatile("buffer_store_dword %0, %1, %2, %3 offen sc1 ; OP %4" ::"v"(v), "v"(voff), "s"(rs), "Is"(soff), "i"(ID) : "memory");
}
template <int ID, int IMM> DI void ld128_sc1(u32x4& dst, rsrc_t rs, unsigned voff, unsigned soff) {
  asm volatile("buffer_load_dwordx4 %0, %1, %2, %3 offen offset:%4 sc1 ; OP %5" : "=v"(dst) : "v"(voff), "s"(rs), "Is"(soff), "i"(IMM), "i"(ID) : "memory");
}
template <int ID, int IMM> DI void ld128(u32x4& dst, rsrc_t rs, unsigned voff, unsigned soff) {
  asm volatile("buffer_load_dwordx4 %0, %1, %2, %3 offen offset:%4 ; OP %5" : "=v"(dst) : "v"(voff), "s"(rs), "Is"(soff), "i"(IMM), "i"(ID) : "memory");
}
template <int ID, int IMM> DI void ld32s_sc1(unsigned& dst, rsrc_t rs, unsigned voff, unsigned soff) {
  asm volatile("buffer_load_dword %0, %1, %2, %3 offen offset:%4 sc1 ; OP %5" : "=v"(dst) : "v"(voff), "s"(rs), "s"(soff), "i"(IMM), "i"(ID) : "memory");
}
template <int ID> DI void ld32_plain(unsigned& dst, rsrc_t rs, unsigned voff) {
  asm volatile("buffer_load_dword %0, %1, %2, 0 offen ; OP %3" : "=v"(dst) : "v"(voff), "s"(rs), "i"(ID) : "memory");
}
template <int ID> DI void ld32_sc1(unsigned& dst, rsrc_t rs, unsigned voff) {
  asm volatile("buffer_load_dword %0, %1, %2, 0 offen sc1 ; OP %3" : "=v"(dst) : "v"(voff), "s"(rs), "i"(ID) : "memory");
}
template <int N, int NEED> DI void waitv() { asm volatile("s_waitcnt vmcnt(%0) ; NEED %1" ::"i"(N), "i"(NEED) : "memory"); }
template <int NEED> DI void need() { asm volatile("; NEED %0" ::"i"(NEED) : "memory"); }
#define BARRIER() asm volatile("s_waitcnt lgkmcnt(0)\n\ts_barrier" ::: "memory")
#define SB() __builtin_amdgcn_sched_barrier(0)
#define ONLY(g) asm volatile("; ONLY " #g ::: "memory")
DI void keep(const f32x4& a, const f32x4& b) { asm volatile("" ::"v"(a), "v"(b)); }
template <class T> DI void PINS(T& x) { asm volatile("" : "+s"(x)); }
DI float opq(float x) { asm volatile("" : "+v"(x)); return x; }
template <class T> DI void PIN(T& x) { asm volatile("" : "+v"(x)); }

DI void poll_flags(rsrc_t rs, unsigned voff, unsigned tag) {
#pragma nounroll
  for (unsigned spins = 0; spins < SPIN_CAP; ++spins) {
    unsigned got;
    asm volatile("buffer_load_dword %0, %1, %2, 0 offen sc1\n\ts_waitcnt vmcnt(0)" : "=v"(got) : "v"(voff), "s"(rs) : "memory");
    if (__all(got == tag)) break;
    __builtin_amdgcn_s_sleep(3);
  }
}
DI void poll_granules(u32x4& g, rsrc_t rs, unsigned voff, unsigned tag) {
#pragma nounroll
  for (unsigned spins = 0; spins < SPIN_CAP; ++spins) {
    __builtin_amdgcn_s_sleep(4);
    asm volatile("buffer_load_dwordx4 %0, %1, %2, 0 offen sc1\n\ts_waitcnt vmcnt(0)" : "=&v"(g) : "v"(voff), "s"(rs) : "memory");
    if (__all(g.y == tag && g.w == tag)) break;
  }
}
DI unsigned csum8(unsigned t, const u32x4& a, const u32x4& b) { return ((t + a.x + a.y) + a.z + a.w) + ((b.x + b.y + b.z) + b.w); }
DI void reload_h(u32x4* d, unsigned* ck, rsrc_t rs, unsigned vo, unsigned vc, unsigned so, unsigned tag) {
#pragma nounroll
  for (unsigned spins = 0; spins < SPIN_CAP; ++spins) {
    __builtin_amdgcn_s_sleep(8);
    asm volatile("buffer_load_dwordx4 %0, %6, %8, %9 offen sc1\n\tbuffer_load_dwordx4 %1, %6, %8, %9 offen offset:1024 sc1\n\t"
                 "buffer_load_dwordx4 %2, %6, %8, %9 offen offset:2048 sc1\n\tbuffer_load_dwordx4 %3, %6, %8, %9 offen offset:3072 sc1\n\t"
                 "buffer_load_dword %4, %7, %8, %9 offen sc1\n\tbuffer_load_dword %5, %7, %8, %9 offen offset:256 sc1\n\ts_waitcnt vmcnt(0)"
                 : "=&v"(d[0]), "=&v"(d[1]), "=&v"(d[2]), "=&v"(d[3]), "=&v"(ck[0]), "=&v"(ck[1])
                 : "v"(vo), "v"(vc), "s"(rs), "s"(so) : "memory");
    if (__all(csum8(tag, d[0], d[1]) == ck[0] && csum8(tag, d[2], d[3]) == ck[1])) break;
  }
}
DI void reload_e(u32x4& d, unsigned& ck, rsrc_t rs, unsigned vo, unsigned vc, unsigned so, unsigned tag) {
#pragma nounroll
  for (unsigned spins = 0; spins < SPIN_CAP; ++spins) {
    __builtin_amdgcn_s_sleep(10);
    asm volatile("buffer_load_dwordx4 %0, %2, %4, %5 offen sc1\n\tbuffer_load_dword %1, %3, %4, %6 offen sc1\n\ts_waitcnt vmcnt(0)"
                 : "=&v"(d), "=&v"(ck) : "v"(vo), "v"(vc), "s"(rs), "s"(so), "s"(so * 8) : "memory");
    if (__all((tag + d.x + d.y) + d.z + d.w == ck)) break;
  }
}
DI unsigned long long mism(unsigned v, unsigned tag) { unsigned long long m_ = __builtin_amdgcn_ballot_w64(v != tag); asm volatile("" : "+s"(m_)); return m_; }

#define STAMP(i)
#define RETRY(i)

constexpr int kTileOfX[7] = {0, 2, 3, 5, 6, 8, 9};
constexpr int kTileOfW[14] = {1, 4, 7, 10, 11, 12, 13, 14, 15, 16, 17, 18, 19, 20};
constexpr int kTileKind[21] = {0, 1, 0, 0, 1, 0, 0, 1, 0, 0, 1, 1, 1, 1, 2, 2, 2, 2, 2, 2, 2};
constexpr int kTileIdx[21] = {0, 0, 1, 2, 1, 3, 4, 2, 5, 6, 3, 4, 5, 6, 0, 1, 2, 3, 4, 5, 6};
constexpr int kSegX0[4] = {0, 1, 3, 5}, kSegNX[4] = {1, 2, 2, 2};
constexpr int kIssue0[5] = {5, 8, 10, 13, 16};
constexpr int tile_of_x(int j) { return kTileOfX[j]; }
constexpr int tile_of_w(int s) { return kTileOfW[s]; }
constexpr int tile_kind(int T) { return kTileKind[T]; }
constexpr int tile_idx(int T) { return kTileIdx[T]; }
constexpr unsigned piece_loc(int g, int P) {
  constexpr unsigned pr[8] = {L_PAUX, 5 * 16384, 6 * 16384, 7 * 16384, 0, 16384, 2 * 16384, 3 * 16384};
  return pr[(g ? P % 8 : P % 6)];
}
enum { OP_EP = 29, OP_P = 30, OP_GS = 50, OP_GC1, OP_GC2, OP_E, OP_H1, OP_H2, OP_POLL, OP_XRES, OP_FL, OP_OPT = 59, OP_HE = 60, OP_HL = 70, OP_Z = 80 };

__global__ __launch_bounds__(512, 2) void gf_glu_kernel(const char* x, const char* wr, const char* wi, const char* wg,
                                                        const char* wu, const char* wd, char* out, char* ws) {
  extern __shared__ __attribute__((aligned(16))) char smem[];
  STAMP(0);
  const int tid = threadIdx.x;
  const int w = __builtin_amdgcn_readfirstlane(tid >> 6);
  const int l = tid & 63, r = l & 15, q = l >> 4;
  const int b = blockIdx.x, xcd = b & 7, bi = b >> 3;
  const unsigned L0 = (unsigned)(uintptr_t)(LAS char*)smem;

  unsigned code_touch;
  {
    unsigned long long pc;
    unsigned code_bytes;
    asm volatile("s_getpc_b64 %0\ngf_code_begin:\n\ts_mov_b32 %1, gf_code_end-gf_code_begin" : "=s"(pc), "=s"(code_bytes));
    const char* pcb = (const char*)(pc & ~127ull);
    unsigned off = (unsigned)((bi * 16 + w * 2 + (l & 1)) * 128);
    off = off < code_bytes ? off : 0;
    asm volatile("global_load_dword %0, %1, %2" : "=v"(code_touch) : "v"(off), "s"(pcb) : "memory");
  }
  unsigned epoch;
#define LOAD_EPOCH() asm volatile("global_load_dword %0, %1, %2 ; OP %3" : "=v"(epoch) : "v"(0u), "s"(ws), "i"(OP_EP) : "memory")
#define GET_TAG() PIN(epoch); PIN(code_touch); const unsigned tag = (unsigned)__builtin_amdgcn_readfirstlane(epoch) * 0x9E3779B1u + 0x7F4A7C15u
  constexpr unsigned OOB = 0x40000000u;

  if (__builtin_expect(bi >= 4, 1)) {
    const int bj = bi - 4;
    const int pn = 4 * (xcd & 3) + (bj & 3), tt = 7 * (xcd >> 2) + (bj >> 2);
    int rot = (bj >> 2) + 2 * (bj & 3) + (xcd & 1);
    rot = rot >= 7 ? rot - 7 : rot;
    const int m = w & 3, nh = w >> 2;
    const int rt = 4 * xcd + (bj & 3), ct = bj >> 2;
    unsigned kx[7];
    kx[0] = (unsigned)(rot * 128);
    unsigned xoff[2], woff, doff;
    const int lr = l >> 3, lc = l & 7;
    {
      const int R = 16 * (w & 1) + lr;
      const int n2 = (pn & 1) + 8 * (pn >> 1) + 2 * (w >> 1);
      const unsigned rb = (unsigned)((64 * R + n2) * 896), cx = (unsigned)((lc ^ (lr >> 1)) << 4);
      xoff[0] = rb + cx;
      xoff[1] = rb + 8 * 64 * 896 + (cx ^ 64);
      const int swz = ((lr >> 1) & 1) | ((w & 1) << 1);
      woff = (unsigned)((8 * w + lr) * 3584 + tt * 256 + ((lc ^ (swz << 1)) << 4));
    }
    unsigned lw1 = L0 + w * 1024, lw2 = L0 + w * 2048;
    PINS(lw1); PINS(lw2); PINS(kx[0]); PIN(xoff[0]); PIN(xoff[1]); PIN(woff);
    SB();
    const rsrc_t rx = mk_rsrc(x, 2048 * 896), rg = mk_rsrc(wg, 448 * 3584), ru = mk_rsrc(wu, 448 * 3584);
    auto issue1 = [&](auto TI, auto HI) {
      constexpr int T = decltype(TI)::v, h = decltype(HI)::v;
      constexpr int kind = tile_kind(T), sl = (T & 7) * 16384;
      if constexpr (T == 14 && h == 0) { woff += 128; PIN(woff); }
      if constexpr (kind == 0) dma_mul<T, sl + h * 1024, 1>(rx, xoff[h], lw2, kx[tile_idx(T)]);
      else dma_mul<T, sl + h * 8192, 1792>(h ? ru : rg, woff, lw1, kx[tile_idx(T)]);
    };
    auto issue = [&](auto TI) { issue1(TI, IC<0>{}); issue1(TI, IC<1>{}); };
    issue(IC<0>{});
    issue(IC<1>{});
    LOAD_EPOCH();
    STAMP(1);
    SB();
#pragma unroll
    for (int j = 1; j < 7; ++j) { kx[j] = kx[j - 1] == 768 ? 0 : kx[j - 1] + 128; PINS(kx[j]); }
    SB();

    {
      int c = 16 * tt + 8 * nh + (r >> 1);
      const float ph = (r & 1) ? 0.25f : 0.0f;
      c = c ? c : 224;
      const float cfl = (float)c, cf = cfl * (1.0f / 448.0f);
      const float k2 = 2.0f * __builtin_amdgcn_cosf(cf);
#pragma unroll
      for (int it = 0; it < 4; ++it) {
        const int ks = m + 4 * it;
        if (it < 3 || m < 2) {
          const unsigned kk = (m & 2) ? kx[it < 3 ? 2 * it + 1 : 0] : kx[2 * it];
          const float t = cfl * (float)(int)((kk >> 1) + 32 * (m & 1) + 8 * q);
          const float rem = __builtin_fmaf(-448.0f, __builtin_floorf(t * (1.0f / 448.0f)), t);
          const float rev = __builtin_fmaf(rem, 1.0f / 448.0f, ph);
          float v[8];
          v[0] = __builtin_amdgcn_cosf(rev);
          v[1] = __builtin_amdgcn_cosf(rev + cf);
#pragma unroll
          for (int e = 2; e < 8; ++e) v[e] = __builtin_fmaf(k2, v[e - 1], -v[e - 2]);
          *(LAS u32x4*)(uintptr_t)(L0 + L_AUX + (nh * 14 + ks) * 1024 + l * 16) =
              u32x4{pk2(v[0], v[1]), pk2(v[2], v[3]), pk2(v[4], v[5]), pk2(v[6], v[7])};
        } else {
          const int k1 = 16 * (m & 1) + r;
          float v[4];
#pragma unroll
          for (int e = 0; e < 4; ++e)
            v[e] = __builtin_amdgcn_cosf((float)((k1 * (4 * q + e)) & 31) * (1.0f / 32.0f) - (nh ? 0.25f : 0.0f));
          const unsigned a0 = pk2(v[0], v[1]), a1 = pk2(v[2], v[3]), sg = (r & 1) ? 0x80008000u : 0u;
          *(LAS u32x4*)(uintptr_t)(L0 + L_P1 + (2 * nh + (m & 1)) * 1024 + l * 16) = u32x4{a0, a1, a0 ^ sg, a1 ^ sg};
        }
      }
    }
    unsigned xa[2][2], wa[2][2];
    {
      const int R = 32 * m + r;
      xa[0][0] = L0 + R * 128 + ((q ^ ((R >> 1) & 7)) << 4);
      xa[0][1] = xa[0][0] ^ 64;
      const int qq = r >> 2, pp = r & 3;
      const int swz = ((qq >> 1) & 1) | ((q & 1) << 1);
      wa[0][0] = L0 + (8 * q + qq) * 128 + (((2 * nh) ^ swz) << 5) + pp * 8;
      wa[0][1] = wa[0][0] ^ 32;
#pragma unroll
      for (int i = 0; i < 2; ++i) { xa[1][i] = xa[0][i] + 65536; wa[1][i] = wa[0][i] + 65536; PIN(xa[1][i]); PIN(wa[1][i]); PIN(xa[0][i]); PIN(wa[0][i]); }
    }
    unsigned l16 = l * 16, aux16 = L0 + L_AUX + l * 16, fad = aux16 + nh * 14336;
    PIN(l16); PIN(aux16); PIN(fad);
    STAMP(2);
    SB();
    const rsrc_t rd = mk_rsrc(wd, 1792 * 896), rw = mk_rsrc(ws, WS_BYTES);
    const rsrc_t rtch = mk_rsrc(nh ? wd : ((m & 1) ? wi : wr), nh ? 1792 * 896 : 921600);
    const int g1 = 4 * pn + m;
    unsigned pso[9], hso[9];
    unsigned hst_d, hst_c, ecs, gs_off, gc_off, pq;
    const unsigned priv = L0 + L_PRIV + w * 256;
    auto step_setup = [&](int g, int s) {
      const int ps = s >= 3 + g;
      int v = 4 * (rot & 1) + (s < 3 ? 8 * s + w : m + (g == 0 ? 8 * (s - 3) : (s == 3 ? 24 : (s == 4 ? 4 : 4 * s - 8))));
      v = v >= 28 ? v - 28 : v;
      pso[s] = (unsigned)((128 * (v >> 1) + 64 * ps + 32 * (v & 1)) * 896);
      hso[s] = (unsigned)(WS_H + (rt * 56 + 28 * ps + v) * HBLK);
      PINS(pso[s]); PINS(hso[s]);
    };
    constexpr unsigned S_ = 16384;
    auto issue_p1 = [&](auto GI, auto PI, auto HI) {
      constexpr int P = decltype(PI)::v, h = decltype(HI)::v;
      dma_add<OP_P + P, piece_loc(decltype(GI)::v, P) + h * 1024, (16 * (P & 1) + 8 * h) * 896>(rd, doff, lw2, pso[P >> 1]);
    };

    u32x4 xf[14][2], ff[4];
    u32x2 fa_[4][2], fb_[4][2];
    f32x4 accD[2], acc[2][4][2];
    float mrow[2], r2row[2];
    u32x4 gran, ebv;
    unsigned pollv;
    u32x4 hb[4][4];
    {
      const unsigned hst = WS_H + (unsigned)((g1 >> 1) * 56 + 2 * tt + nh) * HBLK;
      hst_d = hst + (m & 1) * 2048; hst_c = hst + (m & 1) * 256;
      ecs = (unsigned)(WS_EC + (pn * 14 + tt) * 2048);
      PINS(hst_d); PINS(hst_c); PINS(ecs);
    }
    SB();
    issue(IC<2>{});
    issue(IC<4>{});
    unsigned tag, tch, tch2, tch3, tch4, tco;
    f32x4 cs[4];
    unsigned hpk[2][4];
    auto h_part = [&](auto PS, int i2, int sub) {
      constexpr int ps = decltype(PS)::v;
      f32x4 h0 = ((acc[ps][sub][i2] - mrow[i2] * cs[sub]) * r2row[i2]) * (acc[ps][sub + 2][i2] - mrow[i2] * cs[sub + 2]);
      hpk[i2][2 * sub] = pk2(h0[0], h0[1]); hpk[i2][2 * sub + 1] = pk2(h0[2], h0[3]);
      PIN(hpk[i2][2 * sub]); PIN(hpk[i2][2 * sub + 1]);
    };
    auto h_store = [&](auto PS, auto I2) {
      constexpr int ps = decltype(PS)::v, i2 = decltype(I2)::v;
      st128_sc1<OP_H1 + ps, i2 * 1024>(rw, l16, hst_d + ps * (28 * HBLK), u32x4{hpk[i2][0], hpk[i2][1], hpk[i2][2], hpk[i2][3]});
    };
    unsigned hck[4][2], l4c, hsum;
    auto h_check_store = [&](auto PS) { st32_sc1<OP_H1 + decltype(PS)::v>(rw, l4c, hst_c + decltype(PS)::v * (28 * HBLK), hsum); };
    auto load_h1 = [&](auto SI, auto II) {
      constexpr int s = decltype(SI)::v, i = decltype(II)::v;
      if constexpr (i < 4) ld128_sc1<OP_HE + s, (i & 3) * 1024>(hb[s & 3][i & 3], rw, l16, hso[s]);
      else ld32s_sc1<OP_HE + s, (i & 1) * 256>(hck[s & 3][i & 1], rw, l4c, hso[s]);
    };
    u32x4 p1c[2], p1s[2], df;
    f32x4 rD[2], tc[2], ts[2];
    float rsr[1], dv[8], ev[4];
    unsigned dpk[4], ea, eo, ebo;
    auto hop = [&](auto OI) {
      constexpr int o = decltype(OI)::v, sub = o / 36, i2 = (o / 18) & 1, i = o % 18, e = i & 3;
      if constexpr (i < 4) dv[e] = opq(__builtin_fmaf(-mrow[i2], cs[sub][e], acc[0][sub][i2][e]));
      else if constexpr (i < 8) ev[e] = opq(__builtin_fmaf(-mrow[i2], cs[sub + 2][e], acc[0][sub + 2][i2][e]));
      else if constexpr (i < 12) dv[e] = opq(dv[e] * r2row[i2]);
      else if constexpr (i < 16) dv[e] = opq(dv[e] * ev[e]);
      else { hpk[i2][2 * sub + (i & 1)] = pk2(dv[2 * (i & 1)], dv[2 * (i & 1) + 1]); PIN(hpk[i2][2 * sub + (i & 1)]); }
    };
    u32x4 gf[2][2][4];
    float g_d[4], g_f[4], g_k[4], g_t[4], g_u[4], g_c[4][3], g_s[4][3], g_cq;
    unsigned g_mk;
    unsigned g_ny;
    auto gop = [&](auto OI) {
      constexpr int o = decltype(OI)::v, hv = o / 136, a = o & 3, dt = a, i = (o % 136) >> 2;
      if constexpr (hv == 1 && (i == 0 || i == 2 || i == 4 || i == 10)) { if constexpr (o == 136) g_cq = opq(g_cq + 28.0f); }
      else if constexpr (i == 0) g_d[a] = opq((float)(64 * ct + 16 * dt + r));
      else if constexpr (i == 1) g_t[a] = opq(g_d[a] * g_cq);
      else if constexpr (i == 2) g_f[a] = opq(g_d[a] * (1.0f / 448.0f));
      else if constexpr (i == 3) g_u[a] = opq(g_t[a] * (1.0f / 448.0f));
      else if constexpr (i == 4) g_k[a] = opq(__builtin_amdgcn_cosf(g_f[a]));
      else if constexpr (i == 5) g_u[a] = opq(__builtin_floorf(g_u[a]));
      else if constexpr (i == 6) g_t[a] = opq(__builtin_fmaf(-448.0f, g_u[a], g_t[a]));
      else if constexpr (i == 7) g_t[a] = opq(g_t[a] * (-1.0f / 448.0f));
      else if constexpr (i == 8) g_u[a] = opq(g_t[a] - g_f[a]);
      else if constexpr (i == 9) g_c[a][0] = opq(__builtin_amdgcn_cosf(g_t[a]));
      else if constexpr (i == 10) g_k[a] = opq(g_k[a] + g_k[a]);
      else if constexpr (i == 11) g_s[a][0] = opq(__builtin_amdgcn_sinf(g_t[a]));
      else if constexpr (i == 12) { unsigned p_ = pk2(g_c[a][0], g_s[a][0]); if constexpr (hv == 0) p_ ^= g_ny; PIN(p_); gf[hv][0][dt][0] = p_; }
      else if constexpr (i == 13) g_c[a][1] = opq(__builtin_amdgcn_cosf(g_u[a]));
      else if constexpr (i == 14) g_s[a][1] = opq(__builtin_amdgcn_sinf(g_u[a]));
      else if constexpr (i == 15) { unsigned p_ = pk2(g_c[a][1], g_s[a][1]); PIN(p_); gf[hv][0][dt][1] = p_; }
      else {
        constexpr int e = 2 + (i - 16) / 3, j = (i - 16) % 3;
        if constexpr (j == 0) g_c[a][e % 3] = opq(__builtin_fmaf(g_k[a], g_c[a][(e - 1) % 3], -g_c[a][(e - 2) % 3]));
        else if constexpr (j == 1) g_s[a][e % 3] = opq(__builtin_fmaf(g_k[a], g_s[a][(e - 1) % 3], -g_s[a][(e - 2) % 3]));
        else { unsigned p_ = pk2(g_c[a][e % 3], g_s[a][e % 3]); if constexpr (e >= 4) p_ &= g_mk; PIN(p_); gf[hv][e >> 2][dt][e & 3] = p_; }
      }
    };

    unsigned long long bad0, bad1;
    unsigned da[3][4];
    u32x2 af[2][4][2];
    auto rda1 = [&](int g, int s, int dt, int h) { af[s & 1][dt][h] = ldstr1(da[piece_loc(g, 2 * s + h) >> 16][dt] + (piece_loc(g, 2 * s + h) & 0xffff)); };
    auto pin_af = [&](int s) {
#pragma unroll
      for (int dt = 0; dt < 4; ++dt) { PIN(af[s & 1][dt][0]); PIN(af[s & 1][dt][1]); }
    };

    u32x4 ph[3][2], pc, psn;
    unsigned pz_off, zo, za[4], zva, ooff, hs[2];
    const int orow = tid >> 3;
    auto prep = [&](auto GI, auto SI, auto JI) {
      constexpr int g = decltype(GI)::v, s = decltype(SI)::v, j = decltype(JI)::v;
      u32x4* hfr = hb[s & 3];
      if constexpr (j == 0) { need<OP_HE + s>(); need<OP_P + 2 * s + 1>(); PIN(hfr[0]); PIN(hfr[1]); PIN(hfr[2]); PIN(hfr[3]); PIN(hck[s & 3][0]); PIN(hck[s & 3][1]); }
      if constexpr (j < 4) { rda1(g, s, j, 0); rda1(g, s, j, 1); }
      if constexpr (j >= 1 && j < 5) {
        constexpr int f = (j - 1) >> 1, o = 2 * ((j - 1) & 1);
        hs[0] = (j == 1 ? tag : hs[0]) + hfr[f][o] + hfr[f][o + 1];
        hs[1] = (j == 1 ? tag : hs[1]) + hfr[2 + f][o] + hfr[2 + f][o + 1];
        PIN(hs[0]); PIN(hs[1]);
      }
      if constexpr (j == 5) { bad0 = mism(hs[0], hck[s & 3][0]); bad1 = mism(hs[1], hck[s & 3][1]); }
      if constexpr (j == 7) { if (__builtin_expect((bad0 | bad1) != 0, 0)) { RETRY(s); reload_h(hfr, hck[s & 3], rw, l16, l4c, hso[s], tag); } }
    };

    unsigned pw0, pw1;
    auto side = [&](auto SI, auto KI) {
      constexpr int s = decltype(SI)::v, k = decltype(KI)::v;
      constexpr int G = 32 * s + k;
#define AT(ss, kk) (G == 32 * (ss) + (kk))
#define TILE(ss, kk, T, h) if constexpr (AT(ss, kk)) issue1(IC<T>{}, IC<h>{})
#define PIECE(ss, kk, P, h) if constexpr (AT(ss, kk)) issue_p1(IC<0>{}, IC<P>{}, IC<h>{})
      if constexpr (AT(0, 7)) issue(IC<3>{});
      TILE(0, 11, 5, 0); TILE(0, 14, 5, 1); TILE(0, 17, 6, 0); TILE(0, 19, 6, 1);
      if constexpr (s == 1 && k % 4 == 0) issue1(IC<7 + k / 8>{}, IC<(k / 4) & 1>{});
      if constexpr (s == 2 && k % 4 == 1) issue1(IC<10 + k / 8>{}, IC<(k >> 2) & 1>{});
      TILE(3, 3, 13, 0); TILE(3, 7, 13, 1); TILE(3, 17, 14, 0); TILE(3, 22, 14, 1);
      TILE(4, 4, 15, 0); TILE(4, 9, 15, 1); TILE(4, 15, 16, 0); TILE(5, 3, 16, 1);
      TILE(6, 10, 17, 0); TILE(6, 15, 17, 1); TILE(7, 2, 18, 0);
      TILE(7, 5, 18, 1); TILE(7, 9, 19, 0); TILE(7, 13, 19, 1); TILE(8, 1, 20, 0); TILE(8, 4, 20, 1);
      if constexpr (AT(5, 10)) { tco = (unsigned)(((bj * 8 + w) >> 4) * 64 * 896 + ((bj * 8 + w) * 28 + l) * 128); tco = l < 28 ? tco : OOB; PIN(tco); }
      if constexpr (AT(5, 12)) ld32_plain<OP_EP>(tch3, rd, tco);
      if constexpr (AT(6, 1)) { tco += 64 * 896; PIN(tco); }
      if constexpr (AT(8, 13)) ld32_plain<OP_EP>(tch4, rd, tco);
      if constexpr (AT(1, 13)) {
        const unsigned gw = (unsigned)((bj * 8 + xcd) * 4 + m);
        tco = l < (nh ? 14 : 17) ? ((nh ? gw * 14 : (gw >> 1) * 17) + l) * 128 : OOB; PIN(tco);
      }
      if constexpr (AT(3, 14)) ld32_plain<OP_EP>(tch, rtch, tco);
      PIECE(10, 5, 0, 0); PIECE(10, 12, 0, 1); PIECE(11, 0, 1, 0); PIECE(11, 3, 1, 1);
      PIECE(11, 6, 2, 0); PIECE(11, 9, 2, 1); PIECE(11, 12, 3, 0); PIECE(11, 15, 3, 1);
      if constexpr (s == 12 && k % 4 == 1) load_h1(IC<0>{}, IC<k / 4>{});
      if constexpr (AT(12, 15)) { load_h1(IC<0>{}, IC<4>{}); load_h1(IC<0>{}, IC<5>{}); }
      if constexpr (s == 13 && k % 4 == 2) load_h1(IC<1>{}, IC<k / 4>{});
      if constexpr (AT(13, 15)) { load_h1(IC<1>{}, IC<4>{}); load_h1(IC<1>{}, IC<5>{}); }
      if constexpr (AT(1, 1)) { need<OP_EP>(); PIN(epoch); PIN(code_touch); tag = (unsigned)__builtin_amdgcn_readfirstlane(epoch); PINS(tag); }
      if constexpr (AT(1, 5)) { tag = tag * 0x9E3779B1u + 0x7F4A7C15u; PINS(tag); }
      if constexpr (AT(1, 7)) { gs_off = (unsigned)(WS_STAT + (g1 * 32 + (l & 31)) * 16); PIN(gs_off); }
      if constexpr (AT(1, 10)) { pq = priv + q * 16; pw0 = priv + l * 4; PIN(pq); PIN(pw0); }
      if constexpr (AT(1, 16)) { ea = L0 + L_EB + r * 264 + m * 64 + nh * 32 + q * 8; PIN(ea); }
      if constexpr (AT(1, 19)) { eo = (unsigned)((tid >> 4) * E_K1 + pn * E_PN + (16 * tt + r) * 16); PIN(eo); }
      if constexpr (AT(1, 22)) { ebo = L0 + L_EB + (tid >> 4) * 264 + r * 4; PIN(ebo); }
      if constexpr (AT(2, 18)) { gc_off = (unsigned)(WS_CS + (tt * 4 + (q & 1)) * 512 + (32 * nh + 2 * r) * 8); PIN(gc_off); }
      if constexpr (AT(2, 15)) { pw1 = priv + (l & 31) * 8; PIN(pw1); }
      if constexpr (AT(5, 13)) *(LAS unsigned*)(uintptr_t)(L0 + L_ZOK) = 0u;
      if constexpr (AT(7, 6)) step_setup(0, 0);
      if constexpr (AT(9, 4)) step_setup(0, 1);
      if constexpr (AT(10, 2)) step_setup(0, 2);
      if constexpr (AT(6, 0)) { doff = (unsigned)(lr * 896 + ct * 128 + ((lc ^ (((lr >> 1) & 3) << 1)) << 4)); PIN(doff); }
      if constexpr (AT(6, 2)) { l4c = l * 4 + 4096; PIN(l4c); }
      if constexpr (AT(9, 2)) { const int row = 4 * q + (r >> 2); da[0][0] = L0 + w * 2048 + row * 128 + (((row >> 1) & 3) << 5) + (r & 3) * 8; PIN(da[0][0]); }
      if constexpr (AT(9, 5) || AT(9, 6) || AT(9, 8)) { constexpr int dt = k == 8 ? 3 : k - 4; da[0][dt] = da[0][0] ^ (dt << 5); PIN(da[0][dt]); }
      if constexpr (s == 9 && k >= 10 && k < 14) { da[1][k - 10] = da[0][k - 10] + 65536; da[2][k - 10] = da[0][k - 10] + 131072; PIN(da[1][k - 10]); PIN(da[2][k - 10]); }
      if constexpr (AT(12, 0)) {
        const int gg = 2 * rt + (w >> 2);
        const int n2 = ((gg >> 2) & 1) + 8 * (gg >> 3) + 2 * (gg & 3);
        ooff = (unsigned)((64 * (orow & 31) + n2) * 896 + ct * 128 + lc * 16); PIN(ooff);
      }
      if constexpr (AT(2, 22)) ld128_sc1<OP_GS, 0>(gran, rw, gs_off, 0u);
      if constexpr (AT(3, 10)) { waitv<2, OP_GS>(); PIN(gran); bad0 = mism(gran.y, tag); bad1 = mism(gran.w, tag); }
      if constexpr (AT(3, 13)) {
        if (__builtin_expect((bad0 | bad1) != 0, 0)) { RETRY(12); poll_granules(gran, rw, gs_off, tag); }
        *(LAS unsigned*)(uintptr_t)(pw0) = gran.z;
      }
      if constexpr (AT(3, 15)) {
        auto sw = __builtin_amdgcn_permlane16_swap(gran.x, gran.x, false, false);
        const unsigned a0 = sw[0], a1 = sw[1];
        mrow[0] = opq(asf(a0)); mrow[1] = opq(asf(a1));
      }
      if constexpr (AT(3, 17)) { rsr[0] = opq(asf(gran.z) * asf(gran.z)); }
      if constexpr (AT(3, 21)) {
        auto sw = __builtin_amdgcn_permlane16_swap(asu(rsr[0]), asu(rsr[0]), false, false);
        const unsigned a0 = sw[0], a1 = sw[1];
        r2row[0] = opq(asf(a0)); r2row[1] = opq(asf(a1));
      }
      if constexpr (AT(3, 16) || AT(3, 18)) rD[(k - 16) >> 1] = lds128f(pq + ((k - 16) >> 1) * 64);
      if constexpr (AT(3, 19) || AT(3, 20)) p1s[k - 19] = lds128(aux16 + (L_P1 - L_AUX) + 2048 + (k - 19) * 1024);
      if constexpr (AT(4, 0) || AT(4, 2)) p1c[k >> 1] = lds128(aux16 + (L_P1 - L_AUX) + (k >> 1) * 1024);
      if constexpr (s == 4 && k >= 1 && k < 5) {
        constexpr int e = 2 * (k - 1);
        dv[e] = opq(rD[e >> 2][e & 3] * accD[e >> 2][e & 3]);
        dv[e + 1] = opq(rD[e >> 2][(e & 3) + 1] * accD[e >> 2][(e & 3) + 1]);
      }
      if constexpr (AT(4, 5) || AT(4, 6)) {
        constexpr int e = 4 * (k - 5);
        dpk[e >> 1] = pk2(dv[e], dv[e + 1]); dpk[(e >> 1) + 1] = pk2(dv[e + 2], dv[e + 3]);
        PIN(dpk[e >> 1]); PIN(dpk[(e >> 1) + 1]);
      }
      if constexpr (AT(4, 7)) {
        df = u32x4{dpk[0], dpk[1], dpk[2], dpk[3]};
        mma0(ts[0], df, p1s[0]); mma0(tc[0], df, p1c[0]); mma0(ts[1], df, p1s[1]); mma0(tc[1], df, p1c[1]);
      }
      if constexpr (s == 4 && k >= 10 && k < 14) {
        constexpr int a = (k - 10) >> 1, e = 2 * (k & 1);
        ev[0] = opq(tc[a][e] + ts[a][e + 1]);
        ev[1] = opq(tc[a][e + 1] - ts[a][e]);
        dpk[k & 1] = pk2(ev[0], ev[1]); PIN(dpk[k & 1]);
      }
      if constexpr (AT(4, 11) || AT(4, 13)) *(LAS u32x2*)(uintptr_t)(ea + ((k - 10) >> 1) * (16 * 264)) = u32x2{dpk[0], dpk[1]};
      if constexpr (AT(5, 0)) {
        waitv<6, 13>();
        asm volatile("s_waitcnt lgkmcnt(3)\n\ts_barrier" ::: "memory");
#pragma unroll
        for (int i = 0; i < 4; ++i) ebv[i] = *(LAS const unsigned*)(uintptr_t)(ebo + i * 64);
      }
      if constexpr (AT(5, 5)) ld128_sc1<OP_GC1, 0>(gran, rw, gc_off, 0u);
      if constexpr (AT(5, 4)) { hsum = (tag + ebv.x + ebv.y) + ebv.z + ebv.w; PIN(hsum); tch2 = tid * 4; PIN(tch2); }
      if constexpr (AT(5, 7)) { st128_sc1<OP_E, 0>(rw, eo, WS_E, ebv); st32_sc1<OP_E>(rw, tch2, ecs, hsum); }
      if constexpr (AT(5, 2)) { tco = (tid >= 480 && bj >= 24) ? (unsigned)(WS_HINT + ((bj - 24) * 8 + (xcd ^ 4)) * 128 + (tid - 480) * 4) : OOB; dpk[2] = tag; PIN(tco); PIN(dpk[2]); }
      if constexpr (AT(5, 9)) st32_sc1<OP_E>(rw, tco, 0u, dpk[2]);
      if constexpr (AT(6, 6)) { waitv<4, OP_GC1>(); need<16>(); PIN(gran); bad0 = mism(gran.y, tag); bad1 = mism(gran.w, tag); }
      if constexpr (AT(6, 7)) asm volatile("s_barrier" ::: "memory");
      if constexpr (AT(12, 11)) { waitv<10, OP_GC2>(); PIN(tch); PIN(tch3); PIN(tch4); PIN(gran); bad0 = mism(gran.y, tag); bad1 = mism(gran.w, tag); }
      if constexpr (AT(6, 9) || AT(12, 14)) {
        if (__builtin_expect((bad0 | bad1) != 0, 0)) { RETRY(s == 12 ? 14 : 13); poll_granules(gran, rw, gc_off + (s == 12 ? 1024 : 0), tag); }
        *(LAS unsigned*)(uintptr_t)(pw1) = gran.x; *(LAS unsigned*)(uintptr_t)(pw1 + 4) = gran.z;
      }
      if constexpr (AT(6, 12) || AT(6, 14) || AT(7, 1) || AT(7, 3)) { constexpr int i = s == 6 ? k - 12 : k; cs[i] = lds128f(pq + i * 64); }
      if constexpr (s == 13 && k >= 3 && k < 11 && (k & 1)) cs[(k - 3) >> 1] = lds128f(pq + ((k - 3) >> 1) * 64);
      if constexpr ((s == 7 && k >= 8) || (s == 8 && k < 10)) {
        constexpr int o = 4 * (16 * (s - 7) + k - 8);
        hop(IC<o>{}); hop(IC<o + 1>{}); hop(IC<o + 2>{}); hop(IC<o + 3>{});
      }
      if constexpr (AT(10, 8)) ld128_sc1<OP_GC2, 1024>(gran, rw, gc_off, 0u);
      if constexpr (AT(8, 7)) h_store(IC<0>{}, IC<0>{});
      if constexpr (AT(8, 10)) { h_store(IC<0>{}, IC<1>{}); hsum = (tag + hpk[0][0] + hpk[0][1]) + hpk[0][2] + hpk[0][3]; PIN(hsum); }
      if constexpr (AT(8, 11)) { hsum = (hsum + hpk[1][0] + hpk[1][1]) + hpk[1][2] + hpk[1][3]; PIN(hsum); }
      if constexpr (AT(8, 12)) h_check_store(IC<0>{});
      if constexpr (AT(8, 14)) { waitv<10, 17>(); asm volatile("s_barrier" ::: "memory"); }
      if constexpr (AT(10, 7)) { waitv<5, 20>(); asm volatile("s_barrier" ::: "memory"); }
      if constexpr (AT(13, 7)) asm volatile("s_barrier" ::: "memory");
    };

    sfor<0, 14>([&](auto SI) {
      constexpr int s = decltype(SI)::v;
      constexpr int T = tile_of_w(s), sl = T & 7;
      constexpr int ps = s / 7, j = s % 7, pps = (s - 1) / 7, pj = (s + 6) % 7;
      constexpr int x0 = s < 4 ? kSegX0[s < 4 ? s : 0] : 0, nx = s < 4 ? kSegNX[s < 4 ? s : 0] : 0;
      constexpr int waitB[5] = {5, 4, 4, 5, 6}, lastT[5] = {1, 3, 7, 10, 12};
      if constexpr (s <= 4) { waitv<waitB[s <= 4 ? s : 0], lastT[s <= 4 ? s : 0]>(); BARRIER(); }
      SB();
      sfor<0, 16 + 4 * nx>([&](auto KI) {
        constexpr int k = decltype(KI)::v, i = (k & 7) >> 1, h = k & 1;
        if constexpr (k < 8) {
          if constexpr (s > 0) mma(acc[pps][i][h], cat(fb_[i][0], fb_[i][1]), xf[2 * pj + 1][h]);
        } else if constexpr (k < 16) {
          if constexpr (j == 0) mma0(acc[ps][i][h], cat(fa_[i][0], fa_[i][1]), xf[0][h]);
          else mma(acc[ps][i][h], cat(fa_[i][0], fa_[i][1]), xf[2 * j][h]);
        } else {
          if constexpr (s == 0 && k < 18) mma0(accD[h], xf[0][h], ff[0]);
          else mma(accD[h], xf[2 * x0 + i][h], ff[i]);
        }
        SB();
        constexpr int xr = s == 0 ? k : (k < 2 ? k : ((k & 1) ? -1 : 1 + k / 2));
        if constexpr (xr >= 0 && xr < 4 * nx) {
          constexpr int xt = x0 + (xr >> 2), kk = (xr >> 1) & 1, xs = tile_of_x(xt) & 7;
          xf[2 * xt + kk][xr & 1] = lds128(xa[xs >> 2][kk] + (xs & 3) * 16384 + (xr & 1) * 2048);
        }
        if constexpr (k < 16) {
          const unsigned a = wa[sl >> 2][i & 1] + (sl & 3) * 16384 + (i >> 1) * 8192 + (k >> 3) * 4096 + h * 512;
          if constexpr (k < 8) fa_[i][h] = ldstr1(a); else fb_[i][h] = ldstr1(a);
        }
        if constexpr (k >= 9 && (k & 1) && (k - 9) / 2 < 2 * nx) ff[(k - 9) / 2] = lds128(fad + (2 * x0 + (k - 9) / 2) * 1024);
        SB();
        side(SI, KI);
        SB();
      });
      if constexpr (s == 0) STAMP(3);
      if constexpr (s == 3) STAMP(4);
      if constexpr (s == 5) STAMP(5);
      if constexpr (s == 7) STAMP(6);
      if constexpr (s == 9) STAMP(7);
      if constexpr (s == 11) STAMP(8);
    });
    waitv<6, OP_HE + 0>();
    need<OP_P + 3>();
    sfor<0, 8>([&](auto KI) {
      constexpr int k = decltype(KI)::v, i = ((k >> 1) & 1) * 2 + (k >> 2), h = k & 1;
      mma(acc[1][i][h], cat(fb_[i][0], fb_[i][1]), xf[13][h]);
      SB();
      if constexpr (k < 7) prep(IC<0>{}, IC<0>{}, KI);
      SB();
    });
    STAMP(9);
    h_part(IC<1>{}, 0, 0); h_part(IC<1>{}, 1, 0);
    h_part(IC<1>{}, 0, 1); h_store(IC<1>{}, IC<0>{});
    h_part(IC<1>{}, 1, 1); h_store(IC<1>{}, IC<1>{});
    hsum = csum8(tag, u32x4{hpk[0][0], hpk[0][1], hpk[0][2], hpk[0][3]}, u32x4{hpk[1][0], hpk[1][1], hpk[1][2], hpk[1][3]});
    h_check_store(IC<1>{});
    SB();
    prep(IC<0>{}, IC<0>{}, IC<7>{});
    STAMP(10);

    f32x4 oacc[4][4];
    auto gstep = [&](auto SI, auto&& fn) {
      constexpr int s = decltype(SI)::v;
      sfor<0, 16>([&](auto KI) {
        constexpr int k = decltype(KI)::v, dt = k >> 2, rf = k & 3;
        if constexpr (s == 0) mma0(oacc[dt][rf], cat(af[0][dt][0], af[0][dt][1]), hb[0][rf]);
        else mma(oacc[dt][rf], cat(af[s & 1][dt][0], af[s & 1][dt][1]), hb[s & 3][rf]);
        SB();
        fn(KI);
        SB();
      });
    };
    auto gops = [&](auto B0, auto N) { sfor<decltype(B0)::v, (decltype(B0)::v + decltype(N)::v < 272 ? decltype(B0)::v + decltype(N)::v : 272)>([&](auto O) { gop(O); }); };
    auto step_ld = [&](auto GI, auto SI, auto NI) {
      constexpr int s = decltype(SI)::v, n = decltype(NI)::v;
      if constexpr (n == 0 && s > 2) step_setup(decltype(GI)::v, s);
      if constexpr (n < 4) issue_p1(GI, IC<2 * s + n / 2>{}, IC<n & 1>{});
      else load_h1(SI, IC<n - 4>{});
      if constexpr (n == 7) { load_h1(SI, IC<4>{}); load_h1(SI, IC<5>{}); }
    };
    f32x4 fin[2];
    u32x4 sel[4], xd, xres;
    unsigned pw[4], ra[8], bp;
    auto fin_setup = [&](auto KI) {
      constexpr int k = decltype(KI)::v;
      if constexpr (k == 0) { need<OP_XRES>(); PIN(xres); bp = (unsigned)((8 * (r & 7) + 4 * (r >> 3) + q) * 4); PIN(bp); }
      if constexpr (k >= 1 && k < 5) {
        const unsigned t_ = xres[k - 1]; xd[k - 1] = (unsigned)__builtin_amdgcn_ds_bpermute((int)bp, (int)t_);
        const unsigned o_ = r == 4 * (k - 1) + q ? 0x3f803f80u : 0u; sel[k - 1] = u32x4{o_, o_, o_, o_}; PIN(sel[k - 1]);
      }
      if constexpr (k == 5) { pw[0] = lw2 + r * 128 + ((q ^ r ^ (w & 1) ^ ((w & 2) << 2)) << 3); PIN(pw[0]); }
      if constexpr (k == 6) { pw[1] = pw[0] ^ 32; pw[2] = pw[0] ^ 64; pw[3] = pw[0] ^ 96; PIN(pw[1]); PIN(pw[2]); PIN(pw[3]); }
      if constexpr (k == 7) { bp = (unsigned)((lr + 8 * (lc >> 2) + 16 * (lc & 3)) * 4); PIN(bp); }
      if constexpr (k == 8) {
        const int qq = r >> 2, row = 8 * (w & 1) + q;
        ra[0] = L0 + (w >> 1) * 16384 + qq * 2048 + row * 128 + (((2 * (r & 3)) ^ row ^ (qq & 1) ^ ((qq & 2) << 2)) << 3); PIN(ra[0]);
      }
      if constexpr (k == 9) {
#pragma unroll
        for (int i = 1; i < 8; ++i) { ra[i] = ra[0] ^ (((i >> 1) & 1) << 5) ^ ((i >> 2) << 6) ^ ((i & 1) << 3); PIN(ra[i]); }
      }
      if constexpr (k == 10) {
#pragma unroll
        for (int i = 0; i < 4; ++i) { const unsigned t_ = xd[i]; fin[i >> 1][2 * (i & 1)] = opq(bflo(t_)); fin[i >> 1][2 * (i & 1) + 1] = opq(bfhi(t_)); }
      }
    };
    auto put = [&](int dt, int rf) {
      const f32x4 o = oacc[dt][rf];
      *(LAS u32x2*)(uintptr_t)(pw[dt] + rf * 16384) = u32x2{pk2(o[0], o[1]), pk2(o[2], o[3])};
    };
    waitv<3, OP_HE + 1>();
#pragma unroll
    for (int i = 0; i < 4; ++i) PIN(hb[1][i]);
    PIN(hck[1][0]); PIN(hck[1][1]);
    if (nh) {
      ONLY(Y);
      using G1 = IC<1>;
      gstep(IC<0>{}, [&](auto KI) {
        constexpr int k = decltype(KI)::v;
        if constexpr (k == 0 || k == 3 || k == 6) step_ld(G1{}, IC<2>{}, IC<k / 3>{});
        if constexpr (k >= 8) prep(G1{}, IC<1>{}, IC<k - 8>{});
      });
      gstep(IC<1>{}, [&](auto KI) {
        constexpr int k = decltype(KI)::v;
        if constexpr (k >= 11) step_ld(G1{}, IC<2>{}, IC<k - 8>{});
      });
      asm volatile("s_barrier" ::: "memory");
      STAMP(11);
      sfor<0, 8>([&](auto NI) { step_ld(G1{}, IC<3>{}, NI); });
      ld128<OP_XRES, 0>(xres, rx, ooff, 0u);
      waitv<11, OP_HE + 2>();
      sfor<0, 8>([&](auto JI) { prep(G1{}, IC<2>{}, JI); });
      STAMP(12);
      sfor<2, 9>([&](auto SI) {
        constexpr int s = decltype(SI)::v;
        gstep(SI, [&](auto KI) {
          constexpr int k = decltype(KI)::v;
          if constexpr (s + 2 < 9 && k < 8) step_ld(G1{}, IC<s + 2>{}, KI);
          if constexpr (s < 8 && k == 8) waitv<(s == 2 ? 10 : (s < 7 ? 10 : 0)), OP_HE + (s < 8 ? s + 1 : s)>();
          if constexpr (s < 8 && k >= 8) prep(G1{}, IC<(s < 8 ? s + 1 : s)>{}, IC<k - 8>{});
          if constexpr (s == 3 && k < 8) fin_setup(KI);
          if constexpr (s == 4 && k < 6 && !(k & 1)) fin_setup(IC<8 + k / 2>{});
          if constexpr (s == 8 && k < 3) asm volatile("s_nop 2");
          if constexpr (s == 8 && k >= 3) put((k - 3) >> 2, (k - 3) & 3);
        });
        if constexpr (s == 3) __builtin_amdgcn_s_setprio(1);
        if constexpr (s == 3) STAMP(13);
        if constexpr (s == 5) STAMP(15);
        if constexpr (s == 7) STAMP(17);
      });
      STAMP(19);
      put(3, 1); SB(); put(3, 2); SB(); put(3, 3);
      ONLY(END);
    } else {
      ONLY(O);
      using G0 = IC<0>;
      g_cq = opq((float)(56 * m + 8 * q));
      g_mk = q == 3 ? 0u : ~0u;
      g_ny = (tid < 16 && (tid & 1)) ? 0x8000u : 0u;
      auto phop = [&](auto NI) {
        constexpr int n = decltype(NI)::v, i = n >> 2, v = n & 3;
        const unsigned c_ = pc[i], s_ = psn[i];
        unsigned u0 = (v & 1) ? __builtin_amdgcn_perm(c_, s_, (v & 2) ? 0x07060302u : 0x05040100u) : __builtin_amdgcn_perm(s_, c_, (v & 2) ? 0x07060302u : 0x05040100u);
        if constexpr (v == 0) { u0 ^= 0x80000000u; PIN(u0); ph[0][0][i] = u0; }
        if constexpr (v == 1) { PIN(u0); ph[0][1][i] = u0; }
        if constexpr (v == 2) { unsigned u1 = u0 ^ 0x8000u; u0 ^= 0x80000000u; PIN(u0); PIN(u1); ph[1][0][i] = u0; ph[2][0][i] = u1; }
        if constexpr (v == 3) { unsigned u1 = u0 ^ 0x80008000u; PIN(u0); PIN(u1); ph[1][1][i] = u0; ph[2][1][i] = u1; }
      };
      gstep(IC<0>{}, [&](auto KI) {
        constexpr int k = decltype(KI)::v;
        if constexpr (k == 0 || k == 3 || k == 6) step_ld(G0{}, IC<2>{}, IC<k / 3>{});
        if constexpr (k >= 8) prep(G0{}, IC<1>{}, IC<k - 8>{});
        if constexpr (k < 8) gops(IC<4 * k>{}, IC<4>{}); else gops(IC<32 + 2 * (k - 8)>{}, IC<2>{});
      });
      gstep(IC<1>{}, [&](auto KI) {
        constexpr int k = decltype(KI)::v;
        gops(IC<48 + 4 * k>{}, IC<4>{});
        if constexpr (k >= 11) step_ld(G0{}, IC<2>{}, IC<k - 8>{});
      });
      asm volatile("s_barrier" ::: "memory");
      STAMP(11);
      sfor<0, 8>([&](auto NI) { step_ld(G0{}, IC<3>{}, NI); SB(); gops(IC<112 + 3 * decltype(NI)::v>{}, IC<3>{}); SB(); });
      pz_off = w ? OOB : (unsigned)(WS_FLAGZ + (l & 31) * 128);
      ld128<OP_XRES, 0>(xres, rx, ooff, 0u);
      ld32_sc1<OP_POLL>(pollv, rw, pz_off);
      waitv<12, OP_HE + 2>();
      sfor<0, 8>([&](auto JI) { prep(G0{}, IC<2>{}, JI); });
      STAMP(12);
      gstep(IC<2>{}, [&](auto KI) {
        constexpr int k = decltype(KI)::v;
        if constexpr (k < 8) step_ld(G0{}, IC<4>{}, KI);
        if constexpr (k == 1) { zo = (unsigned)(((l >> 1) & 7) ^ ((q & 1) | ((q & 2) << 1))); PIN(zo); }
        if constexpr (k == 2) { zo = q * 256 + (zo << 5) + (l & 1) * 16; PIN(zo); }
        if constexpr (k == 3) { const int P = 2 * q + (r >> 3); za[0] = (unsigned)(((r >> 1) & 2) ^ ((P & 1) | ((P & 2) << 1))); za[1] = P * 256 + (r & 3) * 8; PIN(za[0]); PIN(za[1]); }
        if constexpr (k == 4) { za[0] = lw2 + za[1] + (za[0] << 5); zva = lw2 + l16; PIN(za[0]); PIN(zva); }
        if constexpr (k == 5) { za[1] = za[0] ^ 128; za[2] = za[0] ^ 32; za[3] = za[0] ^ 160; PIN(za[1]); PIN(za[2]); PIN(za[3]); }
        if constexpr (k == 14) pc = lds128(aux16 + (L_P1 - L_AUX));
        if constexpr (k == 15) psn = lds128(aux16 + (L_P1 - L_AUX) + 2048);
        if constexpr (k == 8) waitv<10, OP_POLL>();
        if constexpr (k >= 8) prep(G0{}, IC<3>{}, IC<k - 8>{});
      });
      STAMP(13);
      PIN(pollv);
      unsigned zso = (unsigned)(WS_Z + 2 * rt * Z_G + 2 * m * 4096);
      if (w == 0) {
        if (mism(pollv, tag) != 0) { RETRY(9); poll_flags(rw, pz_off, tag); }
        *(volatile LAS unsigned*)(uintptr_t)(L0 + L_ZOK) = 1u;
      } else {
#pragma nounroll
        for (unsigned spins = 0; spins < 64 * SPIN_CAP; ++spins) {
          if (__builtin_amdgcn_readfirstlane(*(volatile LAS unsigned*)(uintptr_t)(L0 + L_ZOK))) break;
          __builtin_amdgcn_s_sleep(1);
        }
      }
      asm volatile("" ::: "memory");
      auto issue_z1 = [&](auto HV, auto NI, auto IDI) {
        constexpr int hv = decltype(HV)::v, zi = decltype(NI)::v >> 1, zh = decltype(NI)::v & 1;
        dma_add_sc1<decltype(IDI)::v, (4 * hv + zi) * S_ + zh * 1024, hv * 4096 + (zi >> 1) * Z_G + (zi & 1) * 2048 + zh * 1024>(rw, zo, lw2, zso);
      };
      gstep(IC<3>{}, [&](auto KI) {
        constexpr int k = decltype(KI)::v;
        if constexpr (k < 8) { issue_z1(IC<0>{}, KI, IC<OP_Z>{}); phop(IC<2 * k>{}); phop(IC<2 * k + 1>{}); }
        if constexpr (k == 8) waitv<8, OP_HE + 4>();
        if constexpr (k >= 8) prep(G0{}, IC<4>{}, IC<k - 8>{});
      });
      pin_af(4);
      gstep(IC<4>{}, [&](auto KI) {
        constexpr int k = decltype(KI)::v;
        if constexpr (!(k & 1)) issue_z1(IC<1>{}, IC<k / 2>{}, IC<OP_Z + 1>{});
        gops(IC<136 + 5 * k>{}, IC<5>{});
      });
      STAMP(15);
      sfor<0, 2>([&](auto HV) {
        constexpr int hv = decltype(HV)::v;
        waitv<hv ? 0 : 8, OP_Z + hv>();
        if constexpr (hv == 1) {
#pragma unroll
          for (int i = 0; i < 4; ++i) { za[i] += 65536; PIN(za[i]); }
          zva += 65536; PIN(zva);
        }
        u32x4 z0[2], z1[2];
        f32x4 t[6];
        unsigned yy[2][2][4];
        auto rdz = [&](int u) {
          const unsigned a_ = za[2 * (u & 1)] + (u >> 1) * 2 * S_, b_ = za[2 * (u & 1) + 1] + (u >> 1) * 2 * S_;
          z0[u & 1] = ldstr(a_, a_ + S_); z1[u & 1] = ldstr(b_, b_ + S_);
        };
        auto amma = [&](int u, int i) { if (i < 2) mma0(t[i], z0[u & 1], ph[0][i]); else mmac(t[i], z1[u & 1], ph[i >> 1][i & 1], t[i & 1]); };
        auto ypk = [&](int u, int k) { yy[u & 1][k >> 2][k & 3] = pk2(t[2 + 2 * (k >> 2)][k & 3], t[3 + 2 * (k >> 2)][k & 3]); PIN(yy[u & 1][k >> 2][k & 3]); };
#pragma nounroll
        for (unsigned spins = 0;; ++spins) {
          u32x4 zv[4];
          unsigned zs;
          rdz(0);
#pragma unroll
          for (int i = 0; i < 4; ++i) zv[i] = lds128(zva + (i >> 1) * S_ + (i & 1) * 1024);
          SB();
#pragma unroll
          for (int i = 0; i < 6; ++i) {
            amma(0, i);
            SB();
            if (i >= 1 && i < 5) { zs = ((i == 1 ? 0u : zs) + zv[i - 1].x + zv[i - 1].y) + zv[i - 1].z + zv[i - 1].w; PIN(zs); zv[i - 1] = lds128(zva + (2 + ((i - 1) >> 1)) * S_ + ((i - 1) & 1) * 1024); }
            SB();
          }
          rdz(1);
#pragma unroll
          for (int i = 0; i < 4; ++i) { zs = (zs + zv[i].x + zv[i].y) + zv[i].z + zv[i].w; PIN(zs); }
          keep(t[0], t[1]);
          SB();
#pragma unroll
          for (int k = 0; k < 8; ++k) {
            ypk(0, k);
            if (k == 1) { zs += (unsigned)__builtin_amdgcn_update_dpp(0, (int)zs, 0x128, 0xf, 0xf, true); PIN(zs); }
            if (k == 3) { zs += (unsigned)__builtin_amdgcn_update_dpp(0, (int)zs, 0x124, 0xf, 0xf, true); PIN(zs); }
            if (k == 5) { zs += (unsigned)__builtin_amdgcn_update_dpp(0, (int)zs, 0x122, 0xf, 0xf, true); PIN(zs); }
            if (k == 7) { zs += (unsigned)__builtin_amdgcn_update_dpp(0, (int)zs, 0x121, 0xf, 0xf, true); PIN(zs); }
            SB();
          }
          bad0 = mism(zs, tag * 16u);
#pragma unroll
          for (int i = 0; i < 6; ++i) amma(1, i);
          if (__builtin_expect(bad0 == 0 || spins >= SPIN_CAP, 1)) break;
          RETRY(10 + hv);
          __builtin_amdgcn_s_sleep(10);
          asm volatile("s_waitcnt lgkmcnt(0)" ::: "memory");
          sfor<0, 8>([&](auto NI) { issue_z1(HV, NI, IC<OP_OPT>{}); });
          asm volatile("s_waitcnt vmcnt(0)" ::: "memory");
        }
        sfor<0, 4>([&](auto UI) {
          constexpr int u = decltype(UI)::v;
          const u32x4 y0 = {yy[u & 1][0][0], yy[u & 1][0][1], yy[u & 1][0][2], yy[u & 1][0][3]};
          const u32x4 y1 = {yy[u & 1][1][0], yy[u & 1][1][1], yy[u & 1][1][2], yy[u & 1][1][3]};
          if (u == 1 || u == 2) {
#pragma unroll
            for (int i = 0; i < 6; ++i) amma(u + 1, i);
          } else if (u == 3) asm volatile("s_nop 1");
          sfor<0, 8>([&](auto KI) {
            constexpr int k = decltype(KI)::v;
            mma(oacc[k >> 1][2 * (u >> 1) + (k & 1)], gf[hv][u & 1][k >> 1], (k & 1) ? y1 : y0);
            SB();
            if constexpr (u < 2 && k == 0) rdz(u + 2);
            if constexpr (u == 2 && k == 0) asm volatile("s_nop 1");
            if constexpr (u < 3 && k == 3) keep(t[0], t[1]);
            if constexpr (u < 3 && k >= 4) { ypk(u + 1, 2 * (k - 4)); ypk(u + 1, 2 * (k - 4) + 1); }
            if constexpr (hv == 0 && (u == 3 || k < 4)) gops(IC<216 + 3 * (4 * u + k)>{}, IC<3>{});
            if constexpr (hv == 1 && u < 3 && k < 4 && 4 * u + k < 11) fin_setup(IC<4 * u + k>{});
            if constexpr (hv == 1 && u == 3) put(k >> 1, k & 1);
            SB();
          });
        });
        if constexpr (hv == 0) STAMP(17);
      });
      STAMP(19);
#pragma unroll
      for (int k = 0; k < 8; ++k) { put(k >> 1, 2 + (k & 1)); SB(); }
      ONLY(END);
    }
    BARRIER();
    STAMP(20);
    {
      u32x4 pa[8];
#pragma unroll
      for (int i = 0; i < 8; ++i) pa[i] = ldstr(ra[i] + ((i >> 1) & 1) * 512, ra[i] + ((i >> 1) & 1) * 512 + 8192);
#pragma unroll
      for (int i = 0; i < 8; ++i) fin[i & 1] = mfma16(pa[i], sel[i >> 1], fin[i & 1]);
      const u32x4 o = {pk2(fin[0][0], fin[0][1]), pk2(fin[0][2], fin[0][3]), pk2(fin[1][0], fin[1][1]), pk2(fin[1][2], fin[1][3])};
      u32x4 oc;
#pragma unroll
      for (int i = 0; i < 4; ++i) { const unsigned t_ = o[i]; oc[i] = (unsigned)__builtin_amdgcn_ds_bpermute((int)bp, (int)t_); }
      *(u32x4*)(out + ooff) = oc;
    }
    if (b == 32 && tid == 0) *(unsigned*)(ws + WS_EPOCH) = tag;
    STAMP(21);
  } else {
    const int k1 = 8 * bi + xcd;
    const int lr = l >> 3, lc = l & 7;
    const int gst = 2 * (8 * (xcd & 3) + 4 * (xcd >> 2) + bi) + (w >> 2), n1s = 8 * (w & 3) + lr;
    const int cid = 4 * (xcd & 3) + bi;
    const int ctt = 7 * (xcd >> 2) + ((cid < 14 ? cid : 0) >> 1), cmat = cid & 1;
    const rsrc_t rx = mk_rsrc(x, 2048 * 896), rc = mk_rsrc(cmat ? wu : wg, 448 * 3584), rw = mk_rsrc(ws, WS_BYTES);
    const rsrc_t rfr = mk_rsrc(wr, 921600), rfi = mk_rsrc(wi, 921600);
    LOAD_EPOCH();
    u32x4 xr[7];
    {
      const int n2 = ((gst >> 2) & 1) + 8 * (gst >> 3) + 2 * (gst & 3);
      const unsigned xo = (unsigned)((64 * n1s + n2) * 896 + lc * 16);
      sfor<0, 7>([&](auto J) { constexpr int j = decltype(J)::v; ld128<0, j * 128>(xr[j], rx, xo, 0u); });
    }
    constexpr unsigned L_RED = 28672, L_WT = 32768, L_FILT = 32768;
    const unsigned wo = (unsigned)((56 * w + lr) * 3584 + ctt * 256 + ((lc ^ (((lr >> 1) & 3) << 1)) << 4));
    const unsigned lwt = L0 + L_WT + w * 7168;
    const int c31 = l & 31, h = l >> 5;
    {
      const int s = w, par = s >> 2;
      float vc[4], vs[4];
#pragma unroll
      for (int mm = 0; mm < 4; ++mm) {
        int n2 = par + 8 * (2 * (s & 3) + h) + 2 * mm;
        float rev = (float)((n2 * (k1 + 32 * c31)) & 2047) * (1.0f / 2048.0f);
        vc[mm] = __builtin_amdgcn_cosf(rev); vs[mm] = __builtin_amdgcn_sinf(rev);
      }
      *(LAS u32x4*)(uintptr_t)(L0 + (2 * s) * 1024 + l * 16) = u32x4{pk2(vc[0], vs[0]), pk2(vc[1], vs[1]), pk2(vc[2], vs[2]), pk2(vc[3], vs[3])};
      *(LAS u32x4*)(uintptr_t)(L0 + (2 * s + 1) * 1024 + l * 16) = u32x4{pk2(-vs[0], vc[0]), pk2(-vs[1], vc[1]), pk2(-vs[2], vc[2]), pk2(-vs[3], vc[3])};
    }
    {
      const int par = (w >> 1) & 1, s2 = w & 1;
      float v[8];
#pragma unroll
      for (int e = 0; e < 8; ++e) {
        int k2 = 16 * s2 + 8 * (e >> 2) + 4 * h + (e & 3);
        v[e] = __builtin_amdgcn_cosf((float)(((par + 2 * c31) * (k1 + 32 * k2)) & 2047) * (1.0f / 2048.0f) - (w >= 4 ? 0.25f : 0.0f)) * (2.0f / (2048.0f * 448.0f));
      }
      const u32x4 pk = {pk2(v[0], v[1]), pk2(v[2], v[3]), pk2(v[4], v[5]), pk2(v[6], v[7])};
      const unsigned a = L0 + 16384 + (w & 3) * 3072 + (w >> 2) * 1024 + l * 16;
      *(LAS u32x4*)(uintptr_t)(a) = pk;
      if (w >= 4) *(LAS u32x4*)(uintptr_t)(a + 1024) = pk ^ 0x80008000u;
    }
    SB();
    waitv<0, 0>();
    sfor<0, 3>([&](auto J) { constexpr int j = decltype(J)::v; dma_add<0, j * 1024, 0>(rc, wo, lwt, (unsigned)(j * (8 * 3584))); });
    GET_TAG();
    {
      float s1 = 0.f, s2 = 0.f;
#pragma unroll
      for (int j = 0; j < 7; ++j) { PIN(xr[j]); s1 = sum8(xr[j], s1); s2 = sq8(xr[j], s2); }
      s1 += dpp<0xB1>(s1); s2 += dpp<0xB1>(s2);
      s1 += dpp<0x4E>(s1); s2 += dpp<0x4E>(s2);
      s1 += dpp<0x141>(s1); s2 += dpp<0x141>(s2);
      const float mean = s1 * (1.0f / 448.0f);
      float var = __builtin_fmaf(-mean, mean, s2 * (1.0f / 448.0f));
      var = var > 0.f ? var : 0.f;
      const float rstd = __builtin_amdgcn_rsqf(var + 1e-5f);
      st128_sc1<0, 0>(rw, lc ? OOB : (unsigned)(WS_STAT + (gst * 32 + n1s) * 16), 0u, u32x4{asu(mean), tag, asu(rstd), tag});
    }
    STAMP(1);
    sfor<3, 7>([&](auto J) { constexpr int j = decltype(J)::v; dma_add<0, j * 1024, 0>(rc, wo, lwt, (unsigned)(j * (8 * 3584))); });
    const unsigned fo = (((unsigned)((k1 + 32 * (8 * w + h)) * 450)) & ~15u) + c31 * 16, lfb = L0 + L_FILT + w * 4096;
    const u32x4 ones = {0x3f803f80u, 0x3f803f80u, 0x3f803f80u, 0x3f803f80u};
    const unsigned csa = L0 + L_WT + (4 * q + (r >> 2)) * 128 + ((((4 * q + (r >> 2)) >> 1) & 3) << 5) + (r & 3) * 8;
    const unsigned cso = cid < 14 ? (unsigned)(WS_CS + (ctt * 4 + cmat) * 512) : OOB;
    auto colsum = [&](unsigned base, int ps) {
      const unsigned a = (csa ^ ((w & 3) << 5)) + (w >> 2) * 28672 + base;
      f32x4 sm = {0, 0, 0, 0};
#pragma unroll
      for (int j = 0; j < 7; ++j) sm = mfma16(ldstr(a + j * 4096, a + j * 4096 + 2048), ones, sm);
      if (r == 0) *(LAS f32x4*)(uintptr_t)(L0 + L_RED + ps * 512 + w * 64 + q * 16) = sm;
      BARRIER();
      if (w == 0) {
        const float t = *(LAS const float*)(uintptr_t)(L0 + L_RED + ps * 512 + l * 4) + *(LAS const float*)(uintptr_t)(L0 + L_RED + ps * 512 + 256 + l * 4);
        st64_sc1<0>(rw, cso + ps * 1024 + l * 8, 0u, u32x2{asu(t), tag});
      }
    };
    waitv<0, 0>();
    sfor<0, 4>([&](auto J) { constexpr int j = decltype(J)::v; dma_add<0, 65536 + j * 1024, 128>(rc, wo, lwt, (unsigned)(j * (8 * 3584))); });
    BARRIER();
    colsum(0, 0);
    STAMP(2);
    sfor<4, 7>([&](auto J) { constexpr int j = decltype(J)::v; dma_add<0, 65536 + j * 1024, 128>(rc, wo, lwt, (unsigned)(j * (8 * 3584))); });
    u32x4 fm[16];
#pragma unroll
    for (int i = 0; i < 16; ++i) fm[i] = lds128(L0 + i * 1024 + l * 16);
#pragma unroll
    for (int i = 0; i < 16; ++i) PIN(fm[i]);
    waitv<0, 0>();
    sfor<0, 4>([&](auto I) { constexpr int i = decltype(I)::v; dma_add<0, i * 1024, 0>(rfr, fo, lfb, (unsigned)(i * 28800)); });
    BARRIER();
    colsum(65536, 1);
    sfor<0, 4>([&](auto I) { constexpr int i = decltype(I)::v; dma_add<0, 32768 + i * 1024, 0>(rfi, fo, lfb, (unsigned)(i * 28800)); });
    waitv<0, 0>();
    BARRIER();
    STAMP(3);
    const int c = 28 * w + (c31 < 28 ? c31 : 27);
    f32x2 wpr[8], wpi[8], wmr[8], wmi[8];
    {
      const unsigned fa0 = L0 + L_FILT + 4 * h * 512 + ((2 * k1) & 15) + (c ? c : 224) * 2;
      unsigned short t[4][16];
#pragma unroll
      for (int i = 0; i < 16; ++i)
#pragma unroll
        for (int j = 0; j < 4; ++j) t[j][i] = *(LAS const unsigned short*)(uintptr_t)(fa0 + ((i & 3) + 8 * (i >> 2)) * 512 + j * 16384);
#pragma unroll
      for (int i = 0; i < 8; ++i) {
        const f32x2 p0 = {bflo(t[0][2 * i]), bflo(t[0][2 * i + 1])}, p1 = {bflo(t[1][2 * i]), bflo(t[1][2 * i + 1])};
        const f32x2 q0 = {bflo(t[2][2 * i]), bflo(t[2][2 * i + 1])}, q1 = {bflo(t[3][2 * i]), bflo(t[3][2 * i + 1])};
        wpr[i] = p0 + p1; wpi[i] = q0 + q1; wmr[i] = p0 - p1; wmi[i] = q0 - q1;
      }
      if (w == 0) {
        const float sc = c ? 1.0f : 0.5f;
#pragma unroll
        for (int i = 0; i < 8; ++i) { wpr[i] *= sc; wpi[i] *= sc; wmr[i] *= sc; wmi[i] *= sc; }
      }
#pragma unroll
      for (int i = 0; i < 8; ++i) { PIN(wpr[i]); PIN(wpi[i]); PIN(wmr[i]); PIN(wmi[i]); }
    }
    const unsigned zst = L0 + 98304 + w * 4096;
    unsigned zw[4], zr = zst + lr * 128 + ((lc ^ lr) << 4);
    unsigned zg = (unsigned)((8 * (lr >> 2) + (lr & 3)) * Z_G + lc * 16), ma = L0 + 16384 + l * 16;
    unsigned evo = (unsigned)(WS_E + k1 * E_K1 + h * 2 * E_PN + c * 16), eco = (unsigned)(WS_EC + (28 * h + (c >> 4)) * 2048 + k1 * 64 + (c & 15) * 4);
#pragma unroll
    for (int i = 0; i < 4; ++i) {
      zw[i] = zst + c31 * 128 + (((4 * (i >> 1) + 2 * h + (i & 1)) ^ (c31 & 7)) << 4);
      PIN(zw[i]);
    }
    PIN(zr); PIN(zg); PIN(ma); PIN(evo); PIN(eco);
    STAMP(4);
    if (w == 0) poll_flags(rw, (unsigned)(WS_HINT + k1 * 128), tag);
    asm volatile("s_barrier" ::: "memory");
    {
      u32x4 ef[8];
      unsigned ec[8];
      STAMP(5);
      sfor<0, 8>([&](auto S) {
        constexpr int s = decltype(S)::v;
        ld128_sc1<0, 0>(ef[s], rw, evo, (unsigned)(((s >> 2) + 4 * (s & 3)) * E_PN));
        ld32s_sc1<0, 0>(ec[s], rw, eco, (unsigned)(((s >> 2) + 4 * (s & 3)) * 28672));
      });
      f32x16 ac[2];
      f32x2 pr[2][8], pi[2][8];
      u32x4 mi[12];
      auto pair = [&](const f32x16& v, int i) { return f32x2{v[2 * i], v[2 * i + 1]}; };
      sfor<0, 2>([&](auto HF) {
        constexpr int hf = decltype(HF)::v;
#pragma unroll
        for (int i = 0; i < 16; ++i) { ac[0][i] = 0.f; ac[1][i] = 0.f; }
        sfor<0, 4>([&](auto S) {
          constexpr int s = 4 * hf + decltype(S)::v;
          waitv<14 - 2 * s, 0>();
          PIN(ef[s]); PIN(ec[s]);
          {
            if (__builtin_expect(mism((tag + ef[s].x + ef[s].y) + ef[s].z + ef[s].w, ec[s]) != 0, 0))
            { RETRY(16 + s); reload_e(ef[s], ec[s], rw, evo, eco, (unsigned)(((s >> 2) + 4 * (s & 3)) * E_PN), tag); }
          }
          ac[0] = mfma32(fm[2 * s], ef[s], ac[0]);
          ac[1] = mfma32(fm[2 * s + 1], ef[s], ac[1]);
          SB();
        });
        if constexpr (hf == 1) {
#pragma unroll
          for (int i = 0; i < 12; ++i) mi[i] = lds128(ma + i * 1024);
          st32_sc1<0>(rw, tid >= 480 ? (unsigned)(WS_FLAGZ + k1 * 128 + (tid - 480) * 4) : OOB, 0u, tag);
          STAMP(6);
        }
#pragma unroll
        for (int par = 0; par < 2; ++par) {
          const f32x2* ur = (par ^ hf) ? wmr : wpr;
          const f32x2* ui = (par ^ hf) ? wmi : wpi;
#pragma unroll
          for (int i = 0; i < 8; ++i) {
            if (hf == 0) { pr[par][i] = pair(ac[0], i) * ur[i]; pi[par][i] = pair(ac[0], i) * ui[i]; }
            else { pr[par][i] += pair(ac[0], i) * ur[i]; pi[par][i] += pair(ac[0], i) * ui[i]; }
          }
          SB();
#pragma unroll
          for (int i = 0; i < 8; ++i) { pr[par][i] -= pair(ac[1], i) * ui[i]; pi[par][i] += pair(ac[1], i) * ur[i]; }
          SB();
          if constexpr (hf == 1) {
            f32x16 zre, zim;
#pragma unroll
            for (int i = 0; i < 16; ++i) { zre[i] = 0.f; zim[i] = 0.f; }
#pragma unroll
            for (int s2 = 0; s2 < 2; ++s2) {
              const f32x2 *a_ = pr[par] + 4 * s2, *b_ = pi[par] + 4 * s2;
              const u32x4 fr = {pk2(a_[0][0], a_[0][1]), pk2(a_[1][0], a_[1][1]), pk2(a_[2][0], a_[2][1]), pk2(a_[3][0], a_[3][1])};
              const u32x4 fi = {pk2(b_[0][0], b_[0][1]), pk2(b_[1][0], b_[1][1]), pk2(b_[2][0], b_[2][1]), pk2(b_[3][0], b_[3][1])};
              const u32x4* mm = mi + (par * 2 + s2) * 3;
              zre = mfma32(fr, mm[0], zre);
              zim = mfma32(fr, mm[1], zim);
              zre = mfma32(fi, mm[2], zre);
              zim = mfma32(fi, mm[0], zim);
            }
            SB();
            u32x2 zp[2][4];
#pragma unroll
            for (int g4 = 0; g4 < 4; ++g4) {
              zp[0][g4] = u32x2{pk2(zre[4 * g4], zre[4 * g4 + 1]), pk2(zre[4 * g4 + 2], zre[4 * g4 + 3])};
              zp[1][g4] = u32x2{pk2(zim[4 * g4], zim[4 * g4 + 1]), pk2(zim[4 * g4 + 2], zim[4 * g4 + 3])};
              if (g4 < 3) {
                *(LAS u32x2*)(uintptr_t)(zw[g4 >> 1] + (g4 & 1) * 8) = zp[0][g4];
                *(LAS u32x2*)(uintptr_t)(zw[2 + (g4 >> 1)] + (g4 & 1) * 8) = zp[1][g4];
              }
            }
            {
              unsigned t3 = (zp[0][3].x + zp[0][3].y + zp[1][3].x) + zp[1][3].y;
              unsigned sm = csum8(h ? 0u : t3, cat(zp[0][0], zp[0][1]), cat(zp[1][0], zp[1][1]));
              sm = (sm + zp[0][2].x + zp[0][2].y) + zp[1][2].x + zp[1][2].y;
              auto sw = __builtin_amdgcn_permlane32_swap(sm, sm, false, false);
              const unsigned a0 = sw[0], a1 = sw[1], V = tag - a0 - a1, V2 = (V & 0x40004000u) >> 1;
              *(LAS u32x2*)(uintptr_t)(zw[1] + 8) = h ? u32x2{V & ~0x40004000u, V2} : zp[0][3];
              *(LAS u32x2*)(uintptr_t)(zw[3] + 8) = h ? u32x2{V2, 0u} : zp[1][3];
            }
            u32x4 v[4];
#pragma unroll
            for (int i = 0; i < 4; ++i) v[i] = lds128(zr + i * 1024);
            const unsigned zb = (unsigned)(WS_Z + par * 4 * Z_G + w * 4096 + k1 * 128);
#pragma unroll
            for (int i = 0; i < 4; ++i) st128_sc1<0, 0>(rw, zg, zb + i * (16 * Z_G), v[i]);
            SB();
          }
        }
      });
      STAMP(7);
      asm volatile("gf_code_end:");
    }
  }
}

extern "C" void kernel_launch(void* const* d_in, const int* in_sizes, int n_in, void* d_out, int out_size, void* d_ws,
                              size_t ws_size, hipStream_t stream) {
  (void)in_sizes; (void)n_in; (void)out_size; (void)ws_size;
  (void)hipFuncSetAttribute((const void*)gf_glu_kernel, hipFuncAttributeMaxDynamicSharedMemorySize, L_TOTAL);
  gf_glu_kernel<<<256, 512, L_TOTAL, stream>>>((const char*)d_in[0], (const char*)d_in[1], (const char*)d_in[2],
                                               (const char*)d_in[4], (const char*)d_in[5], (const char*)d_in[6],
                                               (char*)d_out, (char*)d_ws);
}
```

```cpp
#include <hip/hip_runtime.h>
#include <hip/hip_bf16.h>
#include <stdint.h>

typedef __bf16 bf16x8 __attribute__((ext_vector_type(8)));
typedef __bf16 bf16x2 __attribute__((ext_vector_type(2)));
typedef short s16x4 __attribute__((ext_vector_type(4)));
typedef float f32x2 __attribute__((ext_vector_type(2)));
typedef float f32x4 __attribute__((ext_vector_type(4)));
typedef float f32x16 __attribute__((ext_vector_type(16)));
typedef unsigned u32x4 __attribute__((ext_vector_type(4)));
typedef unsigned u32x2 __attribute__((ext_vector_type(2)));
#define LAS __attribute__((address_space(3)))
#define DI __device__ __forceinline__

constexpr unsigned WS_EPOCH = 0;
constexpr unsigned WS_STAT = 65536;
constexpr unsigned WS_CS = 131072;
constexpr unsigned WS_FLAGZ = 196608;
constexpr unsigned WS_HINT = 229376;
constexpr unsigned WS_EC = 524288;
constexpr unsigned WS_E = 1u << 20;
constexpr unsigned WS_Z = 3u << 20;
constexpr unsigned WS_BYTES = 14u << 20;
constexpr unsigned WS_H = 0x520000;
constexpr unsigned HBLK = 4608;
constexpr unsigned E_PN = 3584, E_K1 = 16 * 3584 + 256, Z_G = 32768 + 640;

constexpr unsigned L_AUX = 131072;
constexpr unsigned L_ST = L_AUX;
constexpr unsigned L_CS = L_AUX + 1024;
constexpr unsigned L_EB = L_AUX + 14336;
constexpr unsigned L_SYNC = L_AUX + 2048;
constexpr unsigned L_PAUX = L_AUX + 12288;
constexpr unsigned L_P1 = L_AUX + 28672;
constexpr unsigned L_TOTAL = 163840;

constexpr unsigned SPIN_CAP = 1u << 17;

template <int N> struct IC { static constexpr int v = N; };
template <int B, int E, class Fn> DI void sfor(Fn&& f) {
  if constexpr (B < E) { f(IC<B>{}); sfor<B + 1, E>(f); }
}

DI unsigned pk2(float a, float b) { f32x2 t = {a, b}; bf16x2 p = __builtin_convertvector(t, bf16x2); return __builtin_bit_cast(unsigned, p); }
DI bf16x8 asbf(u32x4 v) { return __builtin_bit_cast(bf16x8, v); }
DI float asf(unsigned u) { return __builtin_bit_cast(float, u); }
DI unsigned asu(float f) { return __builtin_bit_cast(unsigned, f); }
DI float bflo(unsigned u) { return asf(u << 16); }
DI float bfhi(unsigned u) { return asf(u & 0xffff0000u); }
DI f32x4 mfma16(u32x4 a, u32x4 b, f32x4 c) { return __builtin_amdgcn_mfma_f32_16x16x32_bf16(asbf(a), asbf(b), c, 0, 0, 0); }
DI f32x16 mfma32(u32x4 a, u32x4 b, f32x16 c) { return __builtin_amdgcn_mfma_f32_32x32x16_bf16(asbf(a), asbf(b), c, 0, 0, 0); }
DI void mma(f32x4& c, const u32x4& a, const u32x4& b) { asm volatile("v_mfma_f32_16x16x32_bf16 %0, %1, %2, %0" : "+v"(c) : "v"(a), "v"(b)); }
DI void mma0(f32x4& c, const u32x4& a, const u32x4& b) { asm volatile("v_mfma_f32_16x16x32_bf16 %0, %1, %2, 0" : "=&v"(c) : "v"(a), "v"(b)); }
DI void mmac(f32x4& d, const u32x4& a, const u32x4& b, const f32x4& c) { asm volatile("v_mfma_f32_16x16x32_bf16 %0, %1, %2, %3" : "=&v"(d) : "v"(a), "v"(b), "v"(c)); }
DI float dot2(unsigned a, unsigned b, float c) {
  return __builtin_amdgcn_fdot2_f32_bf16(__builtin_bit_cast(bf16x2, a), __builtin_bit_cast(bf16x2, b), c, false);
}
DI float sum8(u32x4 v, float c) { unsigned a = v.x, b = v.y, d = v.z, e = v.w; const unsigned o = 0x3f803f80u;
  return dot2(e, o, dot2(d, o, dot2(b, o, dot2(a, o, c)))); }
DI float sq8(u32x4 v, float c) { unsigned a = v.x, b = v.y, d = v.z, e = v.w;
  return dot2(e, e, dot2(d, d, dot2(b, b, dot2(a, a, c)))); }
template <int CTRL> DI float dpp(float v) { return asf((unsigned)__builtin_amdgcn_update_dpp(0, (int)asu(v), CTRL, 0xf, 0xf, true)); }
DI u32x4 lds128(unsigned a) { return *(LAS const u32x4*)(uintptr_t)a; }
DI f32x4 lds128f(unsigned a) { return *(LAS const f32x4*)(uintptr_t)a; }
DI u32x2 ldstr1(unsigned a) { return __builtin_bit_cast(u32x2, __builtin_amdgcn_ds_read_tr16_b64_v4i16((LAS s16x4*)(uintptr_t)a)); }
DI u32x4 cat(u32x2 lo, u32x2 hi) { return __builtin_shufflevector(lo, hi, 0, 1, 2, 3); }
DI u32x4 ldstr(unsigned a0, unsigned a1) {
  s16x4 lo = __builtin_amdgcn_ds_read_tr16_b64_v4i16((LAS s16x4*)(uintptr_t)a0);
  s16x4 hi = __builtin_amdgcn_ds_read_tr16_b64_v4i16((LAS s16x4*)(uintptr_t)a1);
  return __builtin_bit_cast(u32x4, __builtin_shufflevector(lo, hi, 0, 1, 2, 3, 4, 5, 6, 7));
}
typedef u32x4 rsrc_t;
DI rsrc_t mk_rsrc(const void* p, unsigned bytes) {
  const unsigned long long a = (unsigned long long)p;
  rsrc_t d = {(unsigned)a, (unsigned)(a >> 32) & 0xffffu, bytes, 0x00020000u};
  asm volatile("" : "+s"(d));
  return d;
}
template <int ID, int LIMM, int MUL> DI void dma_mul(rsrc_t rs, unsigned voff, unsigned lbase, unsigned k) {
  unsigned t;
  asm volatile("s_add_u32 m0, %3, %5\n\ts_mul_i32 %0, %4, %6\n\tbuffer_load_dwordx4 %1, %2, %0 offen lds ; OP %7"
               : "=&s"(t) : "v"(voff), "s"(rs), "s"(lbase), "s"(k), "i"(LIMM), "i"(MUL), "i"(ID) : "memory", "scc");
}
template <int ID, int LIMM, int ADD> DI void dma_add(rsrc_t rs, unsigned voff, unsigned lbase, unsigned k) {
  unsigned t;
  asm volatile("s_add_u32 m0, %3, %5\n\ts_add_u32 %0, %4, %6\n\tbuffer_load_dwordx4 %1, %2, %0 offen lds ; OP %7"
               : "=&s"(t) : "v"(voff), "s"(rs), "s"(lbase), "s"(k), "i"(LIMM), "i"(ADD), "i"(ID) : "memory", "scc");
}
template <int ID, int LIMM, int ADD> DI void dma_add_sc1(rsrc_t rs, unsigned voff, unsigned lbase, unsigned k) {
  unsigned t;
  asm volatile("s_add_u32 m0, %3, %5\n\ts_add_u32 %0, %4, %6\n\tbuffer_load_dwordx4 %1, %2, %0 offen sc1 lds ; OP %7"
               : "=&s"(t) : "v"(voff), "s"(rs), "s"(lbase), "s"(k), "i"(LIMM), "i"(ADD), "i"(ID) : "memory", "scc");
}
constexpr unsigned DBIAS = 4096;
template <int LIMM, int MUL> DI unsigned dma_setup(unsigned lbase, unsigned k) {
  unsigned t;
  asm volatile("s_add_u32 m0, %1, %3\n\ts_mul_i32 %0, %2, %4" : "=&s"(t) : "s"(lbase), "s"(k), "i"(LIMM), "i"(MUL) : "memory", "scc");
  return t;
}
template <int ID, int IMM> DI void dma_ld(rsrc_t rs, unsigned voff, unsigned so) {
  asm volatile("buffer_load_dwordx4 %0, %1, %2 offen offset:%3 lds ; OP %4" ::"v"(voff), "s"(rs), "s"(so), "i"(IMM), "i"(ID) : "memory");
}
template <int ID, int IMM> DI void dma_ld_sc1(rsrc_t rs, unsigned voff, unsigned so) {
  asm volatile("buffer_load_dwordx4 %0, %1, %2 offen offset:%3 sc1 lds ; OP %4" ::"v"(voff), "s"(rs), "s"(so), "i"(IMM), "i"(ID) : "memory");
}
template <int ID, int IMM> DI void st128_sc1(rsrc_t rs, unsigned voff, unsigned soff, u32x4 v) {
  asm volatile("buffer_store_dwordx4 %0, %1, %2, %3 offen offset:%4 sc1 ; OP %5\n\ts_nop 1" ::"v"(v), "v"(voff), "s"(rs), "Is"(soff), "i"(IMM), "i"(ID) : "memory");
}
template <int ID> DI void st64_sc1(rsrc_t rs, unsigned voff, unsigned soff, u32x2 v) {
  asm volatile("buffer_store_dwordx2 %0, %1, %2, %3 offen sc1 ; OP %4" ::"v"(v), "v"(voff), "s"(rs), "Is"(soff), "i"(ID) : "memory");
}
template <int ID> DI void st32_sc1(rsrc_t rs, unsigned voff, unsigned soff, unsigned v) {
  asm volatile("buffer_store_dword %0, %1, %2, %3 offen sc1 ; OP %4" ::"v"(v), "v"(voff), "s"(rs), "Is"(soff), "i"(ID) : "memory");
}
template <int ID, int IMM> DI void ld128_sc1(u32x4& dst, rsrc_t rs, unsigned voff, unsigned soff) {
  asm volatile("buffer_load_dwordx4 %0, %1, %2, %3 offen offset:%4 sc1 ; OP %5" : "=v"(dst) : "v"(voff), "s"(rs), "Is"(soff), "i"(IMM), "i"(ID) : "memory");
}
template <int ID, int IMM> DI void ld128(u32x4& dst, rsrc_t rs, unsigned voff, unsigned soff) {
  asm volatile("buffer_load_dwordx4 %0, %1, %2, %3 offen offset:%4 ; OP %5" : "=v"(dst) : "v"(voff), "s"(rs), "Is"(soff), "i"(IMM), "i"(ID) : "memory");
}
template <int ID, int IMM> DI void ld32s_sc1(unsigned& dst, rsrc_t rs, unsigned voff, unsigned soff) {
  asm volatile("buffer_load_dword %0, %1, %2, %3 offen offset:%4 sc1 ; OP %5" : "=v"(dst) : "v"(voff), "s"(rs), "s"(soff), "i"(IMM), "i"(ID) : "memory");
}
template <int ID> DI void ld32_plain(unsigned& dst, rsrc_t rs, unsigned voff) {
  asm volatile("buffer_load_dword %0, %1, %2, 0 offen ; OP %3" : "=v"(dst) : "v"(voff), "s"(rs), "i"(ID) : "memory");
}
template <int ID> DI void ld32_sc1(unsigned& dst, rsrc_t rs, unsigned voff) {
  asm volatile("buffer_load_dword %0, %1, %2, 0 offen sc1 ; OP %3" : "=v"(dst) : "v"(voff), "s"(rs), "i"(ID) : "memory");
}
template <int N, int NEED> DI void waitv() { asm volatile("s_waitcnt vmcnt(%0) ; NEED %1" ::"i"(N), "i"(NEED) : "memory"); }
template <int NEED> DI void need() { asm volatile("; NEED %0" ::"i"(NEED) : "memory"); }
#define BARRIER() asm volatile("s_waitcnt lgkmcnt(0)\n\ts_barrier" ::: "memory")
#define SB() __builtin_amdgcn_sched_barrier(0)
#define ONLY(g) asm volatile("; ONLY " #g ::: "memory")
DI void keep(const f32x4& a, const f32x4& b) { asm volatile("" ::"v"(a), "v"(b)); }
template <class T> DI void PINS(T& x) { asm volatile("" : "+s"(x)); }
DI float opq(float x) { asm volatile("" : "+v"(x)); return x; }
template <class T> DI void PIN(T& x) { asm volatile("" : "+v"(x)); }

DI void poll_flags(rsrc_t rs, unsigned voff, unsigned tag) {
#pragma nounroll
  for (unsigned spins = 0; spins < SPIN_CAP; ++spins) {
    unsigned got;
    asm volatile("buffer_load_dword %0, %1, %2, 0 offen sc1\n\ts_waitcnt vmcnt(0)" : "=v"(got) : "v"(voff), "s"(rs) : "memory");
    if (__all(got == tag)) break;
    __builtin_amdgcn_s_sleep(3);
  }
}
DI void poll_granules(u32x4& g, rsrc_t rs, unsigned voff, unsigned tag) {
#pragma nounroll
  for (unsigned spins = 0; spins < SPIN_CAP; ++spins) {
    __builtin_amdgcn_s_sleep(4);
    asm volatile("buffer_load_dwordx4 %0, %1, %2, 0 offen sc1\n\ts_waitcnt vmcnt(0)" : "=&v"(g) : "v"(voff), "s"(rs) : "memory");
    if (__all(g.y == tag && g.w == tag)) break;
  }
}
DI unsigned csum8(unsigned t, const u32x4& a, const u32x4& b) { return ((t + a.x + a.y) + a.z + a.w) + ((b.x + b.y + b.z) + b.w); }
DI void reload_h(u32x4* d, unsigned* ck, rsrc_t rs, unsigned vo, unsigned vc, unsigned so, unsigned tag) {
#pragma nounroll
  for (unsigned spins = 0; spins < SPIN_CAP; ++spins) {
    __builtin_amdgcn_s_sleep(8);
    asm volatile("buffer_load_dwordx4 %0, %6, %8, %9 offen sc1\n\tbuffer_load_dwordx4 %1, %6, %8, %9 offen offset:1024 sc1\n\t"
                 "buffer_load_dwordx4 %2, %6, %8, %9 offen offset:2048 sc1\n\tbuffer_load_dwordx4 %3, %6, %8, %9 offen offset:3072 sc1\n\t"
                 "buffer_load_dword %4, %7, %8, %9 offen sc1\n\tbuffer_load_dword %5, %7, %8, %9 offen offset:256 sc1\n\ts_waitcnt vmcnt(0)"
                 : "=&v"(d[0]), "=&v"(d[1]), "=&v"(d[2]), "=&v"(d[3]), "=&v"(ck[0]), "=&v"(ck[1])
                 : "v"(vo), "v"(vc), "s"(rs), "s"(so) : "memory");
    if (__all(csum8(tag, d[0], d[1]) == ck[0] && csum8(tag, d[2], d[3]) == ck[1])) break;
  }
}
DI void reload_e(u32x4& d, unsigned& ck, rsrc_t rs, unsigned vo, unsigned vc, unsigned so, unsigned tag) {
#pragma nounroll
  for (unsigned spins = 0; spins < SPIN_CAP; ++spins) {
    __builtin_amdgcn_s_sleep(10);
    asm volatile("buffer_load_dwordx4 %0, %2, %4, %5 offen sc1\n\tbuffer_load_dword %1, %3, %4, %6 offen sc1\n\ts_waitcnt vmcnt(0)"
                 : "=&v"(d), "=&v"(ck) : "v"(vo), "v"(vc), "s"(rs), "s"(so), "s"(so * 8) : "memory");
    if (__all((tag + d.x + d.y) + d.z + d.w == ck)) break;
  }
}
DI unsigned long long mism(unsigned v, unsigned tag) { unsigned long long m_ = __builtin_amdgcn_ballot_w64(v != tag); asm volatile("" : "+s"(m_)); return m_; }

#define STAMP(i)
#define RETRY(i)

constexpr int kTileOfX[7] = {0, 2, 3, 5, 6, 8, 9};
constexpr int kTileOfW[14] = {1, 4, 7, 10, 11, 12, 13, 14, 15, 16, 17, 18, 19, 20};
constexpr int kTileKind[21] = {0, 1, 0, 0, 1, 0, 0, 1, 0, 0, 1, 1, 1, 1, 2, 2, 2, 2, 2, 2, 2};
constexpr int kTileIdx[21] = {0, 0, 1, 2, 1, 3, 4, 2, 5, 6, 3, 4, 5, 6, 0, 1, 2, 3, 4, 5, 6};
constexpr int kSegX0[4] = {0, 1, 3, 5}, kSegNX[4] = {1, 2, 2, 2};
constexpr int kIssue0[5] = {5, 8, 10, 13, 16};
constexpr int tile_of_x(int j) { return kTileOfX[j]; }
constexpr int tile_of_w(int s) { return kTileOfW[s]; }
constexpr int tile_kind(int T) { return kTileKind[T]; }
constexpr int tile_idx(int T) { return kTileIdx[T]; }
constexpr unsigned step_loc(int g, int s) { return g ? (s & 3) * 16384 : ((s & 3) ? (4 + (s & 3)) * 16384 : L_PAUX); }
constexpr unsigned z_loc(int hv, int gs) { return (hv ? 5 + gs : (gs ? 4 : 7)) * 16384; }
enum { OP_EP = 29, OP_P = 30, OP_GS = 50, OP_GC1, OP_GC2, OP_E, OP_H1, OP_H2, OP_POLL, OP_XRES, OP_FL, OP_OPT = 59, OP_HE = 60, OP_HL = 70, OP_Z = 80 };

__global__ __launch_bounds__(512, 2) void gf_glu_kernel(const char* x, const char* wr, const char* wi, const char* wg,
                                                        const char* wu, const char* wd, char* out, char* ws) {
  extern __shared__ __attribute__((aligned(16))) char smem[];
  STAMP(0);
  const int tid = threadIdx.x;
  const int w = __builtin_amdgcn_readfirstlane(tid >> 6);
  const int l = tid & 63, r = l & 15, q = l >> 4;
  const int b = blockIdx.x, xcd = b & 7, bi = b >> 3;
  const unsigned L0 = (unsigned)(uintptr_t)(LAS char*)smem;

  unsigned code_touch;
  {
    unsigned long long pc;
    unsigned code_bytes;
    asm volatile("s_getpc_b64 %0\ngf_code_begin:\n\ts_mov_b32 %1, gf_code_end-gf_code_begin" : "=s"(pc), "=s"(code_bytes));
    const char* pcb = (const char*)(pc & ~127ull);
    unsigned off = (unsigned)((bi * 16 + w * 2 + (l & 1)) * 128);
    off = off < code_bytes ? off : 0;
    asm volatile("global_load_dword %0, %1, %2" : "=v"(code_touch) : "v"(off), "s"(pcb) : "memory");
  }
  unsigned epoch;
#define LOAD_EPOCH() asm volatile("global_load_dword %0, %1, %2 ; OP %3" : "=v"(epoch) : "v"(0u), "s"(ws), "i"(OP_EP) : "memory")
#define GET_TAG() PIN(epoch); PIN(code_touch); const unsigned tag = (unsigned)__builtin_amdgcn_readfirstlane(epoch) * 0x9E3779B1u + 0x7F4A7C15u
  constexpr unsigned OOB = 0x40000000u;

  if (__builtin_expect(bi >= 4, 1)) {
    const int bj = bi - 4;
    const int pn = 4 * (xcd & 3) + (bj & 3), tt = 7 * (xcd >> 2) + (bj >> 2);
    int rot = (bj >> 2) + 2 * (bj & 3) + (xcd & 1);
    rot = rot >= 7 ? rot - 7 : rot;
    const int m = w & 3, nh = w >> 2;
    const int rt = 4 * xcd + (bj & 3), ct = bj >> 2;
    unsigned kx[7];
    kx[0] = (unsigned)(rot * 128);
    unsigned xoff[4], woff[4], doff[4];
    const int lr = l >> 3, lc = l & 7;
    {
      const int n2 = (pn & 1) + 8 * (pn >> 1) + 2 * m;
      const unsigned rb = (unsigned)((64 * lr + n2) * 896) + DBIAS, cx = (unsigned)((lc ^ (lr >> 1)) << 4);
      xoff[0] = rb + cx;
      xoff[1] = rb + (8 * 64 * 896 - 1024) + (cx ^ 64);
      woff[0] = (unsigned)((16 * m + lr) * 3584 + tt * 256 + ((lc ^ (lr & 2)) << 4)) + DBIAS;
      woff[1] = (woff[0] ^ 64) + (8 * 3584 - 1024);
    }
    unsigned lm4 = L0 + m * 4096;
    unsigned lx0 = lm4 + nh * 2048, xs0 = nh * (16 * 64 * 896);
    PINS(lx0); PINS(xs0); PINS(kx[0]); PIN(xoff[0]); PIN(xoff[1]); PIN(woff[0]); PIN(woff[1]);
    SB();
    const rsrc_t rx = mk_rsrc(x - DBIAS, 2048 * 896 + DBIAS), rs0 = mk_rsrc((nh ? wu : wg) - DBIAS, 448 * 3584 + DBIAS);
    auto issue_half = [&](auto TI) {
      constexpr int T = decltype(TI)::v, sl = (T & 7) * 16384;
      if constexpr (tile_kind(T) == 0) {
        const unsigned so = dma_setup<sl, 1>(lx0, kx[tile_idx(T)] + xs0);
        dma_ld<T, 0>(rx, xoff[0], so); dma_ld<T, 1024>(rx, xoff[1], so);
      } else {
        const unsigned so = dma_setup<sl, 1792>(lx0, kx[tile_idx(T)]);
        dma_ld<T, 0>(rs0, woff[0], so); dma_ld<T, 1024>(rs0, woff[1], so);
      }
    };
    issue_half(IC<0>{});
    issue_half(IC<1>{});
    LOAD_EPOCH();
    STAMP(1);
    SB();
#pragma unroll
    for (int j = 1; j < 7; ++j) { kx[j] = kx[j - 1] == 768 ? 0 : kx[j - 1] + 128; PINS(kx[j]); }
    SB();

    {
      int c = 16 * tt + 8 * nh + (r >> 1);
      const float ph = (r & 1) ? 0.25f : 0.0f;
      c = c ? c : 224;
      const float cfl = (float)c, cf = cfl * (1.0f / 448.0f);
      const float k2 = 2.0f * __builtin_amdgcn_cosf(cf);
#pragma unroll
      for (int it = 0; it < 4; ++it) {
        const int ks = m + 4 * it;
        if (it < 3 || m < 2) {
          const unsigned kk = (m & 2) ? kx[it < 3 ? 2 * it + 1 : 0] : kx[2 * it];
          const float t = cfl * (float)(int)((kk >> 1) + 32 * (m & 1) + 8 * q);
          const float rem = __builtin_fmaf(-448.0f, __builtin_floorf(t * (1.0f / 448.0f)), t);
          const float rev = __builtin_fmaf(rem, 1.0f / 448.0f, ph);
          float v[8];
          v[0] = __builtin_amdgcn_cosf(rev);
          v[1] = __builtin_amdgcn_cosf(rev + cf);
#pragma unroll
          for (int e = 2; e < 8; ++e) v[e] = __builtin_fmaf(k2, v[e - 1], -v[e - 2]);
          *(LAS u32x4*)(uintptr_t)(L0 + L_AUX + (nh * 14 + ks) * 1024 + l * 16) =
              u32x4{pk2(v[0], v[1]), pk2(v[2], v[3]), pk2(v[4], v[5]), pk2(v[6], v[7])};
        } else {
          const int k1 = 16 * (m & 1) + r;
          float v[4];
#pragma unroll
          for (int e = 0; e < 4; ++e)
            v[e] = __builtin_amdgcn_cosf((float)((k1 * (4 * q + e)) & 31) * (1.0f / 32.0f) - (nh ? 0.25f : 0.0f));
          const unsigned a0 = pk2(v[0], v[1]), a1 = pk2(v[2], v[3]), sg = (r & 1) ? 0x80008000u : 0u;
          *(LAS u32x4*)(uintptr_t)(L0 + L_P1 + (2 * nh + (m & 1)) * 1024 + l * 16) = u32x4{a0, a1, a0 ^ sg, a1 ^ sg};
        }
      }
    }
    unsigned xa[2][2], wa[2][4];
    {
      const int R = 32 * m + r;
      xa[0][0] = L0 + R * 128 + ((q ^ ((R >> 1) & 7)) << 4);
      xa[0][1] = xa[0][0] ^ 64;
      const int qq = r >> 2, pp = r & 3;
      const int swz = ((qq >> 1) & 1) | ((q & 1) << 1);
      wa[0][0] = L0 + (q >> 1) * 4096 + (8 * (q & 1) + qq) * 128 + (swz << 5) + pp * 8;
#pragma unroll
      for (int i = 0; i < 4; ++i) { wa[0][i] = wa[0][0] ^ (i << 5); wa[1][i] = wa[0][i] + 65536; PIN(wa[0][i]); PIN(wa[1][i]); }
#pragma unroll
      for (int i = 0; i < 2; ++i) { xa[1][i] = xa[0][i] + 65536; PIN(xa[1][i]); PIN(xa[0][i]); }
    }
    unsigned l16 = l * 16, aux16 = L0 + L_AUX + l * 16;
    PIN(l16); PIN(aux16); PINS(lm4);
    const rsrc_t rg = mk_rsrc(wg - DBIAS, 448 * 3584 + DBIAS), ru = mk_rsrc(wu - DBIAS, 448 * 3584 + DBIAS), rd = mk_rsrc(wd, 1792 * 896), rw = mk_rsrc(ws, WS_BYTES);
    STAMP(2);
    SB();
    issue_half(IC<2>{});
    issue_half(IC<4>{});
    waitv<4, OP_EP>();
    need<1>();
    GET_TAG();

    constexpr unsigned S_ = 16384;
    unsigned dso;
    auto issue1 = [&](auto TI, auto II) {
      constexpr int T = decltype(TI)::v, i = decltype(II)::v;
      if constexpr (i == 0) dso = dma_setup<(T & 7) * S_, tile_kind(T) ? 1792 : 1>(lm4, kx[tile_idx(T)]);
      if constexpr (tile_kind(T) == 0) dma_ld<T, i * 1024>(rx, xoff[i], dso);
      else dma_ld<T, i * 1024>((i >> 1) ? ru : rg, woff[i], dso);
    };
    auto woff_setup = [&](unsigned add) { woff[0] += add; woff[1] += add; woff[2] = woff[0] - 2048; woff[3] = woff[1] - 2048; PIN(woff[0]); PIN(woff[1]); PIN(woff[2]); PIN(woff[3]); };
    auto issue = [&](auto TI) { sfor<0, 4>([&](auto II) { issue1(TI, II); }); };
    const int g1 = 4 * pn + m;
    u32x4 xf[14][2];
    u32x2 fa_[4][2], fb_[4][2];
    f32x4 acc[8][2];
    auto rdx = [&](auto XI) {
      constexpr int xr = decltype(XI)::v, xt = xr >> 2, kk = (xr >> 1) & 1, xs = tile_of_x(xt) & 7;
      xf[2 * xt + kk][xr & 1] = lds128(xa[xs >> 2][kk] + (xs & 3) * S_ + (xr & 1) * 2048);
    };
    auto rdw = [&](auto SI, auto KI) {
      constexpr int sl = tile_of_w(decltype(SI)::v) & 7, k = decltype(KI)::v, Q = k >> 3, i = (k & 7) >> 1, h = k & 1;
      const unsigned a = wa[sl >> 2][i] + (sl & 3) * S_ + (Q & 1) * 2048 + (Q >> 1) * 8192 + h * 512;
      if constexpr (Q & 1) fb_[i][h] = ldstr1(a); else fa_[i][h] = ldstr1(a);
    };
    auto mm = [&](auto SI, auto KI) {
      constexpr int j = decltype(SI)::v % 7, k = decltype(KI)::v, Q = k >> 3, i = (k & 7) >> 1, h = k & 1;
      constexpr int ks = 2 * j + ((Q + 1) >> 1) - 1, u = (Q & 1) ? i : 4 + i;
      if constexpr (ks >= 0) {
        const u32x4 a = (Q & 1) ? cat(fa_[i][0], fa_[i][1]) : cat(fb_[i][0], fb_[i][1]);
        if constexpr (ks == 0) mma0(acc[u][h], a, xf[0][h]); else mma(acc[u][h], a, xf[ks][h]);
      }
    };
    auto segment = [&](auto SI, auto EARLY, auto&& fn) {
      sfor<0, 32>([&](auto KI) {
        constexpr int k = decltype(KI)::v;
        mm(SI, KI);
        SB();
        if constexpr (!decltype(EARLY)::v || k < 24) rdw(SI, KI);
        if constexpr (decltype(EARLY)::v && k >= 20 && k < 28) rdw(SI, IC<k + 4>{});
        fn(KI);
        SB();
      });
    };
    auto tail = [&]() {
      sfor<0, 8>([&](auto KI) { constexpr int i = decltype(KI)::v >> 1, h = decltype(KI)::v & 1; mma(acc[4 + i][h], cat(fb_[i][0], fb_[i][1]), xf[13][h]); });
    };

    float mrow[2], r2row[2];
    u32x4 gran;
    unsigned pollv;
    u32x4 hb[4][4];
    unsigned hck[4][2], l4c, hs[2];
    unsigned pso[7], hso[7];
    auto step_setup = [&](int s) {
      int v = 4 * (rot + s) + m;
      v = v >= 28 ? v - 28 : v;
      pso[s] = (unsigned)(128 * (v >> 1) + 64 * nh + 32 * (v & 1));
      hso[s] = (unsigned)(WS_H + (rt * 56 + 28 * nh + v) * HBLK);
      PINS(pso[s]); PINS(hso[s]);
    };
    auto load_p = [&](auto GI, auto SI, auto II) {
      constexpr int s = decltype(SI)::v, i = decltype(II)::v;
      if constexpr (i == 0) dso = dma_setup<step_loc(decltype(GI)::v, s), 896>(lm4, pso[s]);
      dma_ld<OP_P + s, i * 1024>(rd, doff[i], dso);
    };
    auto load_h1 = [&](auto SI, auto II) {
      constexpr int s = decltype(SI)::v, i = decltype(II)::v;
      if constexpr (i < 4) ld128_sc1<OP_HE + s, (i & 3) * 1024>(hb[s & 3][i & 3], rw, l16, hso[s]);
      else ld32s_sc1<OP_HE + s, (i & 1) * 256>(hck[s & 3][i & 1], rw, l4c, hso[s]);
    };
    auto load_ps = [&](auto GI, auto SI) { sfor<0, 4>([&](auto II) { load_p(GI, SI, II); }); };
    auto load_hs = [&](auto SI) { sfor<0, 6>([&](auto II) { load_h1(SI, II); }); };
    const unsigned hst = WS_H + (unsigned)((g1 >> 1) * 56 + 28 * nh + 2 * tt) * HBLK;
    const unsigned csq = L0 + L_CS + nh * 512 + q * 16;
    auto h_block = [&](auto NB, auto PACKED, auto&& fn) {
      constexpr int nb = decltype(NB)::v;
      f32x4 cs[4];
      unsigned hpk[2][4];
#pragma unroll
      for (int i = 0; i < 4; ++i) cs[i] = lds128f(csq + (i >> 1) * 256 + (2 * nb + (i & 1)) * 64);
      sfor<0, 4>([&](auto CI) {
        constexpr int i2 = decltype(CI)::v >> 1, sub = decltype(CI)::v & 1, u = 2 * nb + sub;
        if constexpr (decltype(PACKED)::v) {
          const f32x4 h0 = ((acc[u][i2] - mrow[i2] * cs[sub]) * r2row[i2]) * (acc[4 + u][i2] - mrow[i2] * cs[2 + sub]);
          hpk[i2][2 * sub] = pk2(h0[0], h0[1]); hpk[i2][2 * sub + 1] = pk2(h0[2], h0[3]);
        } else {
          float dv[4], ev[4];
#pragma unroll
          for (int e = 0; e < 4; ++e) dv[e] = opq(__builtin_fmaf(-mrow[i2], cs[sub][e], acc[u][i2][e]));
#pragma unroll
          for (int e = 0; e < 4; ++e) ev[e] = opq(__builtin_fmaf(-mrow[i2], cs[2 + sub][e], acc[4 + u][i2][e]));
#pragma unroll
          for (int e = 0; e < 4; ++e) dv[e] = opq(dv[e] * r2row[i2]);
#pragma unroll
          for (int e = 0; e < 4; ++e) dv[e] = opq(dv[e] * ev[e]);
          hpk[i2][2 * sub] = pk2(dv[0], dv[1]); hpk[i2][2 * sub + 1] = pk2(dv[2], dv[3]);
        }
        PIN(hpk[i2][2 * sub]); PIN(hpk[i2][2 * sub + 1]);
        SB();
        fn(CI);
        SB();
      });
      hb[2 + nb][0] = u32x4{hpk[0][0], hpk[0][1], hpk[0][2], hpk[0][3]};
      hb[2 + nb][1] = u32x4{hpk[1][0], hpk[1][1], hpk[1][2], hpk[1][3]};
      hs[nb] = csum8(tag, hb[2 + nb][0], hb[2 + nb][1]);
      PIN(hs[nb]);
    };
    auto h_store = [&](auto NB) {
      constexpr int nb = decltype(NB)::v;
      st128_sc1<OP_H1, 0>(rw, l16, hst + nb * HBLK + (m & 1) * 2048, hb[2 + nb][0]);
      st128_sc1<OP_H1, 1024>(rw, l16, hst + nb * HBLK + (m & 1) * 2048, hb[2 + nb][1]);
      st32_sc1<OP_H1>(rw, l4c, hst + nb * HBLK + (m & 1) * 256, hs[nb]);
    };
    const unsigned gc_off = (unsigned)(WS_CS + tt * 2048 + l * 16);
    auto cs_to_lds = [&](int ps) {
      if (__builtin_expect((mism(gran.y, tag) | mism(gran.w, tag)) != 0, 0)) { RETRY(13 + ps); poll_granules(gran, rw, gc_off + ps * 1024, tag); }
      const unsigned a = L0 + L_CS + ps * 512 + l * 8;
      *(LAS unsigned*)(uintptr_t)(a) = gran.x; *(LAS unsigned*)(uintptr_t)(a + 4) = gran.z;
    };
    auto row_stats = [&]() {
      const unsigned st = L0 + L_ST + m * 256 + r * 4;
      mrow[0] = *(LAS const float*)(uintptr_t)(st); mrow[1] = *(LAS const float*)(uintptr_t)(st + 64);
      const float r0 = *(LAS const float*)(uintptr_t)(st + 128), r1 = *(LAS const float*)(uintptr_t)(st + 192);
      r2row[0] = opq(r0 * r0); r2row[1] = opq(r1 * r1);
    };
    auto wait_words = [&](unsigned a, unsigned n) {
#pragma nounroll
      for (unsigned spins = 0; spins < 64 * SPIN_CAP; ++spins) {
        const u32x4 v = *(volatile LAS u32x4*)(uintptr_t)a;
        const unsigned lo = min(min(v.x, v.y), min(v.z, v.w));
        if ((unsigned)__builtin_amdgcn_readfirstlane(lo) >= n) break;
        __builtin_amdgcn_s_sleep(1);
      }
      asm volatile("" ::: "memory");
    };

    u32x4 gf[2][2][4];
    float g_d[4], g_f[4], g_k[4], g_t[4], g_u[4], g_c[4][3], g_s[4][3], g_cq;
    unsigned g_mk;
    unsigned g_ny;
    auto gop = [&](auto OI) {
      constexpr int o = decltype(OI)::v, hv = o / 136, a = o & 3, dt = a, i = (o % 136) >> 2;
      if constexpr (hv == 1 && (i == 0 || i == 2 || i == 4 || i == 10)) { if constexpr (o == 136) g_cq = opq(g_cq + 28.0f); }
      else if constexpr (i == 0) g_d[a] = opq((float)(64 * ct + 16 * dt + r));
      else if constexpr (i == 1) g_t[a] = opq(g_d[a] * g_cq);
      else if constexpr (i == 2) g_f[a] = opq(g_d[a] * (1.0f / 448.0f));
      else if constexpr (i == 3) g_u[a] = opq(g_t[a] * (1.0f / 448.0f));
      else if constexpr (i == 4) g_k[a] = opq(__builtin_amdgcn_cosf(g_f[a]));
      else if constexpr (i == 5) g_u[a] = opq(__builtin_floorf(g_u[a]));
      else if constexpr (i == 6) g_t[a] = opq(__builtin_fmaf(-448.0f, g_u[a], g_t[a]));
      else if constexpr (i == 7) g_t[a] = opq(g_t[a] * (-1.0f / 448.0f));
      else if constexpr (i == 8) g_u[a] = opq(g_t[a] - g_f[a]);
      else if constexpr (i == 9) g_c[a][0] = opq(__builtin_amdgcn_cosf(g_t[a]));
      else if constexpr (i == 10) g_k[a] = opq(g_k[a] + g_k[a]);
      else if constexpr (i == 11) g_s[a][0] = opq(__builtin_amdgcn_sinf(g_t[a]));
      else if constexpr (i == 12) { unsigned p_ = pk2(g_c[a][0], g_s[a][0]); if constexpr (hv == 0) p_ ^= g_ny; PIN(p_); gf[hv][0][dt][0] = p_; }
      else if constexpr (i == 13) g_c[a][1] = opq(__builtin_amdgcn_cosf(g_u[a]));
      else if constexpr (i == 14) g_s[a][1] = opq(__builtin_amdgcn_sinf(g_u[a]));
      else if constexpr (i == 15) { unsigned p_ = pk2(g_c[a][1], g_s[a][1]); PIN(p_); gf[hv][0][dt][1] = p_; }
      else {
        constexpr int e = 2 + (i - 16) / 3, j = (i - 16) % 3;
        if constexpr (j == 0) g_c[a][e % 3] = opq(__builtin_fmaf(g_k[a], g_c[a][(e - 1) % 3], -g_c[a][(e - 2) % 3]));
        else if constexpr (j == 1) g_s[a][e % 3] = opq(__builtin_fmaf(g_k[a], g_s[a][(e - 1) % 3], -g_s[a][(e - 2) % 3]));
        else { unsigned p_ = pk2(g_c[a][e % 3], g_s[a][e % 3]); if constexpr (e >= 4) p_ &= g_mk; PIN(p_); gf[hv][e >> 2][dt][e & 3] = p_; }
      }
    };

    unsigned long long bad0, bad1;
    unsigned da[2][4];
    u32x2 af[2][4][2];
    auto da_setup = [&](unsigned b0, unsigned b1) {
      const int row = 4 * q + (r >> 2);
      const unsigned a = lm4 + row * 128 + (((row >> 1) & 3) << 5) + (r & 3) * 8;
#pragma unroll
      for (int dt = 0; dt < 4; ++dt) { da[0][dt] = (a ^ (dt << 5)) + b0; da[1][dt] = (a ^ (dt << 5)) + b1; PIN(da[0][dt]); PIN(da[1][dt]); }
#pragma unroll
      for (int i = 0; i < 4; ++i) { doff[i] = (unsigned)(lr * 896 + ct * 128 + ((lc ^ (((lr >> 1) & 3) << 1)) << 4)) + i * (8 * 896 - 1024); PIN(doff[i]); }
    };
    auto rda1 = [&](int g, int s, int dt, int h) { af[s & 1][dt][h] = ldstr1(da[step_loc(g, s) >> 17][dt] + (step_loc(g, s) & 0xffff) + h * 2048); };
    auto pin_af = [&](int s) {
#pragma unroll
      for (int dt = 0; dt < 4; ++dt) { PIN(af[s & 1][dt][0]); PIN(af[s & 1][dt][1]); }
    };
    auto prep = [&](auto GI, auto SI, auto JI) {
      constexpr int g = decltype(GI)::v, s = decltype(SI)::v, j = decltype(JI)::v;
      u32x4* hfr = hb[s & 3];
      if constexpr (j == 0) { need<OP_HE + s>(); need<OP_P + s>(); PIN(hfr[0]); PIN(hfr[1]); PIN(hfr[2]); PIN(hfr[3]); PIN(hck[s & 3][0]); PIN(hck[s & 3][1]); }
      if constexpr (j < 4) { rda1(g, s, j, 0); rda1(g, s, j, 1); }
      if constexpr (j >= 1 && j < 5) {
        constexpr int f = (j - 1) >> 1, o = 2 * ((j - 1) & 1);
        hs[0] = (j == 1 ? tag : hs[0]) + hfr[f][o] + hfr[f][o + 1];
        hs[1] = (j == 1 ? tag : hs[1]) + hfr[2 + f][o] + hfr[2 + f][o + 1];
        PIN(hs[0]); PIN(hs[1]);
      }
      if constexpr (j == 5) { bad0 = mism(hs[0], hck[s & 3][0]); bad1 = mism(hs[1], hck[s & 3][1]); }
      if constexpr (j == 7) { if (__builtin_expect((bad0 | bad1) != 0, 0)) { RETRY(s + 16 * g); reload_h(hfr, hck[s & 3], rw, l16, l4c, hso[s], tag); } }
    };
    f32x4 oacc[4][4];
    auto gstep = [&](auto SI, auto&& fn) {
      constexpr int s = decltype(SI)::v;
      sfor<0, 16>([&](auto KI) {
        constexpr int k = decltype(KI)::v, dt = k >> 2, rf = k & 3;
        if constexpr (s == 0) mma0(oacc[dt][rf], cat(af[0][dt][0], af[0][dt][1]), hb[0][rf]);
        else mma(oacc[dt][rf], cat(af[s & 1][dt][0], af[s & 1][dt][1]), hb[s & 3][rf]);
        SB();
        fn(KI);
        SB();
      });
    };
    auto step_ld = [&](auto GI, auto SI, auto NI) {
      constexpr int n = decltype(NI)::v;
      if constexpr (n < 4) load_p(GI, SI, NI); else load_h1(SI, IC<n - 4>{});
      if constexpr (n == 7) { load_h1(SI, IC<4>{}); load_h1(SI, IC<5>{}); }
    };
    f32x4 fin[2];
    u32x4 sel[4], xd, xres;
    unsigned pw[4], ra[8], bp, ooff;
    auto ooff_setup = [&]() {
      const int gg = 2 * rt + (w >> 2);
      const int n2 = ((gg >> 2) & 1) + 8 * (gg >> 3) + 2 * (gg & 3);
      ooff = (unsigned)((64 * ((tid >> 3) & 31) + n2) * 896 + ct * 128 + lc * 16); PIN(ooff);
    };
    auto fin_setup = [&](auto KI) {
      constexpr int k = decltype(KI)::v;
      if constexpr (k == 0) { need<OP_XRES>(); PIN(xres); bp = (unsigned)((8 * (r & 7) + 4 * (r >> 3) + q) * 4); PIN(bp); }
      if constexpr (k >= 1 && k < 5) {
        const unsigned t_ = xres[k - 1]; xd[k - 1] = (unsigned)__builtin_amdgcn_ds_bpermute((int)bp, (int)t_);
        const unsigned o_ = r == 4 * (k - 1) + q ? 0x3f803f80u : 0u; sel[k - 1] = u32x4{o_, o_, o_, o_}; PIN(sel[k - 1]);
      }
      if constexpr (k == 5) { pw[0] = lm4 + (nh ? 0 : 65536) + r * 128 + ((q ^ r ^ (m & 1) ^ ((m & 2) << 2)) << 3); PIN(pw[0]); }
      if constexpr (k == 6) { pw[1] = pw[0] ^ 32; pw[2] = pw[0] ^ 64; pw[3] = pw[0] ^ 96; PIN(pw[1]); PIN(pw[2]); PIN(pw[3]); }
      if constexpr (k == 7) { bp = (unsigned)((lr + 8 * (lc >> 2) + 16 * (lc & 3)) * 4); PIN(bp); }
      if constexpr (k == 8) {
        const int qq = r >> 2, row = 8 * (w & 1) + q;
        ra[0] = L0 + (w >> 1) * 16384 + qq * 4096 + row * 128 + (((2 * (r & 3)) ^ row ^ (qq & 1) ^ ((qq & 2) << 2)) << 3) + 2048; PIN(ra[0]);
      }
      if constexpr (k == 9) {
#pragma unroll
        for (int i = 1; i < 8; ++i) { ra[i] = ra[0] ^ (((i >> 1) & 1) << 5) ^ ((i >> 2) << 6) ^ ((i & 1) << 3); PIN(ra[i]); }
      }
      if constexpr (k == 10) {
#pragma unroll
        for (int i = 0; i < 4; ++i) { const unsigned t_ = xd[i]; fin[i >> 1][2 * (i & 1)] = opq(bflo(t_)); fin[i >> 1][2 * (i & 1) + 1] = opq(bfhi(t_)); }
      }
    };
    auto put = [&](int g, int dt, int rf) {
      const f32x4 o = oacc[dt][rf];
      *(LAS u32x2*)(uintptr_t)(pw[dt] + rf * 16384 + g * 2048) = u32x2{pk2(o[0], o[1]), pk2(o[2], o[3])};
    };

    if (nh) {
      ONLY(Y);
      using G1 = IC<1>;
      f32x4 accD[2][2];
      u32x4 ff[8];
      unsigned tch, tco;
      const rsrc_t rtch = mk_rsrc((m & 2) ? wd : ((m & 1) ? wi : wr), (m & 2) ? 1792 * 896 : 921600);
      {
        const unsigned gid = (unsigned)(bj * 8 + xcd);
        tco = l < ((m & 2) ? 28 : 33) ? (((m & 2) ? (gid * 2 + (m & 1)) * 28 : gid * 33) + l) * 128 : OOB;
      }
      auto dft_seg = [&](auto SI, auto TA, auto TB, auto TC) {
        constexpr int s = decltype(SI)::v, x0 = kSegX0[s], nx = kSegNX[s];
        issue(TA);
        SB();
        sfor<0, 4 * nx>([&](auto II) {
          constexpr int i = decltype(II)::v;
          rdx(IC<4 * x0 + i>{});
          ff[i] = lds128(aux16 + ((i & 1) * 14 + 2 * x0 + (i >> 1)) * 1024);
        });
        SB();
        if constexpr (decltype(TB)::v == 14) {
          woff_setup(128);
          ld32_plain<OP_EP>(tch, rtch, tco);
        }
        __builtin_amdgcn_s_setprio(3);
        sfor<0, 4 * nx>([&](auto II) {
          constexpr int i = decltype(II)::v, ks = 2 * x0 + (i >> 1);
          if constexpr (decltype(TC)::v < 0) { if constexpr (i & 1) issue1(TB, IC<(i >> 1)>{}); }
          else if constexpr (i < 4) issue1(TB, II); else issue1(TC, IC<i & 3>{});
          SB();
          if constexpr (ks == 0) { mma0(accD[i & 1][0], xf[0][0], ff[i]); mma0(accD[i & 1][1], xf[0][1], ff[i]); }
          else { mma(accD[i & 1][0], xf[ks][0], ff[i]); mma(accD[i & 1][1], xf[ks][1], ff[i]); }
          SB();
        });
        __builtin_amdgcn_s_setprio(0);
        if constexpr (nx == 1) issue(TC);
      };
      u32x4 gran2;
      const unsigned gs_off = (unsigned)(WS_STAT + (g1 * 32 + (l & 31)) * 16);
      xoff[2] = xoff[0] + (16 * 64 * 896 - 2048); xoff[3] = xoff[1] + (16 * 64 * 896 - 2048); PIN(xoff[2]); PIN(xoff[3]);
      woff_setup(0);
      BARRIER();
      dft_seg(IC<0>{}, IC<3>{}, IC<5>{}, IC<6>{});
      waitv<8, 3>();
      BARRIER();
      STAMP(3);
      dft_seg(IC<1>{}, IC<7>{}, IC<8>{}, IC<9>{});
      waitv<8, 7>();
      BARRIER();
      dft_seg(IC<2>{}, IC<10>{}, IC<11>{}, IC<12>{});
      ld128_sc1<OP_GS, 0>(gran, rw, gs_off, 0u);
      waitv<9, 10>();
      BARRIER();
      STAMP(4);
      unsigned st = L0 + L_ST + m * 256 + (l & 31) * 4, stq = L0 + L_ST + m * 256 + 128 + q * 16, ea = L0 + L_EB + r * 264 + m * 64 + q * 8;
      PIN(st); PIN(stq); PIN(ea);
      dft_seg(IC<3>{}, IC<13>{}, IC<14>{}, IC<-1>{});
      waitv<5, 13>();
      need<OP_GS>();
      BARRIER();
      STAMP(5);
      {
        PIN(gran);
        bad0 = mism(gran.y, tag); bad1 = mism(gran.w, tag);
        u32x4 p1c[2], p1s[2];
#pragma unroll
        for (int a = 0; a < 2; ++a) { p1c[a] = lds128(aux16 + (L_P1 - L_AUX) + a * 1024); p1s[a] = lds128(aux16 + (L_P1 - L_AUX) + 2048 + a * 1024); }
        ld128_sc1<OP_GC1, 0>(gran2, rw, gc_off, 0u);
        if (__builtin_expect((bad0 | bad1) != 0, 0)) { RETRY(12); poll_granules(gran, rw, gs_off, tag); }
        *(LAS unsigned*)(uintptr_t)(st) = gran.x; *(LAS unsigned*)(uintptr_t)(st + 128) = gran.z;
        const f32x4 rD0 = lds128f(stq), rD1 = lds128f(stq + 64);
        issue(IC<15>{});
        SB();
        f32x4 tc[2][2], ts[2][2];
        u32x4 df[2];
#pragma unroll
        for (int u = 0; u < 2; ++u) {
          float dv[8];
#pragma unroll
          for (int e = 0; e < 4; ++e) { dv[e] = opq(rD0[e] * accD[u][0][e]); dv[4 + e] = opq(rD1[e] * accD[u][1][e]); }
          df[u] = u32x4{pk2(dv[0], dv[1]), pk2(dv[2], dv[3]), pk2(dv[4], dv[5]), pk2(dv[6], dv[7])};
          PIN(df[u]);
        }
        SB();
        __builtin_amdgcn_s_setprio(3);
#pragma unroll
        for (int u = 0; u < 2; ++u) { mma0(ts[u][0], df[u], p1s[0]); mma0(tc[u][0], df[u], p1c[0]); mma0(ts[u][1], df[u], p1s[1]); mma0(tc[u][1], df[u], p1c[1]); }
        __builtin_amdgcn_s_setprio(0);
        issue(IC<16>{});
        SB();
#pragma unroll
        for (int u = 0; u < 2; ++u)
#pragma unroll
          for (int a = 0; a < 2; ++a) {
            const float e0 = opq(tc[u][a][0] + ts[u][a][1]), e1 = opq(tc[u][a][1] - ts[u][a][0]);
            const float e2 = opq(tc[u][a][2] + ts[u][a][3]), e3 = opq(tc[u][a][3] - ts[u][a][2]);
            *(LAS u32x2*)(uintptr_t)(ea + a * (16 * 264) + u * 32) = u32x2{pk2(e0, e1), pk2(e2, e3)};
          }
        SB();
        issue(IC<17>{});
        *(LAS u32x4*)(uintptr_t)(L0 + L_SYNC + (l & 3) * 16) = u32x4{0, 0, 0, 0};
        waitv<8, 15>();
        need<OP_GC1>();
        PIN(gran2); PIN(tch);
        gran = gran2;
        cs_to_lds(0);
      }
      BARRIER();
      STAMP(6);
      __builtin_amdgcn_s_setprio(2);
      unsigned pgw = L0 + L_SYNC + m * 4;
      PIN(pgw);
      sfor<7, 14>([&](auto SI) {
        constexpr int s = decltype(SI)::v;
        if constexpr (s == 9) wait_words(L0 + L_SYNC, 1);
        if constexpr (s == 11) wait_words(L0 + L_SYNC + 16, 1);
        if constexpr (s == 12) wait_words(L0 + L_SYNC + 16, 2);
        segment(SI, IC<0>{}, [&](auto KI) {
          constexpr int k = decltype(KI)::v;
          if constexpr (k == 8 && s == 8) waitv<0, 17>();
          if constexpr (k == 12 && s == 8) { l4c = l * 4 + 4096; PIN(l4c); }
          if constexpr (k == 18 && s == 13) row_stats();
          if constexpr (k == 8 && s > 7) *(volatile LAS unsigned*)(uintptr_t)pgw = (unsigned)(s - 7);
          if constexpr (s >= 11 && k % 8 == 4 && 4 * (s - 11) + k / 8 < 7) step_setup(4 * (s - 11) + k / 8);
        });
        if constexpr (s == 9) STAMP(7);
      });
      tail();
      STAMP(9);
      __builtin_amdgcn_s_setprio(3);
      *(volatile LAS unsigned*)(uintptr_t)pgw = 7u;
      h_block(IC<0>{}, IC<1>{}, [&](auto) {});
      h_store(IC<0>{});
      h_block(IC<1>{}, IC<1>{}, [&](auto) {});
      h_store(IC<1>{});
      __builtin_amdgcn_s_setprio(0);
      STAMP(10);
      da_setup(0, 0);
      ooff_setup();
      wait_words(L0 + L_SYNC, 6);
      load_ps(G1{}, IC<0>{}); load_ps(G1{}, IC<1>{}); load_ps(G1{}, IC<2>{});
      ld128<OP_XRES, 0>(xres, rx, ooff, DBIAS);
      load_hs(IC<0>{}); load_hs(IC<1>{});
      STAMP(11);
      waitv<12, OP_XRES>();
      sfor<0, 11>([&](auto KI) { fin_setup(KI); });
      waitv<6, OP_HE + 0>();
      sfor<0, 8>([&](auto JI) { prep(G1{}, IC<0>{}, JI); });
      STAMP(12);
      sfor<0, 7>([&](auto SI) {
        constexpr int s = decltype(SI)::v;
        gstep(SI, [&](auto KI) {
          constexpr int k = decltype(KI)::v;
          if constexpr (s + 2 < 7 && k < 4) load_h1(IC<s + 2>{}, KI);
          if constexpr (s + 2 < 7 && k == 3) { load_h1(IC<s + 2>{}, IC<4>{}); load_h1(IC<s + 2>{}, IC<5>{}); }
          if constexpr (s + 3 < 7 && k >= 4 && k < 8) load_p(G1{}, IC<s + 3>{}, IC<k - 4>{});
          if constexpr (s < 6 && k == 8) waitv<(s == 0 ? 10 : (s < 4 ? 14 : (s == 4 ? 10 : 0))), OP_HE + (s < 6 ? s + 1 : s)>();
          if constexpr (s < 6 && k >= 8) prep(G1{}, IC<(s < 6 ? s + 1 : s)>{}, IC<k - 8>{});
          if constexpr (s == 6 && k == 0) pin_af(6);
          if constexpr (s == 6 && k < 3) asm volatile("s_nop 2");
          if constexpr (s == 6 && k >= 3) put(1, (k - 3) >> 2, (k - 3) & 3);
        });
        if constexpr (s == 1) STAMP(13);
        if constexpr (s == 3) STAMP(15);
        if constexpr (s == 5) STAMP(17);
      });
      STAMP(19);
      put(1, 3, 1); SB(); put(1, 3, 2); SB(); put(1, 3, 3);
      ONLY(END);
    } else {
      ONLY(O);
      using G0 = IC<0>;
      unsigned ebo, eo;
      __builtin_amdgcn_s_setprio(2);
      sfor<0, 7>([&](auto SI) {
        constexpr int s = decltype(SI)::v;
        constexpr int x0 = kSegX0[s < 4 ? s : 0], nx = s < 4 ? kSegNX[s < 4 ? s : 0] : 0;
        if constexpr (s == 1) waitv<0, 4>();
        if constexpr (s <= 4) BARRIER();
        if constexpr (s == 0) { rdx(IC<0>{}); rdx(IC<1>{}); }
        segment(SI, IC<(s <= 3 || s == 6)>{}, [&](auto KI) {
          constexpr int k = decltype(KI)::v;
          constexpr int xr = s == 0 ? (k == 8 ? 2 : (k == 10 ? 3 : -1)) : (k < 2 ? k : ((k & 1) ? -1 : 1 + k / 2));
          if constexpr (xr >= 0 && xr < 4 * nx) rdx(IC<4 * x0 + xr>{});
          if constexpr (s == 5 && k == 3) { ebo = L0 + L_EB + (tid >> 4) * 264 + r * 4; PIN(ebo); }
          if constexpr (s == 5 && k == 9) { eo = (unsigned)((tid >> 4) * E_K1 + pn * E_PN + (16 * tt + r) * 16); PIN(eo); }
        });
        if constexpr (s == 0) STAMP(3);
        if constexpr (s == 2) STAMP(4);
        if constexpr (s == 3) STAMP(5);
      });
      BARRIER();
      u32x4 e0, e1;
#pragma unroll
      for (int i = 0; i < 4; ++i) { e0[i] = *(LAS const unsigned*)(uintptr_t)(ebo + i * 64); e1[i] = *(LAS const unsigned*)(uintptr_t)(ebo + 16 * 264 + i * 64); }
      SB();
      tail();
      STAMP(6);
      {
        const unsigned ecs = (unsigned)(WS_EC + (pn * 14 + tt) * 2048);
        st128_sc1<OP_E, 0>(rw, eo, WS_E, e0);
        st128_sc1<OP_E, 0>(rw, eo, WS_E + 16 * E_K1, e1);
        st64_sc1<OP_E>(rw, tid * 8, ecs, u32x2{(tag + e0.x + e0.y) + e0.z + e0.w, (tag + e1.x + e1.y) + e1.z + e1.w});
        if (w == 3 && bj >= 24) st32_sc1<OP_OPT>(rw, l < 32 ? l * 4 : OOB, (unsigned)(WS_HINT + ((bj - 24) * 8 + (xcd ^ 4)) * 128), tag);
      }
      __builtin_amdgcn_s_setprio(0);
      woff_setup(128);
      issue(IC<18>{});
      row_stats();
      l4c = l * 4 + 4096; PIN(l4c);
      unsigned rdw_ = L0 + L_SYNC + 16 + m * 4;
      h_block(IC<0>{}, IC<0>{}, [&](auto CI) { issue1(IC<19>{}, CI); });
      h_block(IC<1>{}, IC<0>{}, [&](auto CI) { issue1(IC<20>{}, CI); });
      ld128_sc1<OP_GC2, 1024>(gran, rw, gc_off, 0u);
      waitv<9, 18>();
      *(volatile LAS unsigned*)(uintptr_t)rdw_ = 1u;
      h_store(IC<0>{});
      h_store(IC<1>{});
      STAMP(7);
      waitv<6, OP_GC2>();
      need<20>();
      PIN(gran);
      cs_to_lds(1);
      asm volatile("s_waitcnt lgkmcnt(0)" ::: "memory");
      *(volatile LAS unsigned*)(uintptr_t)rdw_ = 2u;
      da_setup(65536, 131072);
      step_setup(0); step_setup(1);
      load_ps(G0{}, IC<1>{});
      wait_words(L0 + L_SYNC + 16, 1);
      load_ps(G0{}, IC<0>{});
      g_cq = opq((float)(56 * m + 8 * q));
      g_mk = q == 3 ? 0u : ~0u;
      g_ny = (tid < 16 && (tid & 1)) ? 0x8000u : 0u;
      auto gops = [&](auto B0, auto N) { sfor<decltype(B0)::v, (decltype(B0)::v + decltype(N)::v < 272 ? decltype(B0)::v + decltype(N)::v : 272)>([&](auto O) { gop(O); }); };
      gops(IC<0>{}, IC<72>{});
#pragma unroll
      for (int s = 2; s < 7; ++s) step_setup(s);
      STAMP(8);
      wait_words(L0 + L_SYNC, 2);
      load_hs(IC<0>{}); load_hs(IC<1>{}); load_ps(G0{}, IC<2>{});
      ooff_setup();
      unsigned pz_off = w ? OOB : (unsigned)(WS_FLAGZ + (l & 31) * 128), zo, za[4], zva;
      waitv<10, OP_HE + 0>();
      sfor<0, 8>([&](auto JI) { prep(G0{}, IC<0>{}, JI); });
      STAMP(11);
      u32x4 ph[3][2], pc, psn;
      auto phop = [&](auto NI) {
        constexpr int n = decltype(NI)::v, i = n >> 2, v = n & 3;
        const unsigned c_ = pc[i], s_ = psn[i];
        unsigned u0 = (v & 1) ? __builtin_amdgcn_perm(c_, s_, (v & 2) ? 0x07060302u : 0x05040100u) : __builtin_amdgcn_perm(s_, c_, (v & 2) ? 0x07060302u : 0x05040100u);
        if constexpr (v == 0) { u0 ^= 0x80000000u; PIN(u0); ph[0][0][i] = u0; }
        if constexpr (v == 1) { PIN(u0); ph[0][1][i] = u0; }
        if constexpr (v == 2) { unsigned u1 = u0 ^ 0x8000u; u0 ^= 0x80000000u; PIN(u0); PIN(u1); ph[1][0][i] = u0; ph[2][0][i] = u1; }
        if constexpr (v == 3) { unsigned u1 = u0 ^ 0x80008000u; PIN(u0); PIN(u1); ph[1][1][i] = u0; ph[2][1][i] = u1; }
      };
      const unsigned zso = (unsigned)(WS_Z + 2 * rt * Z_G + 2 * m * 4096);
      auto issue_z1 = [&](auto HV, auto NI, auto IDI) {
        constexpr int hv = decltype(HV)::v, gs = decltype(NI)::v >> 2, i = decltype(NI)::v & 3;
        if constexpr (i == 0) dso = dma_setup<z_loc(hv, gs), 1>(lm4, zso + hv * 4096 + gs * Z_G);
        dma_ld_sc1<decltype(IDI)::v, i * 1024>(rw, zo, dso);
      };
      sfor<0, 7>([&](auto SI) {
        constexpr int s = decltype(SI)::v;
        if constexpr (s == 5) {
          PIN(pollv);
          if (w == 0) {
            if (mism(pollv, tag) != 0) { RETRY(9); poll_flags(rw, pz_off, tag); }
            *(volatile LAS u32x4*)(uintptr_t)(L0 + L_SYNC + 32) = u32x4{1, 1, 1, 1};
          } else {
            wait_words(L0 + L_SYNC + 32, 1);
          }
          wait_words(L0 + L_SYNC, 7);
        }
        if constexpr (s == 6) pin_af(6);
        gstep(SI, [&](auto KI) {
          constexpr int k = decltype(KI)::v;
          if constexpr (s + 2 < 7 && k < 4) load_h1(IC<s + 2>{}, KI);
          if constexpr (s + 2 < 7 && k == 3) { load_h1(IC<s + 2>{}, IC<4>{}); load_h1(IC<s + 2>{}, IC<5>{}); }
          if constexpr (s + 3 < 7 && k >= 4 && k < 8) load_p(G0{}, IC<s + 3>{}, IC<k - 4>{});
          if constexpr (s == 3 && k == 7) { ld128<OP_XRES, 0>(xres, rx, ooff, DBIAS); ld32_sc1<OP_POLL>(pollv, rw, pz_off); }
          if constexpr (s == 5 && k < 8) { issue_z1(IC<0>{}, KI, IC<OP_Z>{}); phop(IC<2 * k>{}); phop(IC<2 * k + 1>{}); }
          if constexpr (s == 6 && !(k & 1)) issue_z1(IC<1>{}, IC<k / 2>{}, IC<OP_Z + 1>{});
          if constexpr (s < 4 && k < 8) gops(IC<72 + 16 * s + 2 * k>{}, IC<2>{});
          if constexpr (s == 6) gops(IC<136 + 5 * k>{}, IC<5>{});
          if constexpr (s < 6 && k == 8) waitv<(s < 3 ? 14 : (s == 3 ? 16 : (s == 4 ? 6 : 8))), OP_HE + (s < 6 ? s + 1 : s)>();
          if constexpr (s == 4 && k == 8) need<OP_POLL>();
          if constexpr (s < 6 && k >= 8) prep(G0{}, IC<(s < 6 ? s + 1 : s)>{}, IC<k - 8>{});
          if constexpr (s == 4 && k == 4) { zo = (unsigned)(q * 256 + ((((l >> 1) & 7) ^ ((q & 1) | ((q & 2) << 1))) << 5) + (l & 1) * 16); PIN(zo); }
          if constexpr (s == 4 && k == 5) { const int P = 2 * q + (r >> 3); za[0] = (unsigned)(((r >> 1) & 2) ^ ((P & 1) | ((P & 2) << 1))); za[1] = P * 256 + (r & 3) * 8; PIN(za[0]); PIN(za[1]); }
          if constexpr (s == 4 && k == 6) { za[0] = lm4 + 65536 + za[1] + (za[0] << 5); zva = lm4 + 65536 + l16; PIN(za[0]); PIN(zva); }
          if constexpr (s == 4 && k == 7) { za[1] = za[0] ^ 128; za[2] = za[0] ^ 32; za[3] = za[0] ^ 160; PIN(za[1]); PIN(za[2]); PIN(za[3]); }
          if constexpr (s == 4 && k == 14) pc = lds128(aux16 + (L_P1 - L_AUX));
          if constexpr (s == 4 && k == 15) psn = lds128(aux16 + (L_P1 - L_AUX) + 2048);
        });
        if constexpr (s == 0) STAMP(9);
        if constexpr (s == 2) STAMP(12);
        if constexpr (s == 4) STAMP(13);
      });
      STAMP(15);
      sfor<0, 2>([&](auto HV) {
        constexpr int hv = decltype(HV)::v;
        waitv<hv ? 0 : 8, OP_Z + hv>();
        u32x4 z0[2], z1[2];
        f32x4 t[6];
        unsigned yy[2][2][4];
        auto rdz = [&](int u) {
          const unsigned a_ = za[2 * (u & 1)] + (z_loc(hv, u >> 1) & 0xffff), b_ = za[2 * (u & 1) + 1] + (z_loc(hv, u >> 1) & 0xffff);
          z0[u & 1] = ldstr(a_, a_ + 2048); z1[u & 1] = ldstr(b_, b_ + 2048);
        };
        auto amma = [&](int u, int i) { if (i < 2) mma0(t[i], z0[u & 1], ph[0][i]); else mmac(t[i], z1[u & 1], ph[i >> 1][i & 1], t[i & 1]); };
        auto ypk = [&](int u, int k) { yy[u & 1][k >> 2][k & 3] = pk2(t[2 + 2 * (k >> 2)][k & 3], t[3 + 2 * (k >> 2)][k & 3]); PIN(yy[u & 1][k >> 2][k & 3]); };
#pragma nounroll
        for (unsigned spins = 0;; ++spins) {
          u32x4 zv[4];
          unsigned zs;
          rdz(0);
#pragma unroll
          for (int i = 0; i < 4; ++i) zv[i] = lds128(zva + (z_loc(hv, 0) & 0xffff) + i * 1024);
          SB();
#pragma unroll
          for (int i = 0; i < 6; ++i) {
            amma(0, i);
            SB();
            if (i >= 1 && i < 5) { zs = ((i == 1 ? 0u : zs) + zv[i - 1].x + zv[i - 1].y) + zv[i - 1].z + zv[i - 1].w; PIN(zs); zv[i - 1] = lds128(zva + (z_loc(hv, 1) & 0xffff) + (i - 1) * 1024); }
            SB();
          }
          rdz(1);
#pragma unroll
          for (int i = 0; i < 4; ++i) { zs = (zs + zv[i].x + zv[i].y) + zv[i].z + zv[i].w; PIN(zs); }
          keep(t[0], t[1]);
          SB();
#pragma unroll
          for (int k = 0; k < 8; ++k) {
            ypk(0, k);
            if (k == 1) { zs += (unsigned)__builtin_amdgcn_update_dpp(0, (int)zs, 0x128, 0xf, 0xf, true); PIN(zs); }
            if (k == 3) { zs += (unsigned)__builtin_amdgcn_update_dpp(0, (int)zs, 0x124, 0xf, 0xf, true); PIN(zs); }
            if (k == 5) { zs += (unsigned)__builtin_amdgcn_update_dpp(0, (int)zs, 0x122, 0xf, 0xf, true); PIN(zs); }
            if (k == 7) { zs += (unsigned)__builtin_amdgcn_update_dpp(0, (int)zs, 0x121, 0xf, 0xf, true); PIN(zs); }
            SB();
          }
          bad0 = mism(zs, tag * 16u);
#pragma unroll
          for (int i = 0; i < 6; ++i) amma(1, i);
          if (__builtin_expect(bad0 == 0 || spins >= SPIN_CAP, 1)) break;
          RETRY(10 + hv);
          __builtin_amdgcn_s_sleep(10);
          asm volatile("s_waitcnt lgkmcnt(0)" ::: "memory");
          sfor<0, 8>([&](auto NI) { issue_z1(HV, NI, IC<OP_OPT>{}); });
          asm volatile("s_waitcnt vmcnt(0)" ::: "memory");
        }
        sfor<0, 4>([&](auto UI) {
          constexpr int u = decltype(UI)::v;
          const u32x4 y0 = {yy[u & 1][0][0], yy[u & 1][0][1], yy[u & 1][0][2], yy[u & 1][0][3]};
          const u32x4 y1 = {yy[u & 1][1][0], yy[u & 1][1][1], yy[u & 1][1][2], yy[u & 1][1][3]};
          if (u == 1 || u == 2) {
#pragma unroll
            for (int i = 0; i < 6; ++i) amma(u + 1, i);
          } else if (u == 3) asm volatile("s_nop 1");
          sfor<0, 8>([&](auto KI) {
            constexpr int k = decltype(KI)::v;
            mma(oacc[k >> 1][2 * (u >> 1) + (k & 1)], gf[hv][u & 1][k >> 1], (k & 1) ? y1 : y0);
            SB();
            if constexpr (u < 2 && k == 0) rdz(u + 2);
            if constexpr (u == 2 && k == 0) asm volatile("s_nop 1");
            if constexpr (u < 3 && k == 3) keep(t[0], t[1]);
            if constexpr (u < 3 && k >= 4) { ypk(u + 1, 2 * (k - 4)); ypk(u + 1, 2 * (k - 4) + 1); }
            if constexpr (hv == 0 && (u == 3 || k < 4)) gops(IC<216 + 3 * (4 * u + k)>{}, IC<3>{});
            if constexpr (hv == 1 && u < 3 && k < 4 && 4 * u + k < 11) fin_setup(IC<4 * u + k>{});
            if constexpr (hv == 1 && u == 3) put(0, k >> 1, k & 1);
            SB();
          });
        });
        if constexpr (hv == 0) STAMP(17);
      });
      STAMP(19);
#pragma unroll
      for (int k = 0; k < 8; ++k) { put(0, k >> 1, 2 + (k & 1)); SB(); }
      ONLY(END);
    }
    BARRIER();
    STAMP(20);
    {
      u32x4 pa[8];
#pragma unroll
      for (int i = 0; i < 8; ++i) pa[i] = ldstr(ra[i] + ((i >> 1) & 1) * 512, ra[i] + ((i >> 1) & 1) * 512 + 63488);
#pragma unroll
      for (int i = 0; i < 8; ++i) fin[i & 1] = mfma16(pa[i], sel[i >> 1], fin[i & 1]);
      const u32x4 o = {pk2(fin[0][0], fin[0][1]), pk2(fin[0][2], fin[0][3]), pk2(fin[1][0], fin[1][1]), pk2(fin[1][2], fin[1][3])};
      u32x4 oc;
#pragma unroll
      for (int i = 0; i < 4; ++i) { const unsigned t_ = o[i]; oc[i] = (unsigned)__builtin_amdgcn_ds_bpermute((int)bp, (int)t_); }
      *(u32x4*)(out + ooff) = oc;
    }
    if (b == 32 && tid == 0) *(unsigned*)(ws + WS_EPOCH) = tag;
    STAMP(21);
  } else {
    const int k1 = 8 * bi + xcd;
    const int lr = l >> 3, lc = l & 7;
    const int gst = 2 * (8 * (xcd & 3) + 4 * (xcd >> 2) + bi) + (w >> 2), n1s = 8 * (w & 3) + lr;
    const int cid = 4 * (xcd & 3) + bi;
    const int ctt = 7 * (xcd >> 2) + ((cid < 14 ? cid : 0) >> 1), cmat = cid & 1;
    const rsrc_t rx = mk_rsrc(x, 2048 * 896), rc = mk_rsrc(cmat ? wu : wg, 448 * 3584), rw = mk_rsrc(ws, WS_BYTES);
    const rsrc_t rfr = mk_rsrc(wr, 921600), rfi = mk_rsrc(wi, 921600);
    LOAD_EPOCH();
    u32x4 xr[7];
    {
      const int n2 = ((gst >> 2) & 1) + 8 * (gst >> 3) + 2 * (gst & 3);
      const unsigned xo = (unsigned)((64 * n1s + n2) * 896 + lc * 16);
      sfor<0, 7>([&](auto J) { constexpr int j = decltype(J)::v; ld128<0, j * 128>(xr[j], rx, xo, 0u); });
    }
    constexpr unsigned L_RED = 28672, L_WT = 32768, L_FILT = 32768;
    const unsigned wo = (unsigned)((56 * w + lr) * 3584 + ctt * 256 + ((lc ^ (((lr >> 1) & 3) << 1)) << 4));
    const unsigned lwt = L0 + L_WT + w * 7168;
    const int c31 = l & 31, h = l >> 5;
    {
      const int s = w, par = s >> 2;
      float vc[4], vs[4];
#pragma unroll
      for (int mm = 0; mm < 4; ++mm) {
        int n2 = par + 8 * (2 * (s & 3) + h) + 2 * mm;
        float rev = (float)((n2 * (k1 + 32 * c31)) & 2047) * (1.0f / 2048.0f);
        vc[mm] = __builtin_amdgcn_cosf(rev); vs[mm] = __builtin_amdgcn_sinf(rev);
      }
      *(LAS u32x4*)(uintptr_t)(L0 + (2 * s) * 1024 + l * 16) = u32x4{pk2(vc[0], vs[0]), pk2(vc[1], vs[1]), pk2(vc[2], vs[2]), pk2(vc[3], vs[3])};
      *(LAS u32x4*)(uintptr_t)(L0 + (2 * s + 1) * 1024 + l * 16) = u32x4{pk2(-vs[0], vc[0]), pk2(-vs[1], vc[1]), pk2(-vs[2], vc[2]), pk2(-vs[3], vc[3])};
    }
    {
      const int par = (w >> 1) & 1, s2 = w & 1;
      float v[8];
#pragma unroll
      for (int e = 0; e < 8; ++e) {
        int k2 = 16 * s2 + 8 * (e >> 2) + 4 * h + (e & 3);
        v[e] = __builtin_amdgcn_cosf((float)(((par + 2 * c31) * (k1 + 32 * k2)) & 2047) * (1.0f / 2048.0f) - (w >= 4 ? 0.25f : 0.0f)) * (2.0f / (2048.0f * 448.0f));
      }
      const u32x4 pk = {pk2(v[0], v[1]), pk2(v[2], v[3]), pk2(v[4], v[5]), pk2(v[6], v[7])};
      const unsigned a = L0 + 16384 + (w & 3) * 3072 + (w >> 2) * 1024 + l * 16;
      *(LAS u32x4*)(uintptr_t)(a) = pk;
      if (w >= 4) *(LAS u32x4*)(uintptr_t)(a + 1024) = pk ^ 0x80008000u;
    }
    SB();
    waitv<0, 0>();
    sfor<0, 3>([&](auto J) { constexpr int j = decltype(J)::v; dma_add<0, j * 1024, 0>(rc, wo, lwt, (unsigned)(j * (8 * 3584))); });
    GET_TAG();
    {
      float s1 = 0.f, s2 = 0.f;
#pragma unroll
      for (int j = 0; j < 7; ++j) { PIN(xr[j]); s1 = sum8(xr[j], s1); s2 = sq8(xr[j], s2); }
      s1 += dpp<0xB1>(s1); s2 += dpp<0xB1>(s2);
      s1 += dpp<0x4E>(s1); s2 += dpp<0x4E>(s2);
      s1 += dpp<0x141>(s1); s2 += dpp<0x141>(s2);
      const float mean = s1 * (1.0f / 448.0f);
      float var = __builtin_fmaf(-mean, mean, s2 * (1.0f / 448.0f));
      var = var > 0.f ? var : 0.f;
      const float rstd = __builtin_amdgcn_rsqf(var + 1e-5f);
      st128_sc1<0, 0>(rw, lc ? OOB : (unsigned)(WS_STAT + (gst * 32 + n1s) * 16), 0u, u32x4{asu(mean), tag, asu(rstd), tag});
    }
    STAMP(1);
    sfor<3, 7>([&](auto J) { constexpr int j = decltype(J)::v; dma_add<0, j * 1024, 0>(rc, wo, lwt, (unsigned)(j * (8 * 3584))); });
    const unsigned fo = (((unsigned)((k1 + 32 * (8 * w + h)) * 450)) & ~15u) + c31 * 16, lfb = L0 + L_FILT + w * 4096;
    const u32x4 ones = {0x3f803f80u, 0x3f803f80u, 0x3f803f80u, 0x3f803f80u};
    const unsigned csa = L0 + L_WT + (4 * q + (r >> 2)) * 128 + ((((4 * q + (r >> 2)) >> 1) & 3) << 5) + (r & 3) * 8;
    const unsigned cso = cid < 14 ? (unsigned)(WS_CS + (ctt * 4 + cmat) * 512) : OOB;
    auto colsum = [&](unsigned base, int ps) {
      const unsigned a = (csa ^ ((w & 3) << 5)) + (w >> 2) * 28672 + base;
      f32x4 sm = {0, 0, 0, 0};
#pragma unroll
      for (int j = 0; j < 7; ++j) sm = mfma16(ldstr(a + j * 4096, a + j * 4096 + 2048), ones, sm);
      if (r == 0) *(LAS f32x4*)(uintptr_t)(L0 + L_RED + ps * 512 + w * 64 + q * 16) = sm;
      BARRIER();
      if (w == 0) {
        const float t = *(LAS const float*)(uintptr_t)(L0 + L_RED + ps * 512 + l * 4) + *(LAS const float*)(uintptr_t)(L0 + L_RED + ps * 512 + 256 + l * 4);
        st64_sc1<0>(rw, cso + ps * 1024 + l * 8, 0u, u32x2{asu(t), tag});
      }
    };
    waitv<0, 0>();
    sfor<0, 4>([&](auto J) { constexpr int j = decltype(J)::v; dma_add<0, 65536 + j * 1024, 128>(rc, wo, lwt, (unsigned)(j * (8 * 3584))); });
    BARRIER();
    colsum(0, 0);
    STAMP(2);
    sfor<4, 7>([&](auto J) { constexpr int j = decltype(J)::v; dma_add<0, 65536 + j * 1024, 128>(rc, wo, lwt, (unsigned)(j * (8 * 3584))); });
    u32x4 fm[16];
#pragma unroll
    for (int i = 0; i < 16; ++i) fm[i] = lds128(L0 + i * 1024 + l * 16);
#pragma unroll
    for (int i = 0; i < 16; ++i) PIN(fm[i]);
    waitv<0, 0>();
    sfor<0, 4>([&](auto I) { constexpr int i = decltype(I)::v; dma_add<0, i * 1024, 0>(rfr, fo, lfb, (unsigned)(i * 28800)); });
    BARRIER();
    colsum(65536, 1);
    sfor<0, 4>([&](auto I) { constexpr int i = decltype(I)::v; dma_add<0, 32768 + i * 1024, 0>(rfi, fo, lfb, (unsigned)(i * 28800)); });
    waitv<0, 0>();
    BARRIER();
    STAMP(3);
    const int c = 28 * w + (c31 < 28 ? c31 : 27);
    f32x2 wpr[8], wpi[8], wmr[8], wmi[8];
    {
      const unsigned fa0 = L0 + L_FILT + 4 * h * 512 + ((2 * k1) & 15) + (c ? c : 224) * 2;
      unsigned short t[4][16];
#pragma unroll
      for (int i = 0; i < 16; ++i)
#pragma unroll
        for (int j = 0; j < 4; ++j) t[j][i] = *(LAS const unsigned short*)(uintptr_t)(fa0 + ((i & 3) + 8 * (i >> 2)) * 512 + j * 16384);
#pragma unroll
      for (int i = 0; i < 8; ++i) {
        const f32x2 p0 = {bflo(t[0][2 * i]), bflo(t[0][2 * i + 1])}, p1 = {bflo(t[1][2 * i]), bflo(t[1][2 * i + 1])};
        const f32x2 q0 = {bflo(t[2][2 * i]), bflo(t[2][2 * i + 1])}, q1 = {bflo(t[3][2 * i]), bflo(t[3][2 * i + 1])};
        wpr[i] = p0 + p1; wpi[i] = q0 + q1; wmr[i] = p0 - p1; wmi[i] = q0 - q1;
      }
      if (w == 0) {
        const float sc = c ? 1.0f : 0.5f;
#pragma unroll
        for (int i = 0; i < 8; ++i) { wpr[i] *= sc; wpi[i] *= sc; wmr[i] *= sc; wmi[i] *= sc; }
      }
#pragma unroll
      for (int i = 0; i < 8; ++i) { PIN(wpr[i]); PIN(wpi[i]); PIN(wmr[i]); PIN(wmi[i]); }
    }
    const unsigned zst = L0 + 98304 + w * 4096;
    unsigned zw[4], zr = zst + lr * 128 + ((lc ^ lr) << 4);
    unsigned zg = (unsigned)((8 * (lr >> 2) + (lr & 3)) * Z_G + lc * 16), ma = L0 + 16384 + l * 16;
    unsigned evo = (unsigned)(WS_E + k1 * E_K1 + h * 2 * E_PN + c * 16), eco = (unsigned)(WS_EC + (28 * h + (c >> 4)) * 2048 + (k1 & 15) * 128 + (c & 15) * 8 + (k1 >> 4) * 4);
#pragma unroll
    for (int i = 0; i < 4; ++i) {
      zw[i] = zst + c31 * 128 + (((4 * (i >> 1) + 2 * h + (i & 1)) ^ (c31 & 7)) << 4);
      PIN(zw[i]);
    }
    PIN(zr); PIN(zg); PIN(ma); PIN(evo); PIN(eco);
    STAMP(4);
    if (w == 0) poll_flags(rw, (unsigned)(WS_HINT + k1 * 128), tag);
    asm volatile("s_barrier" ::: "memory");
    {
      u32x4 ef[8];
      unsigned ec[8];
      STAMP(5);
      sfor<0, 8>([&](auto S) {
        constexpr int s = decltype(S)::v;
        ld128_sc1<0, 0>(ef[s], rw, evo, (unsigned)(((s >> 2) + 4 * (s & 3)) * E_PN));
        ld32s_sc1<0, 0>(ec[s], rw, eco, (unsigned)(((s >> 2) + 4 * (s & 3)) * 28672));
      });
      f32x16 ac[2];
      f32x2 pr[2][8], pi[2][8];
      u32x4 mi[12];
      auto pair = [&](const f32x16& v, int i) { return f32x2{v[2 * i], v[2 * i + 1]}; };
      sfor<0, 2>([&](auto HF) {
        constexpr int hf = decltype(HF)::v;
#pragma unroll
        for (int i = 0; i < 16; ++i) { ac[0][i] = 0.f; ac[1][i] = 0.f; }
        sfor<0, 4>([&](auto S) {
          constexpr int s = 4 * hf + decltype(S)::v;
          waitv<14 - 2 * s, 0>();
          PIN(ef[s]); PIN(ec[s]);
          {
            if (__builtin_expect(mism((tag + ef[s].x + ef[s].y) + ef[s].z + ef[s].w, ec[s]) != 0, 0))
            { RETRY(16 + s); reload_e(ef[s], ec[s], rw, evo, eco, (unsigned)(((s >> 2) + 4 * (s & 3)) * E_PN), tag); }
          }
          ac[0] = mfma32(fm[2 * s], ef[s], ac[0]);
          ac[1] = mfma32(fm[2 * s + 1], ef[s], ac[1]);
          SB();
        });
        if constexpr (hf == 1) {
#pragma unroll
          for (int i = 0; i < 12; ++i) mi[i] = lds128(ma + i * 1024);
          st32_sc1<0>(rw, tid >= 480 ? (unsigned)(WS_FLAGZ + k1 * 128 + (tid - 480) * 4) : OOB, 0u, tag);
          STAMP(6);
        }
#pragma unroll
        for (int par = 0; par < 2; ++par) {
          const f32x2* ur = (par ^ hf) ? wmr : wpr;
          const f32x2* ui = (par ^ hf) ? wmi : wpi;
#pragma unroll
          for (int i = 0; i < 8; ++i) {
            if (hf == 0) { pr[par][i] = pair(ac[0], i) * ur[i]; pi[par][i] = pair(ac[0], i) * ui[i]; }
            else { pr[par][i] += pair(ac[0], i) * ur[i]; pi[par][i] += pair(ac[0], i) * ui[i]; }
          }
          SB();
#pragma unroll
          for (int i = 0; i < 8; ++i) { pr[par][i] -= pair(ac[1], i) * ui[i]; pi[par][i] += pair(ac[1], i) * ur[i]; }
          SB();
          if constexpr (hf == 1) {
            f32x16 zre, zim;
#pragma unroll
            for (int i = 0; i < 16; ++i) { zre[i] = 0.f; zim[i] = 0.f; }
#pragma unroll
            for (int s2 = 0; s2 < 2; ++s2) {
              const f32x2 *a_ = pr[par] + 4 * s2, *b_ = pi[par] + 4 * s2;
              const u32x4 fr = {pk2(a_[0][0], a_[0][1]), pk2(a_[1][0], a_[1][1]), pk2(a_[2][0], a_[2][1]), pk2(a_[3][0], a_[3][1])};
              const u32x4 fi = {pk2(b_[0][0], b_[0][1]), pk2(b_[1][0], b_[1][1]), pk2(b_[2][0], b_[2][1]), pk2(b_[3][0], b_[3][1])};
              const u32x4* mm = mi + (par * 2 + s2) * 3;
              zre = mfma32(fr, mm[0], zre);
              zim = mfma32(fr, mm[1], zim);
              zre = mfma32(fi, mm[2], zre);
              zim = mfma32(fi, mm[0], zim);
            }
            SB();
            u32x2 zp[2][4];
#pragma unroll
            for (int g4 = 0; g4 < 4; ++g4) {
              zp[0][g4] = u32x2{pk2(zre[4 * g4], zre[4 * g4 + 1]), pk2(zre[4 * g4 + 2], zre[4 * g4 + 3])};
              zp[1][g4] = u32x2{pk2(zim[4 * g4], zim[4 * g4 + 1]), pk2(zim[4 * g4 + 2], zim[4 * g4 + 3])};
              if (g4 < 3) {
                *(LAS u32x2*)(uintptr_t)(zw[g4 >> 1] + (g4 & 1) * 8) = zp[0][g4];
                *(LAS u32x2*)(uintptr_t)(zw[2 + (g4 >> 1)] + (g4 & 1) * 8) = zp[1][g4];
              }
            }
            {
              unsigned t3 = (zp[0][3].x + zp[0][3].y + zp[1][3].x) + zp[1][3].y;
              unsigned sm = csum8(h ? 0u : t3, cat(zp[0][0], zp[0][1]), cat(zp[1][0], zp[1][1]));
              sm = (sm + zp[0][2].x + zp[0][2].y) + zp[1][2].x + zp[1][2].y;
              auto sw = __builtin_amdgcn_permlane32_swap(sm, sm, false, false);
              const unsigned a0 = sw[0], a1 = sw[1], V = tag - a0 - a1, V2 = (V & 0x40004000u) >> 1;
              *(LAS u32x2*)(uintptr_t)(zw[1] + 8) = h ? u32x2{V & ~0x40004000u, V2} : zp[0][3];
              *(LAS u32x2*)(uintptr_t)(zw[3] + 8) = h ? u32x2{V2, 0u} : zp[1][3];
            }
            u32x4 v[4];
#pragma unroll
            for (int i = 0; i < 4; ++i) v[i] = lds128(zr + i * 1024);
            const unsigned zb = (unsigned)(WS_Z + par * 4 * Z_G + w * 4096 + k1 * 128);
#pragma unroll
            for (int i = 0; i < 4; ++i) st128_sc1<0, 0>(rw, zg, zb + i * (16 * Z_G), v[i]);
            SB();
          }
        }
      });
      STAMP(7);
      asm volatile("gf_code_end:");
    }
  }
}

extern "C" void kernel_launch(void* const* d_in, const int* in_sizes, int n_in, void* d_out, int out_size, void* d_ws,
                              size_t ws_size, hipStream_t stream) {
  (void)in_sizes; (void)n_in; (void)out_size; (void)ws_size;
  (void)hipFuncSetAttribute((const void*)gf_glu_kernel, hipFuncAttributeMaxDynamicSharedMemorySize, L_TOTAL);
  gf_glu_kernel<<<256, 512, L_TOTAL, stream>>>((const char*)d_in[0], (const char*)d_in[1], (const char*)d_in[2],
                                               (const char*)d_in[4], (const char*)d_in[5], (const char*)d_in[6],
                                               (char*)d_out, (char*)d_ws);
}
```

```cpp
#include <hip/hip_runtime.h>
#include <hip/hip_bf16.h>
#include <stdint.h>

typedef __bf16 bf16x8 __attribute__((ext_vector_type(8)));
typedef __bf16 bf16x2 __attribute__((ext_vector_type(2)));
typedef short s16x4 __attribute__((ext_vector_type(4)));
typedef float f32x2 __attribute__((ext_vector_type(2)));
typedef float f32x4 __attribute__((ext_vector_type(4)));
typedef float f32x16 __attribute__((ext_vector_type(16)));
typedef unsigned u32x4 __attribute__((ext_vector_type(4)));
typedef unsigned u32x2 __attribute__((ext_vector_type(2)));
#define LAS __attribute__((address_space(3)))
#define DI __device__ __forceinline__

constexpr unsigned WS_EPOCH = 0;
constexpr unsigned WS_STAT = 65536;
constexpr unsigned WS_CS = 131072;
constexpr unsigned WS_FLAGZ = 196608;
constexpr unsigned WS_HINT = 229376;
constexpr unsigned WS_EC = 524288;
constexpr unsigned WS_E = 1u << 20;
constexpr unsigned WS_Z = 3u << 20;
constexpr unsigned WS_BYTES = 14u << 20;
constexpr unsigned WS_H = 0x520000;
constexpr unsigned HBLK = 4608;
constexpr unsigned E_PN = 3584, E_K1 = 16 * 3584 + 256, Z_G = 32768 + 640;

constexpr unsigned L_AUX = 131072;
constexpr unsigned L_PRIV = L_AUX;
constexpr unsigned L_ZOK = L_AUX + 3072;
constexpr unsigned L_EB = L_AUX + 3584;
constexpr unsigned L_PAUX = L_AUX + 12288;
constexpr unsigned L_P1 = L_AUX + 28672;
constexpr unsigned L_TOTAL = 163840;

constexpr unsigned SPIN_CAP = 1u << 17;

template <int N> struct IC { static constexpr int v = N; };
template <int B, int E, class Fn> DI void sfor(Fn&& f) {
  if constexpr (B < E) { f(IC<B>{}); sfor<B + 1, E>(f); }
}

DI unsigned pk2(float a, float b) { f32x2 t = {a, b}; bf16x2 p = __builtin_convertvector(t, bf16x2); return __builtin_bit_cast(unsigned, p); }
DI bf16x8 asbf(u32x4 v) { return __builtin_bit_cast(bf16x8, v); }
DI float asf(unsigned u) { return __builtin_bit_cast(float, u); }
DI unsigned asu(float f) { return __builtin_bit_cast(unsigned, f); }
DI float bflo(unsigned u) { return asf(u << 16); }
DI float bfhi(unsigned u) { return asf(u & 0xffff0000u); }
DI f32x4 mfma16(u32x4 a, u32x4 b, f32x4 c) { return __builtin_amdgcn_mfma_f32_16x16x32_bf16(asbf(a), asbf(b), c, 0, 0, 0); }
DI f32x16 mfma32(u32x4 a, u32x4 b, f32x16 c) { return __builtin_amdgcn_mfma_f32_32x32x16_bf16(asbf(a), asbf(b), c, 0, 0, 0); }
DI void mma(f32x4& c, const u32x4& a, const u32x4& b) { asm volatile("v_mfma_f32_16x16x32_bf16 %0, %1, %2, %0" : "+v"(c) : "v"(a), "v"(b)); }
DI void mma0(f32x4& c, const u32x4& a, const u32x4& b) { asm volatile("v_mfma_f32_16x16x32_bf16 %0, %1, %2, 0" : "=&v"(c) : "v"(a), "v"(b)); }
DI void mmac(f32x4& d, const u32x4& a, const u32x4& b, const f32x4& c) { asm volatile("v_mfma_f32_16x16x32_bf16 %0, %1, %2, %3" : "=&v"(d) : "v"(a), "v"(b), "v"(c)); }
DI float dot2(unsigned a, unsigned b, float c) {
  return __builtin_amdgcn_fdot2_f32_bf16(__builtin_bit_cast(bf16x2, a), __builtin_bit_cast(bf16x2, b), c, false);
}
DI float sum8(u32x4 v, float c) { unsigned a = v.x, b = v.y, d = v.z, e = v.w; const unsigned o = 0x3f803f80u;
  return dot2(e, o, dot2(d, o, dot2(b, o, dot2(a, o, c)))); }
DI float sq8(u32x4 v, float c) { unsigned a = v.x, b = v.y, d = v.z, e = v.w;
  return dot2(e, e, dot2(d, d, dot2(b, b, dot2(a, a, c)))); }
template <int CTRL> DI float dpp(float v) { return asf((unsigned)__builtin_amdgcn_update_dpp(0, (int)asu(v), CTRL, 0xf, 0xf, true)); }
DI u32x4 lds128(unsigned a) { return *(LAS const u32x4*)(uintptr_t)a; }
DI f32x4 lds128f(unsigned a) { return *(LAS const f32x4*)(uintptr_t)a; }
DI u32x2 ldstr1(unsigned a) { return __builtin_bit_cast(u32x2, __builtin_amdgcn_ds_read_tr16_b64_v4i16((LAS s16x4*)(uintptr_t)a)); }
DI u32x4 cat(u32x2 lo, u32x2 hi) { return __builtin_shufflevector(lo, hi, 0, 1, 2, 3); }
DI u32x4 ldstr(unsigned a0, unsigned a1) {
  s16x4 lo = __builtin_amdgcn_ds_read_tr16_b64_v4i16((LAS s16x4*)(uintptr_t)a0);
  s16x4 hi = __builtin_amdgcn_ds_read_tr16_b64_v4i16((LAS s16x4*)(uintptr_t)a1);
  return __builtin_bit_cast(u32x4, __builtin_shufflevector(lo, hi, 0, 1, 2, 3, 4, 5, 6, 7));
}
typedef u32x4 rsrc_t;
DI rsrc_t mk_rsrc(const void* p, unsigned bytes) {
  const unsigned long long a = (unsigned long long)p;
  rsrc_t d = {(unsigned)a, (unsigned)(a >> 32) & 0xffffu, bytes, 0x00020000u};
  asm volatile("" : "+s"(d));
  return d;
}
template <int ID, int LIMM, int MUL> DI void dma_mul(rsrc_t rs, unsigned voff, unsigned lbase, unsigned k) {
  unsigned t;
  asm volatile("s_add_u32 m0, %3, %5\n\ts_mul_i32 %0, %4, %6\n\tbuffer_load_dwordx4 %1, %2, %0 offen lds ; OP %7"
               : "=&s"(t) : "v"(voff), "s"(rs), "s"(lbase), "s"(k), "i"(LIMM), "i"(MUL), "i"(ID) : "memory", "scc");
}
template <int ID, int LIMM, int ADD> DI void dma_add(rsrc_t rs, unsigned voff, unsigned lbase, unsigned k) {
  unsigned t;
  asm volatile("s_add_u32 m0, %3, %5\n\ts_add_u32 %0, %4, %6\n\tbuffer_load_dwordx4 %1, %2, %0 offen lds ; OP %7"
               : "=&s"(t) : "v"(voff), "s"(rs), "s"(lbase), "s"(k), "i"(LIMM), "i"(ADD), "i"(ID) : "memory", "scc");
}
template <int ID, int LIMM, int ADD> DI void dma_add_sc1(rsrc_t rs, unsigned voff, unsigned lbase, unsigned k) {
  unsigned t;
  asm volatile("s_add_u32 m0, %3, %5\n\ts_add_u32 %0, %4, %6\n\tbuffer_load_dwordx4 %1, %2, %0 offen sc1 lds ; OP %7"
               : "=&s"(t) : "v"(voff), "s"(rs), "s"(lbase), "s"(k), "i"(LIMM), "i"(ADD), "i"(ID) : "memory", "scc");
}
template <int ID, int IMM> DI void st128_sc1(rsrc_t rs, unsigned voff, unsigned soff, u32x4 v) {
  asm volatile("buffer_store_dwordx4 %0, %1, %2, %3 offen offset:%4 sc1 ; OP %5\n\ts_nop 1" ::"v"(v), "v"(voff), "s"(rs), "Is"(soff), "i"(IMM), "i"(ID) : "memory");
}
template <int ID> DI void st64_sc1(rsrc_t rs, unsigned voff, unsigned soff, u32x2 v) {
  asm volatile("buffer_store_dwordx2 %0, %1, %2, %3 offen sc1 ; OP %4" ::"v"(v), "v"(voff), "s"(rs), "Is"(soff), "i"(ID) : "memory");
}
template <int ID> DI void st32_sc1(rsrc_t rs, unsigned voff, unsigned soff, unsigned v) {
  asm volatile("buffer_store_dword %0, %1, %2, %3 offen sc1 ; OP %4" ::"v"(v), "v"(voff), "s"(rs), "Is"(soff), "i"(ID) : "memory");
}
template <int ID, int IMM> DI void ld128_sc1(u32x4& dst, rsrc_t rs, unsigned voff, unsigned soff) {
  asm volatile("buffer_load_dwordx4 %0, %1, %2, %3 offen offset:%4 sc1 ; OP %5" : "=v"(dst) : "v"(voff), "s"(rs), "Is"(soff), "i"(IMM), "i"(ID) : "memory");
}
template <int ID, int IMM> DI void ld128(u32x4& dst, rsrc_t rs, unsigned voff, unsigned soff) {
  asm volatile("buffer_load_dwordx4 %0, %1, %2, %3 offen offset:%4 ; OP %5" : "=v"(dst) : "v"(voff), "s"(rs), "Is"(soff), "i"(IMM), "i"(ID) : "memory");
}
template <int ID, int IMM> DI void ld32s_sc1(unsigned& dst, rsrc_t rs, unsigned voff, unsigned soff) {
  asm volatile("buffer_load_dword %0, %1, %2, %3 offen offset:%4 sc1 ; OP %5" : "=v"(dst) : "v"(voff), "s"(rs), "s"(soff), "i"(IMM), "i"(ID) : "memory");
}
template <int ID> DI void ld32_plain(unsigned& dst, rsrc_t rs, unsigned voff) {
  asm volatile("buffer_load_dword %0, %1, %2, 0 offen ; OP %3" : "=v"(dst) : "v"(voff), "s"(rs), "i"(ID) : "memory");
}
template <int ID> DI void ld32_sc1(unsigned& dst, rsrc_t rs, unsigned voff) {
  asm volatile("buffer_load_dword %0, %1, %2, 0 offen sc1 ; OP %3" : "=v"(dst) : "v"(voff), "s"(rs), "i"(ID) : "memory");
}
template <int N, int NEED> DI void waitv() { asm volatile("s_waitcnt vmcnt(%0) ; NEED %1" ::"i"(N), "i"(NEED) : "memory"); }
template <int NEED> DI void need() { asm volatile("; NEED %0" ::"i"(NEED) : "memory"); }
#define BARRIER() asm volatile("s_waitcnt lgkmcnt(0)\n\ts_barrier" ::: "memory")
#define SB() __builtin_amdgcn_sched_barrier(0)
#define ONLY(g) asm volatile("; ONLY " #g ::: "memory")
DI void keep(const f32x4& a, const f32x4& b) { asm volatile("" ::"v"(a), "v"(b)); }
template <class T> DI void PINS(T& x) { asm volatile("" : "+s"(x)); }
DI float opq(float x) { asm volatile("" : "+v"(x)); return x; }
template <class T> DI void PIN(T& x) { asm volatile("" : "+v"(x)); }

DI void poll_flags(rsrc_t rs, unsigned voff, unsigned tag) {
#pragma nounroll
  for (unsigned spins = 0; spins < SPIN_CAP; ++spins) {
    unsigned got;
    asm volatile("buffer_load_dword %0, %1, %2, 0 offen sc1\n\ts_waitcnt vmcnt(0)" : "=v"(got) : "v"(voff), "s"(rs) : "memory");
    if (__all(got == tag)) break;
    __builtin_amdgcn_s_sleep(3);
  }
}
DI void poll_granules(u32x4& g, rsrc_t rs, unsigned voff, unsigned tag) {
#pragma nounroll
  for (unsigned spins = 0; spins < SPIN_CAP; ++spins) {
    __builtin_amdgcn_s_sleep(4);
    asm volatile("buffer_load_dwordx4 %0, %1, %2, 0 offen sc1\n\ts_waitcnt vmcnt(0)" : "=&v"(g) : "v"(voff), "s"(rs) : "memory");
    if (__all(g.y == tag && g.w == tag)) break;
  }
}
DI unsigned csum8(unsigned t, const u32x4& a, const u32x4& b) { return ((t + a.x + a.y) + a.z + a.w) + ((b.x + b.y + b.z) + b.w); }
DI void reload_h(u32x4* d, unsigned* ck, rsrc_t rs, unsigned vo, unsigned vc, unsigned so, unsigned tag) {
#pragma nounroll
  for (unsigned spins = 0; spins < SPIN_CAP; ++spins) {
    __builtin_amdgcn_s_sleep(8);
    asm volatile("buffer_load_dwordx4 %0, %6, %8, %9 offen sc1\n\tbuffer_load_dwordx4 %1, %6, %8, %9 offen offset:1024 sc1\n\t"
                 "buffer_load_dwordx4 %2, %6, %8, %9 offen offset:2048 sc1\n\tbuffer_load_dwordx4 %3, %6, %8, %9 offen offset:3072 sc1\n\t"
                 "buffer_load_dword %4, %7, %8, %9 offen sc1\n\tbuffer_load_dword %5, %7, %8, %9 offen offset:256 sc1\n\ts_waitcnt vmcnt(0)"
                 : "=&v"(d[0]), "=&v"(d[1]), "=&v"(d[2]), "=&v"(d[3]), "=&v"(ck[0]), "=&v"(ck[1])
                 : "v"(vo), "v"(vc), "s"(rs), "s"(so) : "memory");
    if (__all(csum8(tag, d[0], d[1]) == ck[0] && csum8(tag, d[2], d[3]) == ck[1])) break;
  }
}
DI void reload_e(u32x4& d, unsigned& ck, rsrc_t rs, unsigned vo, unsigned vc, unsigned so, unsigned tag) {
#pragma nounroll
  for (unsigned spins = 0; spins < SPIN_CAP; ++spins) {
    __builtin_amdgcn_s_sleep(10);
    asm volatile("buffer_load_dwordx4 %0, %2, %4, %5 offen sc1\n\tbuffer_load_dword %1, %3, %4, %6 offen sc1\n\ts_waitcnt vmcnt(0)"
                 : "=&v"(d), "=&v"(ck) : "v"(vo), "v"(vc), "s"(rs), "s"(so), "s"(so * 8) : "memory");
    if (__all((tag + d.x + d.y) + d.z + d.w == ck)) break;
  }
}
DI unsigned long long mism(unsigned v, unsigned tag) { unsigned long long m_ = __builtin_amdgcn_ballot_w64(v != tag); asm volatile("" : "+s"(m_)); return m_; }

#define STAMP(i)
#define RETRY(i)

constexpr int kTileOfX[7] = {0, 2, 3, 5, 6, 8, 9};
constexpr int kTileOfW[14] = {1, 4, 7, 10, 11, 12, 13, 14, 15, 16, 17, 18, 19, 20};
constexpr int kTileKind[21] = {0, 1, 0, 0, 1, 0, 0, 1, 0, 0, 1, 1, 1, 1, 2, 2, 2, 2, 2, 2, 2};
constexpr int kTileIdx[21] = {0, 0, 1, 2, 1, 3, 4, 2, 5, 6, 3, 4, 5, 6, 0, 1, 2, 3, 4, 5, 6};
constexpr int kSegX0[4] = {0, 1, 3, 5}, kSegNX[4] = {1, 2, 2, 2};
constexpr int kIssue0[5] = {5, 8, 10, 13, 16};
constexpr int tile_of_x(int j) { return kTileOfX[j]; }
constexpr int tile_of_w(int s) { return kTileOfW[s]; }
constexpr int tile_kind(int T) { return kTileKind[T]; }
constexpr int tile_idx(int T) { return kTileIdx[T]; }
constexpr unsigned piece_loc(int g, int P) {
  constexpr unsigned pr[8] = {L_PAUX, 5 * 16384, 6 * 16384, 7 * 16384, 0, 16384, 2 * 16384, 3 * 16384};
  return pr[(g ? P % 8 : P % 6)];
}
enum { OP_EP = 29, OP_P = 30, OP_GS = 50, OP_GC1, OP_GC2, OP_E, OP_H1, OP_H2, OP_POLL, OP_XRES, OP_FL, OP_OPT = 59, OP_HE = 60, OP_HL = 70, OP_Z = 80 };

__global__ __launch_bounds__(512, 2) void gf_glu_kernel(const char* x, const char* wr, const char* wi, const char* wg,
                                                        const char* wu, const char* wd, char* out, char* ws) {
  extern __shared__ __attribute__((aligned(16))) char smem[];
  STAMP(0);
  const int tid = threadIdx.x;
  const int w = __builtin_amdgcn_readfirstlane(tid >> 6);
  const int l = tid & 63, r = l & 15, q = l >> 4;
  const int b = blockIdx.x, xcd = b & 7, bi = b >> 3;
  const unsigned L0 = (unsigned)(uintptr_t)(LAS char*)smem;

  unsigned code_touch;
  {
    unsigned long long pc;
    unsigned code_bytes;
    asm volatile("s_getpc_b64 %0\ngf_code_begin:\n\ts_mov_b32 %1, gf_code_end-gf_code_begin" : "=s"(pc), "=s"(code_bytes));
    const char* pcb = (const char*)(pc & ~127ull);
    unsigned off = (unsigned)((bi * 16 + w * 2 + (l & 1)) * 128);
    off = off < code_bytes ? off : 0;
    asm volatile("global_load_dword %0, %1, %2" : "=v"(code_touch) : "v"(off), "s"(pcb) : "memory");
  }
  unsigned epoch;
#define LOAD_EPOCH() asm volatile("global_load_dword %0, %1, %2 ; OP %3" : "=v"(epoch) : "v"(0u), "s"(ws), "i"(OP_EP) : "memory")
#define GET_TAG() PIN(epoch); PIN(code_touch); const unsigned tag = (unsigned)__builtin_amdgcn_readfirstlane(epoch) * 0x9E3779B1u + 0x7F4A7C15u
  constexpr unsigned OOB = 0x40000000u;

  if (__builtin_expect(bi >= 4, 1)) {
    const int bj = bi - 4;
    const int pn = 4 * (xcd & 3) + (bj & 3), tt = 7 * (xcd >> 2) + (bj >> 2);
    int rot = (bj >> 2) + 2 * (bj & 3) + (xcd & 1);
    rot = rot >= 7 ? rot - 7 : rot;
    const int m = w & 3, nh = w >> 2;
    const int rt = 4 * xcd + (bj & 3), ct = bj >> 2;
    unsigned kx[7];
    kx[0] = (unsigned)(rot * 128);
    unsigned xoff[2], woff, doff;
    const int lr = l >> 3, lc = l & 7;
    {
      const int R = 16 * (w & 1) + lr;
      const int n2 = (pn & 1) + 8 * (pn >> 1) + 2 * (w >> 1);
      const unsigned rb = (unsigned)((64 * R + n2) * 896), cx = (unsigned)((lc ^ (lr >> 1)) << 4);
      xoff[0] = rb + cx;
      xoff[1] = rb + 8 * 64 * 896 + (cx ^ 64);
      const int swz = ((lr >> 1) & 1) | ((w & 1) << 1);
      woff = (unsigned)((8 * w + lr) * 3584 + tt * 256 + ((lc ^ (swz << 1)) << 4));
    }
    unsigned lw1 = L0 + w * 1024, lw2 = L0 + w * 2048;
    PINS(lw1); PINS(lw2); PINS(kx[0]); PIN(xoff[0]); PIN(xoff[1]); PIN(woff);
    SB();
    const rsrc_t rx = mk_rsrc(x, 2048 * 896), rg = mk_rsrc(wg, 448 * 3584), ru = mk_rsrc(wu, 448 * 3584);
    auto issue1 = [&](auto TI, auto HI) {
      constexpr int T = decltype(TI)::v, h = decltype(HI)::v;
      constexpr int kind = tile_kind(T), sl = (T & 7) * 16384;
      if constexpr (T == 14 && h == 0) { woff += 128; PIN(woff); }
      if constexpr (kind == 0) dma_mul<T, sl + h * 1024, 1>(rx, xoff[h], lw2, kx[tile_idx(T)]);
      else dma_mul<T, sl + h * 8192, 1792>(h ? ru : rg, woff, lw1, kx[tile_idx(T)]);
    };
    auto issue = [&](auto TI) { issue1(TI, IC<0>{}); issue1(TI, IC<1>{}); };
    issue(IC<0>{});
    issue(IC<1>{});
    LOAD_EPOCH();
    STAMP(1);
    SB();
#pragma unroll
    for (int j = 1; j < 7; ++j) { kx[j] = kx[j - 1] == 768 ? 0 : kx[j - 1] + 128; PINS(kx[j]); }
    SB();

    {
      int c = 16 * tt + 8 * nh + (r >> 1);
      const float ph = (r & 1) ? 0.25f : 0.0f;
      c = c ? c : 224;
      const float cfl = (float)c, cf = cfl * (1.0f / 448.0f);
      const float k2 = 2.0f * __builtin_amdgcn_cosf(cf);
#pragma unroll
      for (int it = 0; it < 4; ++it) {
        const int ks = m + 4 * it;
        if (it < 3 || m < 2) {
          const unsigned kk = (m & 2) ? kx[it < 3 ? 2 * it + 1 : 0] : kx[2 * it];
          const float t = cfl * (float)(int)((kk >> 1) + 32 * (m & 1) + 8 * q);
          const float rem = __builtin_fmaf(-448.0f, __builtin_floorf(t * (1.0f / 448.0f)), t);
          const float rev = __builtin_fmaf(rem, 1.0f / 448.0f, ph);
          float v[8];
          v[0] = __builtin_amdgcn_cosf(rev);
          v[1] = __builtin_amdgcn_cosf(rev + cf);
#pragma unroll
          for (int e = 2; e < 8; ++e) v[e] = __builtin_fmaf(k2, v[e - 1], -v[e - 2]);
          *(LAS u32x4*)(uintptr_t)(L0 + L_AUX + (nh * 14 + ks) * 1024 + l * 16) =
              u32x4{pk2(v[0], v[1]), pk2(v[2], v[3]), pk2(v[4], v[5]), pk2(v[6], v[7])};
        } else {
          const int k1 = 16 * (m & 1) + r;
          float v[4];
#pragma unroll
          for (int e = 0; e < 4; ++e)
            v[e] = __builtin_amdgcn_cosf((float)((k1 * (4 * q + e)) & 31) * (1.0f / 32.0f) - (nh ? 0.25f : 0.0f));
          const unsigned a0 = pk2(v[0], v[1]), a1 = pk2(v[2], v[3]), sg = (r & 1) ? 0x80008000u : 0u;
          *(LAS u32x4*)(uintptr_t)(L0 + L_P1 + (2 * nh + (m & 1)) * 1024 + l * 16) = u32x4{a0, a1, a0 ^ sg, a1 ^ sg};
        }
      }
    }
    unsigned xa[2][2], wa[2][2];
    {
      const int R = 32 * m + r;
      xa[0][0] = L0 + R * 128 + ((q ^ ((R >> 1) & 7)) << 4);
      xa[0][1] = xa[0][0] ^ 64;
      const int qq = r >> 2, pp = r & 3;
      const int swz = ((qq >> 1) & 1) | ((q & 1) << 1);
      wa[0][0] = L0 + (8 * q + qq) * 128 + (((2 * nh) ^ swz) << 5) + pp * 8;
      wa[0][1] = wa[0][0] ^ 32;
#pragma unroll
      for (int i = 0; i < 2; ++i) { xa[1][i] = xa[0][i] + 65536; wa[1][i] = wa[0][i] + 65536; PIN(xa[1][i]); PIN(wa[1][i]); PIN(xa[0][i]); PIN(wa[0][i]); }
    }
    unsigned l16 = l * 16, aux16 = L0 + L_AUX + l * 16, fad = aux16 + nh * 14336;
    PIN(l16); PIN(aux16); PIN(fad);
    STAMP(2);
    SB();
    const rsrc_t rd = mk_rsrc(wd, 1792 * 896), rw = mk_rsrc(ws, WS_BYTES);
    const rsrc_t rtch = mk_rsrc(nh ? wd : ((m & 1) ? wi : wr), nh ? 1792 * 896 : 921600);
    const int g1 = 4 * pn + m;
    unsigned pso[9], hso[9];
    unsigned hst_d, hst_c, ecs, gs_off, gc_off, pq;
    const unsigned priv = L0 + L_PRIV + w * 256;
    auto step_setup = [&](int g, int s) {
      const int ps = s >= 3 + g;
      int v = (s < 3 ? 8 * s + w : m + (g == 0 ? 8 * (s - 3) : (s == 3 ? 24 : (s == 4 ? 4 : 4 * s - 8))));
      v = v >= 28 ? v - 28 : v;
      pso[s] = (unsigned)((128 * (v >> 1) + 64 * ps + 32 * (v & 1)) * 896);
      hso[s] = (unsigned)(WS_H + (rt * 56 + 28 * ps + v) * HBLK);
      PINS(pso[s]); PINS(hso[s]);
    };
    constexpr unsigned S_ = 16384;
    auto issue_p1 = [&](auto GI, auto PI, auto HI) {
      constexpr int P = decltype(PI)::v, h = decltype(HI)::v;
      dma_add<OP_P + P, piece_loc(decltype(GI)::v, P) + h * 1024, (16 * (P & 1) + 8 * h) * 896>(rd, doff, lw2, pso[P >> 1]);
    };

    u32x4 xf[14][2], ff[4];
    u32x2 fa_[4][2], fb_[4][2];
    f32x4 accD[2], acc[2][4][2];
    float mrow[2], r2row[2];
    u32x4 gran, ebv;
    unsigned pollv;
    u32x4 hb[4][4];
    {
      const unsigned hst = WS_H + (unsigned)((g1 >> 1) * 56 + 2 * tt + nh) * HBLK;
      hst_d = hst + (m & 1) * 2048; hst_c = hst + (m & 1) * 256;
      ecs = (unsigned)(WS_EC + (pn * 14 + tt) * 2048);
      PINS(hst_d); PINS(hst_c); PINS(ecs);
    }
    SB();
    issue(IC<2>{});
    issue(IC<4>{});
    unsigned tag, tch, tch2, tch3, tch4, tco;
    f32x4 cs[4];
    unsigned hpk[2][4];
    auto h_part = [&](auto PS, int i2, int sub) {
      constexpr int ps = decltype(PS)::v;
      f32x4 h0 = ((acc[ps][sub][i2] - mrow[i2] * cs[sub]) * r2row[i2]) * (acc[ps][sub + 2][i2] - mrow[i2] * cs[sub + 2]);
      hpk[i2][2 * sub] = pk2(h0[0], h0[1]); hpk[i2][2 * sub + 1] = pk2(h0[2], h0[3]);
      PIN(hpk[i2][2 * sub]); PIN(hpk[i2][2 * sub + 1]);
    };
    auto h_store = [&](auto PS, auto I2) {
      constexpr int ps = decltype(PS)::v, i2 = decltype(I2)::v;
      st128_sc1<OP_H1 + ps, i2 * 1024>(rw, l16, hst_d + ps * (28 * HBLK), u32x4{hpk[i2][0], hpk[i2][1], hpk[i2][2], hpk[i2][3]});
    };
    unsigned hck[4][2], l4c, hsum;
    auto h_check_store = [&](auto PS) { st32_sc1<OP_H1 + decltype(PS)::v>(rw, l4c, hst_c + decltype(PS)::v * (28 * HBLK), hsum); };
    auto load_h1 = [&](auto SI, auto II) {
      constexpr int s = decltype(SI)::v, i = decltype(II)::v;
      if constexpr (i < 4) ld128_sc1<OP_HE + s, (i & 3) * 1024>(hb[s & 3][i & 3], rw, l16, hso[s]);
      else ld32s_sc1<OP_HE + s, (i & 1) * 256>(hck[s & 3][i & 1], rw, l4c, hso[s]);
    };
    u32x4 p1c[2], p1s[2], df;
    f32x4 rD[2], tc[2], ts[2];
    float rsr[1], dv[8], ev[4];
    unsigned dpk[4], ea, eo, ebo;
    auto hop = [&](auto OI) {
      constexpr int o = decltype(OI)::v, sub = o / 36, i2 = (o / 18) & 1, i = o % 18, e = i & 3;
      if constexpr (i < 4) dv[e] = opq(__builtin_fmaf(-mrow[i2], cs[sub][e], acc[0][sub][i2][e]));
      else if constexpr (i < 8) ev[e] = opq(__builtin_fmaf(-mrow[i2], cs[sub + 2][e], acc[0][sub + 2][i2][e]));
      else if constexpr (i < 12) dv[e] = opq(dv[e] * r2row[i2]);
      else if constexpr (i < 16) dv[e] = opq(dv[e] * ev[e]);
      else { hpk[i2][2 * sub + (i & 1)] = pk2(dv[2 * (i & 1)], dv[2 * (i & 1) + 1]); PIN(hpk[i2][2 * sub + (i & 1)]); }
    };
    u32x4 gf[2][2][4];
    float g_d[4], g_f[4], g_k[4], g_t[4], g_u[4], g_c[4][3], g_s[4][3], g_cq;
    unsigned g_mk;
    unsigned g_ny;
    auto gop = [&](auto OI) {
      constexpr int o = decltype(OI)::v, hv = o / 136, a = o & 3, dt = a, i = (o % 136) >> 2;
      if constexpr (hv == 1 && (i == 0 || i == 2 || i == 4 || i == 10)) { if constexpr (o == 136) g_cq = opq(g_cq + 28.0f); }
      else if constexpr (i == 0) g_d[a] = opq((float)(64 * ct + 16 * dt + r));
      else if constexpr (i == 1) g_t[a] = opq(g_d[a] * g_cq);
      else if constexpr (i == 2) g_f[a] = opq(g_d[a] * (1.0f / 448.0f));
      else if constexpr (i == 3) g_u[a] = opq(g_t[a] * (1.0f / 448.0f));
      else if constexpr (i == 4) g_k[a] = opq(__builtin_amdgcn_cosf(g_f[a]));
      else if constexpr (i == 5) g_u[a] = opq(__builtin_floorf(g_u[a]));
      else if constexpr (i == 6) g_t[a] = opq(__builtin_fmaf(-448.0f, g_u[a], g_t[a]));
      else if constexpr (i == 7) g_t[a] = opq(g_t[a] * (-1.0f / 448.0f));
      else if constexpr (i == 8) g_u[a] = opq(g_t[a] - g_f[a]);
      else if constexpr (i == 9) g_c[a][0] = opq(__builtin_amdgcn_cosf(g_t[a]));
      else if constexpr (i == 10) g_k[a] = opq(g_k[a] + g_k[a]);
      else if constexpr (i == 11) g_s[a][0] = opq(__builtin_amdgcn_sinf(g_t[a]));
      else if constexpr (i == 12) { unsigned p_ = pk2(g_c[a][0], g_s[a][0]); if constexpr (hv == 0) p_ ^= g_ny; PIN(p_); gf[hv][0][dt][0] = p_; }
      else if constexpr (i == 13) g_c[a][1] = opq(__builtin_amdgcn_cosf(g_u[a]));
      else if constexpr (i == 14) g_s[a][1] = opq(__builtin_amdgcn_sinf(g_u[a]));
      else if constexpr (i == 15) { unsigned p_ = pk2(g_c[a][1], g_s[a][1]); PIN(p_); gf[hv][0][dt][1] = p_; }
      else {
        constexpr int e = 2 + (i - 16) / 3, j = (i - 16) % 3;
        if constexpr (j == 0) g_c[a][e % 3] = opq(__builtin_fmaf(g_k[a], g_c[a][(e - 1) % 3], -g_c[a][(e - 2) % 3]));
        else if constexpr (j == 1) g_s[a][e % 3] = opq(__builtin_fmaf(g_k[a], g_s[a][(e - 1) % 3], -g_s[a][(e - 2) % 3]));
        else { unsigned p_ = pk2(g_c[a][e % 3], g_s[a][e % 3]); if constexpr (e >= 4) p_ &= g_mk; PIN(p_); gf[hv][e >> 2][dt][e & 3] = p_; }
      }
    };

    unsigned long long bad0, bad1;
    unsigned da[3][4];
    u32x2 af[2][4][2];
    auto rda1 = [&](int g, int s, int dt, int h) { af[s & 1][dt][h] = ldstr1(da[piece_loc(g, 2 * s + h) >> 16][dt] + (piece_loc(g, 2 * s + h) & 0xffff)); };
    auto pin_af = [&](int s) {
#pragma unroll
      for (int dt = 0; dt < 4; ++dt) { PIN(af[s & 1][dt][0]); PIN(af[s & 1][dt][1]); }
    };

    u32x4 ph[3][2], pc, psn;
    unsigned pz_off, zo, za[4], zva, ooff, hs[2];
    const int orow = tid >> 3;
    auto prep = [&](auto GI, auto SI, auto JI) {
      constexpr int g = decltype(GI)::v, s = decltype(SI)::v, j = decltype(JI)::v;
      u32x4* hfr = hb[s & 3];
      if constexpr (j == 0) { need<OP_HE + s>(); need<OP_P + 2 * s + 1>(); PIN(hfr[0]); PIN(hfr[1]); PIN(hfr[2]); PIN(hfr[3]); PIN(hck[s & 3][0]); PIN(hck[s & 3][1]); }
      if constexpr (j < 4) { rda1(g, s, j, 0); rda1(g, s, j, 1); }
      if constexpr (j >= 1 && j < 5) {
        constexpr int f = (j - 1) >> 1, o = 2 * ((j - 1) & 1);
        hs[0] = (j == 1 ? tag : hs[0]) + hfr[f][o] + hfr[f][o + 1];
        hs[1] = (j == 1 ? tag : hs[1]) + hfr[2 + f][o] + hfr[2 + f][o + 1];
        PIN(hs[0]); PIN(hs[1]);
      }
      if constexpr (j == 5) { bad0 = mism(hs[0], hck[s & 3][0]); bad1 = mism(hs[1], hck[s & 3][1]); }
      if constexpr (j == 7) { if (__builtin_expect((bad0 | bad1) != 0, 0)) { RETRY(s); reload_h(hfr, hck[s & 3], rw, l16, l4c, hso[s], tag); } }
    };

    unsigned pw0, pw1;
    auto side = [&](auto SI, auto KI) {
      constexpr int s = decltype(SI)::v, k = decltype(KI)::v;
      constexpr int G = 32 * s + k;
#define AT(ss, kk) (G == 32 * (ss) + (kk))
#define TILE(ss, kk, T, h) if constexpr (AT(ss, kk)) issue1(IC<T>{}, IC<h>{})
#define PIECE(ss, kk, P, h) if constexpr (AT(ss, kk)) issue_p1(IC<0>{}, IC<P>{}, IC<h>{})
      if constexpr (AT(0, 7)) issue(IC<3>{});
      TILE(0, 11, 5, 0); TILE(0, 14, 5, 1); TILE(0, 17, 6, 0); TILE(0, 19, 6, 1);
      if constexpr (s == 1 && k % 4 == 0) issue1(IC<7 + k / 8>{}, IC<(k / 4) & 1>{});
      if constexpr (s == 2 && k % 4 == 1) issue1(IC<10 + k / 8>{}, IC<(k >> 2) & 1>{});
      TILE(3, 3, 13, 0); TILE(3, 7, 13, 1); TILE(3, 17, 14, 0); TILE(3, 22, 14, 1);
      TILE(4, 4, 15, 0); TILE(4, 9, 15, 1); TILE(4, 15, 16, 0); TILE(5, 3, 16, 1);
      TILE(6, 10, 17, 0); TILE(6, 15, 17, 1); TILE(7, 2, 18, 0);
      TILE(7, 5, 18, 1); TILE(7, 9, 19, 0); TILE(7, 13, 19, 1); TILE(8, 1, 20, 0); TILE(8, 4, 20, 1);
      if constexpr (AT(5, 10)) { tco = (unsigned)(((bj * 8 + w) >> 4) * 64 * 896 + ((bj * 8 + w) * 28 + l) * 128); tco = OOB; PIN(tco); }
      if constexpr (AT(5, 12)) ld32_plain<OP_EP>(tch3, rd, tco);
      if constexpr (AT(6, 1)) { tco += 64 * 896; PIN(tco); }
      if constexpr (AT(8, 13)) ld32_plain<OP_EP>(tch4, rd, tco);
      if constexpr (AT(1, 13)) {
        const unsigned gw = (unsigned)((bj * 8 + xcd) * 4 + m);
        tco = l < (nh ? 14 : 17) ? ((nh ? gw * 14 : (gw >> 1) * 17) + l) * 128 : OOB; PIN(tco);
      }
      if constexpr (AT(3, 14)) ld32_plain<OP_EP>(tch, rtch, tco);
      PIECE(10, 5, 0, 0); PIECE(10, 12, 0, 1); PIECE(11, 0, 1, 0); PIECE(11, 3, 1, 1);
      PIECE(11, 6, 2, 0); PIECE(11, 9, 2, 1); PIECE(11, 12, 3, 0); PIECE(11, 15, 3, 1);
      if constexpr (s == 12 && k % 4 == 1) load_h1(IC<0>{}, IC<k / 4>{});
      if constexpr (AT(12, 15)) { load_h1(IC<0>{}, IC<4>{}); load_h1(IC<0>{}, IC<5>{}); }
      if constexpr (s == 13 && k % 4 == 2) load_h1(IC<1>{}, IC<k / 4>{});
      if constexpr (AT(13, 15)) { load_h1(IC<1>{}, IC<4>{}); load_h1(IC<1>{}, IC<5>{}); }
      if constexpr (AT(1, 1)) { need<OP_EP>(); PIN(epoch); PIN(code_touch); tag = (unsigned)__builtin_amdgcn_readfirstlane(epoch); PINS(tag); }
      if constexpr (AT(1, 5)) { tag = tag * 0x9E3779B1u + 0x7F4A7C15u; PINS(tag); }
      if constexpr (AT(1, 7)) { gs_off = (unsigned)(WS_STAT + (g1 * 32 + (l & 31)) * 16); PIN(gs_off); }
      if constexpr (AT(1, 10)) { pq = priv + q * 16; pw0 = priv + l * 4; PIN(pq); PIN(pw0); }
      if constexpr (AT(1, 16)) { ea = L0 + L_EB + r * 264 + m * 64 + nh * 32 + q * 8; PIN(ea); }
      if constexpr (AT(1, 19)) { eo = (unsigned)((tid >> 4) * E_K1 + pn * E_PN + (16 * tt + r) * 16); PIN(eo); }
      if constexpr (AT(1, 22)) { ebo = L0 + L_EB + (tid >> 4) * 264 + r * 4; PIN(ebo); }
      if constexpr (AT(2, 18)) { gc_off = (unsigned)(WS_CS + (tt * 4 + (q & 1)) * 512 + (32 * nh + 2 * r) * 8); PIN(gc_off); }
      if constexpr (AT(2, 15)) { pw1 = priv + (l & 31) * 8; PIN(pw1); }
      if constexpr (AT(5, 13)) *(LAS unsigned*)(uintptr_t)(L0 + L_ZOK) = 0u;
      if constexpr (AT(7, 6)) step_setup(0, 0);
      if constexpr (AT(9, 4)) step_setup(0, 1);
      if constexpr (AT(10, 2)) step_setup(0, 2);
      if constexpr (AT(6, 0)) { doff = (unsigned)(lr * 896 + ct * 128 + ((lc ^ (((lr >> 1) & 3) << 1)) << 4)); PIN(doff); }
      if constexpr (AT(6, 2)) { l4c = l * 4 + 4096; PIN(l4c); }
      if constexpr (AT(9, 2)) { const int row = 4 * q + (r >> 2); da[0][0] = L0 + w * 2048 + row * 128 + (((row >> 1) & 3) << 5) + (r & 3) * 8; PIN(da[0][0]); }
      if constexpr (AT(9, 5) || AT(9, 6) || AT(9, 8)) { constexpr int dt = k == 8 ? 3 : k - 4; da[0][dt] = da[0][0] ^ (dt << 5); PIN(da[0][dt]); }
      if constexpr (s == 9 && k >= 10 && k < 14) { da[1][k - 10] = da[0][k - 10] + 65536; da[2][k - 10] = da[0][k - 10] + 131072; PIN(da[1][k - 10]); PIN(da[2][k - 10]); }
      if constexpr (AT(12, 0)) {
        const int gg = 2 * rt + (w >> 2);
        const int n2 = ((gg >> 2) & 1) + 8 * (gg >> 3) + 2 * (gg & 3);
        ooff = (unsigned)((64 * (orow & 31) + n2) * 896 + ct * 128 + lc * 16); PIN(ooff);
      }
      if constexpr (AT(2, 22)) ld128_sc1<OP_GS, 0>(gran, rw, gs_off, 0u);
      if constexpr (AT(3, 10)) { waitv<2, OP_GS>(); PIN(gran); bad0 = mism(gran.y, tag); bad1 = mism(gran.w, tag); }
      if constexpr (AT(3, 13)) {
        if (__builtin_expect((bad0 | bad1) != 0, 0)) { RETRY(12); poll_granules(gran, rw, gs_off, tag); }
        *(LAS unsigned*)(uintptr_t)(pw0) = gran.z;
      }
      if constexpr (AT(3, 15)) {
        auto sw = __builtin_amdgcn_permlane16_swap(gran.x, gran.x, false, false);
        const unsigned a0 = sw[0], a1 = sw[1];
        mrow[0] = opq(asf(a0)); mrow[1] = opq(asf(a1));
      }
      if constexpr (AT(3, 17)) { rsr[0] = opq(asf(gran.z) * asf(gran.z)); }
      if constexpr (AT(3, 21)) {
        auto sw = __builtin_amdgcn_permlane16_swap(asu(rsr[0]), asu(rsr[0]), false, false);
        const unsigned a0 = sw[0], a1 = sw[1];
        r2row[0] = opq(asf(a0)); r2row[1] = opq(asf(a1));
      }
      if constexpr (AT(3, 16) || AT(3, 18)) rD[(k - 16) >> 1] = lds128f(pq + ((k - 16) >> 1) * 64);
      if constexpr (AT(3, 19) || AT(3, 20)) p1s[k - 19] = lds128(aux16 + (L_P1 - L_AUX) + 2048 + (k - 19) * 1024);
      if constexpr (AT(4, 0) || AT(4, 2)) p1c[k >> 1] = lds128(aux16 + (L_P1 - L_AUX) + (k >> 1) * 1024);
      if constexpr (s == 4 && k >= 1 && k < 5) {
        constexpr int e = 2 * (k - 1);
        dv[e] = opq(rD[e >> 2][e & 3] * accD[e >> 2][e & 3]);
        dv[e + 1] = opq(rD[e >> 2][(e & 3) + 1] * accD[e >> 2][(e & 3) + 1]);
      }
      if constexpr (AT(4, 5) || AT(4, 6)) {
        constexpr int e = 4 * (k - 5);
        dpk[e >> 1] = pk2(dv[e], dv[e + 1]); dpk[(e >> 1) + 1] = pk2(dv[e + 2], dv[e + 3]);
        PIN(dpk[e >> 1]); PIN(dpk[(e >> 1) + 1]);
      }
      if constexpr (AT(4, 7)) {
        df = u32x4{dpk[0], dpk[1], dpk[2], dpk[3]};
        mma0(ts[0], df, p1s[0]); mma0(tc[0], df, p1c[0]); mma0(ts[1], df, p1s[1]); mma0(tc[1], df, p1c[1]);
      }
      if constexpr (s == 4 && k >= 10 && k < 14) {
        constexpr int a = (k - 10) >> 1, e = 2 * (k & 1);
        ev[0] = opq(tc[a][e] + ts[a][e + 1]);
        ev[1] = opq(tc[a][e + 1] - ts[a][e]);
        dpk[k & 1] = pk2(ev[0], ev[1]); PIN(dpk[k & 1]);
      }
      if constexpr (AT(4, 11) || AT(4, 13)) *(LAS u32x2*)(uintptr_t)(ea + ((k - 10) >> 1) * (16 * 264)) = u32x2{dpk[0], dpk[1]};
      if constexpr (AT(5, 0)) {
        waitv<6, 13>();
        asm volatile("s_waitcnt lgkmcnt(3)\n\ts_barrier" ::: "memory");
#pragma unroll
        for (int i = 0; i < 4; ++i) ebv[i] = *(LAS const unsigned*)(uintptr_t)(ebo + i * 64);
      }
      if constexpr (AT(5, 5)) ld128_sc1<OP_GC1, 0>(gran, rw, gc_off, 0u);
      if constexpr (AT(5, 4)) { hsum = (tag + ebv.x + ebv.y) + ebv.z + ebv.w; PIN(hsum); tch2 = tid * 4; PIN(tch2); }
      if constexpr (AT(5, 7)) { st128_sc1<OP_E, 0>(rw, eo, WS_E, ebv); st32_sc1<OP_E>(rw, tch2, ecs, hsum); }
      if constexpr (AT(5, 2)) { tco = (tid >= 480 && bj >= 24) ? (unsigned)(WS_HINT + ((bj - 24) * 8 + (xcd ^ 4)) * 128 + (tid - 480) * 4) : OOB; dpk[2] = tag; PIN(tco); PIN(dpk[2]); }
      if constexpr (AT(5, 9)) st32_sc1<OP_E>(rw, tco, 0u, dpk[2]);
      if constexpr (AT(6, 6)) { waitv<4, OP_GC1>(); need<16>(); PIN(gran); bad0 = mism(gran.y, tag); bad1 = mism(gran.w, tag); }
      if constexpr (AT(6, 7)) asm volatile("s_barrier" ::: "memory");
      if constexpr (AT(12, 11)) { waitv<10, OP_GC2>(); PIN(tch); PIN(tch3); PIN(tch4); PIN(gran); bad0 = mism(gran.y, tag); bad1 = mism(gran.w, tag); }
      if constexpr (AT(6, 9) || AT(12, 14)) {
        if (__builtin_expect((bad0 | bad1) != 0, 0)) { RETRY(s == 12 ? 14 : 13); poll_granules(gran, rw, gc_off + (s == 12 ? 1024 : 0), tag); }
        *(LAS unsigned*)(uintptr_t)(pw1) = gran.x; *(LAS unsigned*)(uintptr_t)(pw1 + 4) = gran.z;
      }
      if constexpr (AT(6, 12) || AT(6, 14) || AT(7, 1) || AT(7, 3)) { constexpr int i = s == 6 ? k - 12 : k; cs[i] = lds128f(pq + i * 64); }
      if constexpr (s == 13 && k >= 3 && k < 11 && (k & 1)) cs[(k - 3) >> 1] = lds128f(pq + ((k - 3) >> 1) * 64);
      if constexpr ((s == 7 && k >= 8) || (s == 8 && k < 10)) {
        constexpr int o = 4 * (16 * (s - 7) + k - 8);
        hop(IC<o>{}); hop(IC<o + 1>{}); hop(IC<o + 2>{}); hop(IC<o + 3>{});
      }
      if constexpr (AT(10, 8)) ld128_sc1<OP_GC2, 1024>(gran, rw, gc_off, 0u);
      if constexpr (AT(8, 7)) h_store(IC<0>{}, IC<0>{});
      if constexpr (AT(8, 10)) { h_store(IC<0>{}, IC<1>{}); hsum = (tag + hpk[0][0] + hpk[0][1]) + hpk[0][2] + hpk[0][3]; PIN(hsum); }
      if constexpr (AT(8, 11)) { hsum = (hsum + hpk[1][0] + hpk[1][1]) + hpk[1][2] + hpk[1][3]; PIN(hsum); }
      if constexpr (AT(8, 12)) h_check_store(IC<0>{});
      if constexpr (AT(8, 14)) { waitv<10, 17>(); asm volatile("s_barrier" ::: "memory"); }
      if constexpr (AT(10, 7)) { waitv<5, 20>(); asm volatile("s_barrier" ::: "memory"); }
      if constexpr (AT(13, 7)) asm volatile("s_barrier" ::: "memory");
    };

    sfor<0, 14>([&](auto SI) {
      constexpr int s = decltype(SI)::v;
      constexpr int T = tile_of_w(s), sl = T & 7;
      constexpr int ps = s / 7, j = s % 7, pps = (s - 1) / 7, pj = (s + 6) % 7;
      constexpr int x0 = s < 4 ? kSegX0[s < 4 ? s : 0] : 0, nx = s < 4 ? kSegNX[s < 4 ? s : 0] : 0;
      constexpr int waitB[5] = {5, 4, 4, 5, 6}, lastT[5] = {1, 3, 7, 10, 12};
      if constexpr (s <= 4) { waitv<waitB[s <= 4 ? s : 0], lastT[s <= 4 ? s : 0]>(); BARRIER(); }
      SB();
      sfor<0, 16 + 4 * nx>([&](auto KI) {
        constexpr int k = decltype(KI)::v, i = (k & 7) >> 1, h = k & 1;
        if constexpr (k < 8) {
          if constexpr (s > 0) mma(acc[pps][i][h], cat(fb_[i][0], fb_[i][1]), xf[2 * pj + 1][h]);
        } else if constexpr (k < 16) {
          if constexpr (j == 0) mma0(acc[ps][i][h], cat(fa_[i][0], fa_[i][1]), xf[0][h]);
          else mma(acc[ps][i][h], cat(fa_[i][0], fa_[i][1]), xf[2 * j][h]);
        } else {
          if constexpr (s == 0 && k < 18) mma0(accD[h], xf[0][h], ff[0]);
          else mma(accD[h], xf[2 * x0 + i][h], ff[i]);
        }
        SB();
        constexpr int xr = s == 0 ? k : (k < 2 ? k : ((k & 1) ? -1 : 1 + k / 2));
        if constexpr (xr >= 0 && xr < 4 * nx) {
          constexpr int xt = x0 + (xr >> 2), kk = (xr >> 1) & 1, xs = tile_of_x(xt) & 7;
          xf[2 * xt + kk][xr & 1] = lds128(xa[xs >> 2][kk] + (xs & 3) * 16384 + (xr & 1) * 2048);
        }
        if constexpr (k < 16) {
          const unsigned a = wa[sl >> 2][i & 1] + (sl & 3) * 16384 + (i >> 1) * 8192 + (k >> 3) * 4096 + h * 512;
          if constexpr (k < 8) fa_[i][h] = ldstr1(a); else fb_[i][h] = ldstr1(a);
        }
        if constexpr (k >= 9 && (k & 1) && (k - 9) / 2 < 2 * nx) ff[(k - 9) / 2] = lds128(fad + (2 * x0 + (k - 9) / 2) * 1024);
        SB();
        side(SI, KI);
        SB();
      });
      if constexpr (s == 0) STAMP(3);
      if constexpr (s == 3) STAMP(4);
      if constexpr (s == 5) STAMP(5);
      if constexpr (s == 7) STAMP(6);
      if constexpr (s == 9) STAMP(7);
      if constexpr (s == 11) STAMP(8);
    });
    waitv<6, OP_HE + 0>();
    need<OP_P + 3>();
    sfor<0, 8>([&](auto KI) {
      constexpr int k = decltype(KI)::v, i = ((k >> 1) & 1) * 2 + (k >> 2), h = k & 1;
      mma(acc[1][i][h], cat(fb_[i][0], fb_[i][1]), xf[13][h]);
      SB();
      if constexpr (k < 7) prep(IC<0>{}, IC<0>{}, KI);
      SB();
    });
    STAMP(9);
    h_part(IC<1>{}, 0, 0); h_part(IC<1>{}, 1, 0);
    h_part(IC<1>{}, 0, 1); h_store(IC<1>{}, IC<0>{});
    h_part(IC<1>{}, 1, 1); h_store(IC<1>{}, IC<1>{});
    hsum = csum8(tag, u32x4{hpk[0][0], hpk[0][1], hpk[0][2], hpk[0][3]}, u32x4{hpk[1][0], hpk[1][1], hpk[1][2], hpk[1][3]});
    h_check_store(IC<1>{});
    SB();
    prep(IC<0>{}, IC<0>{}, IC<7>{});
    STAMP(10);

    f32x4 oacc[4][4];
    auto gstep = [&](auto SI, auto&& fn) {
      constexpr int s = decltype(SI)::v;
      sfor<0, 16>([&](auto KI) {
        constexpr int k = decltype(KI)::v, dt = k >> 2, rf = k & 3;
        if constexpr (s == 0) mma0(oacc[dt][rf], cat(af[0][dt][0], af[0][dt][1]), hb[0][rf]);
        else mma(oacc[dt][rf], cat(af[s & 1][dt][0], af[s & 1][dt][1]), hb[s & 3][rf]);
        SB();
        fn(KI);
        SB();
      });
    };
    auto gops = [&](auto B0, auto N) { sfor<decltype(B0)::v, (decltype(B0)::v + decltype(N)::v < 272 ? decltype(B0)::v + decltype(N)::v : 272)>([&](auto O) { gop(O); }); };
    auto step_ld = [&](auto GI, auto SI, auto NI) {
      constexpr int s = decltype(SI)::v, n = decltype(NI)::v;
      if constexpr (n == 0 && s > 2) step_setup(decltype(GI)::v, s);
      if constexpr (n < 4) issue_p1(GI, IC<2 * s + n / 2>{}, IC<n & 1>{});
      else load_h1(SI, IC<n - 4>{});
      if constexpr (n == 7) { load_h1(SI, IC<4>{}); load_h1(SI, IC<5>{}); }
    };
    f32x4 fin[2];
    u32x4 sel[4], xd, xres;
    unsigned pw[4], ra[8], bp;
    auto fin_setup = [&](auto KI) {
      constexpr int k = decltype(KI)::v;
      if constexpr (k == 0) { need<OP_XRES>(); PIN(xres); bp = (unsigned)((8 * (r & 7) + 4 * (r >> 3) + q) * 4); PIN(bp); }
      if constexpr (k >= 1 && k < 5) {
        const unsigned t_ = xres[k - 1]; xd[k - 1] = (unsigned)__builtin_amdgcn_ds_bpermute((int)bp, (int)t_);
        const unsigned o_ = r == 4 * (k - 1) + q ? 0x3f803f80u : 0u; sel[k - 1] = u32x4{o_, o_, o_, o_}; PIN(sel[k - 1]);
      }
      if constexpr (k == 5) { pw[0] = lw2 + r * 128 + ((q ^ r ^ (w & 1) ^ ((w & 2) << 2)) << 3); PIN(pw[0]); }
      if constexpr (k == 6) { pw[1] = pw[0] ^ 32; pw[2] = pw[0] ^ 64; pw[3] = pw[0] ^ 96; PIN(pw[1]); PIN(pw[2]); PIN(pw[3]); }
      if constexpr (k == 7) { bp = (unsigned)((lr + 8 * (lc >> 2) + 16 * (lc & 3)) * 4); PIN(bp); }
      if constexpr (k == 8) {
        const int qq = r >> 2, row = 8 * (w & 1) + q;
        ra[0] = L0 + (w >> 1) * 16384 + qq * 2048 + row * 128 + (((2 * (r & 3)) ^ row ^ (qq & 1) ^ ((qq & 2) << 2)) << 3); PIN(ra[0]);
      }
      if constexpr (k == 9) {
#pragma unroll
        for (int i = 1; i < 8; ++i) { ra[i] = ra[0] ^ (((i >> 1) & 1) << 5) ^ ((i >> 2) << 6) ^ ((i & 1) << 3); PIN(ra[i]); }
      }
      if constexpr (k == 10) {
#pragma unroll
        for (int i = 0; i < 4; ++i) { const unsigned t_ = xd[i]; fin[i >> 1][2 * (i & 1)] = opq(bflo(t_)); fin[i >> 1][2 * (i & 1) + 1] = opq(bfhi(t_)); }
      }
    };
    auto put = [&](int dt, int rf) {
      const f32x4 o = oacc[dt][rf];
      *(LAS u32x2*)(uintptr_t)(pw[dt] + rf * 16384) = u32x2{pk2(o[0], o[1]), pk2(o[2], o[3])};
    };
    waitv<3, OP_HE + 1>();
#pragma unroll
    for (int i = 0; i < 4; ++i) PIN(hb[1][i]);
    PIN(hck[1][0]); PIN(hck[1][1]);
    if (nh) {
      ONLY(Y);
      using G1 = IC<1>;
      gstep(IC<0>{}, [&](auto KI) {
        constexpr int k = decltype(KI)::v;
        if constexpr (k == 0 || k == 3 || k == 6) step_ld(G1{}, IC<2>{}, IC<k / 3>{});
        if constexpr (k >= 8) prep(G1{}, IC<1>{}, IC<k - 8>{});
      });
      gstep(IC<1>{}, [&](auto KI) {
        constexpr int k = decltype(KI)::v;
        if constexpr (k >= 11) step_ld(G1{}, IC<2>{}, IC<k - 8>{});
      });
      asm volatile("s_barrier" ::: "memory");
      STAMP(11);
      sfor<0, 8>([&](auto NI) { step_ld(G1{}, IC<3>{}, NI); });
      ld128<OP_XRES, 0>(xres, rx, ooff, 0u);
      waitv<11, OP_HE + 2>();
      sfor<0, 8>([&](auto JI) { prep(G1{}, IC<2>{}, JI); });
      STAMP(12);
      sfor<2, 9>([&](auto SI) {
        constexpr int s = decltype(SI)::v;
        gstep(SI, [&](auto KI) {
          constexpr int k = decltype(KI)::v;
          if constexpr (s + 2 < 9 && k < 8) step_ld(G1{}, IC<s + 2>{}, KI);
          if constexpr (s < 8 && k == 8) waitv<(s == 2 ? 10 : (s < 7 ? 10 : 0)), OP_HE + (s < 8 ? s + 1 : s)>();
          if constexpr (s < 8 && k >= 8) prep(G1{}, IC<(s < 8 ? s + 1 : s)>{}, IC<k - 8>{});
          if constexpr (s == 3 && k < 8) fin_setup(KI);
          if constexpr (s == 4 && k < 6 && !(k & 1)) fin_setup(IC<8 + k / 2>{});
          if constexpr (s == 8 && k < 3) asm volatile("s_nop 2");
          if constexpr (s == 8 && k >= 3) put((k - 3) >> 2, (k - 3) & 3);
        });
        if constexpr (s == 3) __builtin_amdgcn_s_setprio(1);
        if constexpr (s == 3) STAMP(13);
        if constexpr (s == 5) STAMP(15);
        if constexpr (s == 7) STAMP(17);
      });
      STAMP(19);
      put(3, 1); SB(); put(3, 2); SB(); put(3, 3);
      ONLY(END);
    } else {
      ONLY(O);
      using G0 = IC<0>;
      g_cq = opq((float)(56 * m + 8 * q));
      g_mk = q == 3 ? 0u : ~0u;
      g_ny = (tid < 16 && (tid & 1)) ? 0x8000u : 0u;
      auto phop = [&](auto NI) {
        constexpr int n = decltype(NI)::v, i = n >> 2, v = n & 3;
        const unsigned c_ = pc[i], s_ = psn[i];
        unsigned u0 = (v & 1) ? __builtin_amdgcn_perm(c_, s_, (v & 2) ? 0x07060302u : 0x05040100u) : __builtin_amdgcn_perm(s_, c_, (v & 2) ? 0x07060302u : 0x05040100u);
        if constexpr (v == 0) { u0 ^= 0x80000000u; PIN(u0); ph[0][0][i] = u0; }
        if constexpr (v == 1) { PIN(u0); ph[0][1][i] = u0; }
        if constexpr (v == 2) { unsigned u1 = u0 ^ 0x8000u; u0 ^= 0x80000000u; PIN(u0); PIN(u1); ph[1][0][i] = u0; ph[2][0][i] = u1; }
        if constexpr (v == 3) { unsigned u1 = u0 ^ 0x80008000u; PIN(u0); PIN(u1); ph[1][1][i] = u0; ph[2][1][i] = u1; }
      };
      gstep(IC<0>{}, [&](auto KI) {
        constexpr int k = decltype(KI)::v;
        if constexpr (k == 0 || k == 3 || k == 6) step_ld(G0{}, IC<2>{}, IC<k / 3>{});
        if constexpr (k >= 8) prep(G0{}, IC<1>{}, IC<k - 8>{});
        if constexpr (k < 8) gops(IC<4 * k>{}, IC<4>{}); else gops(IC<32 + 2 * (k - 8)>{}, IC<2>{});
      });
      gstep(IC<1>{}, [&](auto KI) {
        constexpr int k = decltype(KI)::v;
        gops(IC<48 + 4 * k>{}, IC<4>{});
        if constexpr (k >= 11) step_ld(G0{}, IC<2>{}, IC<k - 8>{});
      });
      asm volatile("s_barrier" ::: "memory");
      STAMP(11);
      sfor<0, 8>([&](auto NI) { step_ld(G0{}, IC<3>{}, NI); SB(); gops(IC<112 + 3 * decltype(NI)::v>{}, IC<3>{}); SB(); });
      pz_off = w ? OOB : (unsigned)(WS_FLAGZ + (l & 31) * 128);
      ld128<OP_XRES, 0>(xres, rx, ooff, 0u);
      ld32_sc1<OP_POLL>(pollv, rw, pz_off);
      waitv<12, OP_HE + 2>();
      sfor<0, 8>([&](auto JI) { prep(G0{}, IC<2>{}, JI); });
      STAMP(12);
      gstep(IC<2>{}, [&](auto KI) {
        constexpr int k = decltype(KI)::v;
        if constexpr (k < 8) step_ld(G0{}, IC<4>{}, KI);
        if constexpr (k == 1) { zo = (unsigned)(((l >> 1) & 7) ^ ((q & 1) | ((q & 2) << 1))); PIN(zo); }
        if constexpr (k == 2) { zo = q * 256 + (zo << 5) + (l & 1) * 16; PIN(zo); }
        if constexpr (k == 3) { const int P = 2 * q + (r >> 3); za[0] = (unsigned)(((r >> 1) & 2) ^ ((P & 1) | ((P & 2) << 1))); za[1] = P * 256 + (r & 3) * 8; PIN(za[0]); PIN(za[1]); }
        if constexpr (k == 4) { za[0] = lw2 + za[1] + (za[0] << 5); zva = lw2 + l16; PIN(za[0]); PIN(zva); }
        if constexpr (k == 5) { za[1] = za[0] ^ 128; za[2] = za[0] ^ 32; za[3] = za[0] ^ 160; PIN(za[1]); PIN(za[2]); PIN(za[3]); }
        if constexpr (k == 14) pc = lds128(aux16 + (L_P1 - L_AUX));
        if constexpr (k == 15) psn = lds128(aux16 + (L_P1 - L_AUX) + 2048);
        if constexpr (k == 8) waitv<10, OP_POLL>();
        if constexpr (k >= 8) prep(G0{}, IC<3>{}, IC<k - 8>{});
      });
      STAMP(13);
      PIN(pollv);
      unsigned zso = (unsigned)(WS_Z + 2 * rt * Z_G + 2 * m * 4096);
      if (w == 0) {
        if (mism(pollv, tag) != 0) { RETRY(9); poll_flags(rw, pz_off, tag); }
        *(volatile LAS unsigned*)(uintptr_t)(L0 + L_ZOK) = 1u;
      } else {
#pragma nounroll
        for (unsigned spins = 0; spins < 64 * SPIN_CAP; ++spins) {
          if (__builtin_amdgcn_readfirstlane(*(volatile LAS unsigned*)(uintptr_t)(L0 + L_ZOK))) break;
          __builtin_amdgcn_s_sleep(1);
        }
      }
      asm volatile("" ::: "memory");
      auto issue_z1 = [&](auto HV, auto NI, auto IDI) {
        constexpr int hv = decltype(HV)::v, zi = decltype(NI)::v >> 1, zh = decltype(NI)::v & 1;
        dma_add_sc1<decltype(IDI)::v, (4 * hv + zi) * S_ + zh * 1024, hv * 4096 + (zi >> 1) * Z_G + (zi & 1) * 2048 + zh * 1024>(rw, zo, lw2, zso);
      };
      gstep(IC<3>{}, [&](auto KI) {
        constexpr int k = decltype(KI)::v;
        if constexpr (k < 8) { issue_z1(IC<0>{}, KI, IC<OP_Z>{}); phop(IC<2 * k>{}); phop(IC<2 * k + 1>{}); }
        if constexpr (k == 8) waitv<8, OP_HE + 4>();
        if constexpr (k >= 8) prep(G0{}, IC<4>{}, IC<k - 8>{});
      });
      pin_af(4);
      gstep(IC<4>{}, [&](auto KI) {
        constexpr int k = decltype(KI)::v;
        if constexpr (!(k & 1)) issue_z1(IC<1>{}, IC<k / 2>{}, IC<OP_Z + 1>{});
        gops(IC<136 + 5 * k>{}, IC<5>{});
      });
      STAMP(15);
      sfor<0, 2>([&](auto HV) {
        constexpr int hv = decltype(HV)::v;
        waitv<hv ? 0 : 8, OP_Z + hv>();
        if constexpr (hv == 1) {
#pragma unroll
          for (int i = 0; i < 4; ++i) { za[i] += 65536; PIN(za[i]); }
          zva += 65536; PIN(zva);
        }
        u32x4 z0[2], z1[2];
        f32x4 t[6];
        unsigned yy[2][2][4];
        auto rdz = [&](int u) {
          const unsigned a_ = za[2 * (u & 1)] + (u >> 1) * 2 * S_, b_ = za[2 * (u & 1) + 1] + (u >> 1) * 2 * S_;
          z0[u & 1] = ldstr(a_, a_ + S_); z1[u & 1] = ldstr(b_, b_ + S_);
        };
        auto amma = [&](int u, int i) { if (i < 2) mma0(t[i], z0[u & 1], ph[0][i]); else mmac(t[i], z1[u & 1], ph[i >> 1][i & 1], t[i & 1]); };
        auto ypk = [&](int u, int k) { yy[u & 1][k >> 2][k & 3] = pk2(t[2 + 2 * (k >> 2)][k & 3], t[3 + 2 * (k >> 2)][k & 3]); PIN(yy[u & 1][k >> 2][k & 3]); };
#pragma nounroll
        for (unsigned spins = 0;; ++spins) {
          u32x4 zv[4];
          unsigned zs;
          rdz(0);
#pragma unroll
          for (int i = 0; i < 4; ++i) zv[i] = lds128(zva + (i >> 1) * S_ + (i & 1) * 1024);
          SB();
#pragma unroll
          for (int i = 0; i < 6; ++i) {
            amma(0, i);
            SB();
            if (i >= 1 && i < 5) { zs = ((i == 1 ? 0u : zs) + zv[i - 1].x + zv[i - 1].y) + zv[i - 1].z + zv[i - 1].w; PIN(zs); zv[i - 1] = lds128(zva + (2 + ((i - 1) >> 1)) * S_ + ((i - 1) & 1) * 1024); }
            SB();
          }
          rdz(1);
#pragma unroll
          for (int i = 0; i < 4; ++i) { zs = (zs + zv[i].x + zv[i].y) + zv[i].z + zv[i].w; PIN(zs); }
          keep(t[0], t[1]);
          SB();
#pragma unroll
          for (int k = 0; k < 8; ++k) {
            ypk(0, k);
            if (k == 1) { zs += (unsigned)__builtin_amdgcn_update_dpp(0, (int)zs, 0x128, 0xf, 0xf, true); PIN(zs); }
            if (k == 3) { zs += (unsigned)__builtin_amdgcn_update_dpp(0, (int)zs, 0x124, 0xf, 0xf, true); PIN(zs); }
            if (k == 5) { zs += (unsigned)__builtin_amdgcn_update_dpp(0, (int)zs, 0x122, 0xf, 0xf, true); PIN(zs); }
            if (k == 7) { zs += (unsigned)__builtin_amdgcn_update_dpp(0, (int)zs, 0x121, 0xf, 0xf, true); PIN(zs); }
            SB();
          }
          bad0 = mism(zs, tag * 16u);
#pragma unroll
          for (int i = 0; i < 6; ++i) amma(1, i);
          if (__builtin_expect(bad0 == 0 || spins >= SPIN_CAP, 1)) break;
          RETRY(10 + hv);
          __builtin_amdgcn_s_sleep(10);
          asm volatile("s_waitcnt lgkmcnt(0)" ::: "memory");
          sfor<0, 8>([&](auto NI) { issue_z1(HV, NI, IC<OP_OPT>{}); });
          asm volatile("s_waitcnt vmcnt(0)" ::: "memory");
        }
        sfor<0, 4>([&](auto UI) {
          constexpr int u = decltype(UI)::v;
          const u32x4 y0 = {yy[u & 1][0][0], yy[u & 1][0][1], yy[u & 1][0][2], yy[u & 1][0][3]};
          const u32x4 y1 = {yy[u & 1][1][0], yy[u & 1][1][1], yy[u & 1][1][2], yy[u & 1][1][3]};
          if (u == 1 || u == 2) {
#pragma unroll
            for (int i = 0; i < 6; ++i) amma(u + 1, i);
          } else if (u == 3) asm volatile("s_nop 1");
          sfor<0, 8>([&](auto KI) {
            constexpr int k = decltype(KI)::v;
            mma(oacc[k >> 1][2 * (u >> 1) + (k & 1)], gf[hv][u & 1][k >> 1], (k & 1) ? y1 : y0);
            SB();
            if constexpr (u < 2 && k == 0) rdz(u + 2);
            if constexpr (u == 2 && k == 0) asm volatile("s_nop 1");
            if constexpr (u < 3 && k == 3) keep(t[0], t[1]);
            if constexpr (u < 3 && k >= 4) { ypk(u + 1, 2 * (k - 4)); ypk(u + 1, 2 * (k - 4) + 1); }
            if constexpr (hv == 0 && (u == 3 || k < 4)) gops(IC<216 + 3 * (4 * u + k)>{}, IC<3>{});
            if constexpr (hv == 1 && u < 3 && k < 4 && 4 * u + k < 11) fin_setup(IC<4 * u + k>{});
            if constexpr (hv == 1 && u == 3) put(k >> 1, k & 1);
            SB();
          });
        });
        if constexpr (hv == 0) STAMP(17);
      });
      STAMP(19);
#pragma unroll
      for (int k = 0; k < 8; ++k) { put(k >> 1, 2 + (k & 1)); SB(); }
      ONLY(END);
    }
    BARRIER();
    STAMP(20);
    {
      u32x4 pa[8];
#pragma unroll
      for (int i = 0; i < 8; ++i) pa[i] = ldstr(ra[i] + ((i >> 1) & 1) * 512, ra[i] + ((i >> 1) & 1) * 512 + 8192);
#pragma unroll
      for (int i = 0; i < 8; ++i) fin[i & 1] = mfma16(pa[i], sel[i >> 1], fin[i & 1]);
      const u32x4 o = {pk2(fin[0][0], fin[0][1]), pk2(fin[0][2], fin[0][3]), pk2(fin[1][0], fin[1][1]), pk2(fin[1][2], fin[1][3])};
      u32x4 oc;
#pragma unroll
      for (int i = 0; i < 4; ++i) { const unsigned t_ = o[i]; oc[i] = (unsigned)__builtin_amdgcn_ds_bpermute((int)bp, (int)t_); }
      asm volatile("global_store_dwordx4 %0, %1, %2 sc1" ::"v"(ooff), "v"(oc), "s"(out) : "memory");
    }
    if (b == 32 && tid == 0) *(unsigned*)(ws + WS_EPOCH) = tag;
    STAMP(21);
  } else {
    const int k1 = 8 * bi + xcd;
    const int lr = l >> 3, lc = l & 7;
    const int gst = 2 * (8 * (xcd & 3) + 4 * (xcd >> 2) + bi) + (w >> 2), n1s = 8 * (w & 3) + lr;
    const int cid = 4 * (xcd & 3) + bi;
    const int ctt = 7 * (xcd >> 2) + ((cid < 14 ? cid : 0) >> 1), cmat = cid & 1;
    const rsrc_t rx = mk_rsrc(x, 2048 * 896), rc = mk_rsrc(cmat ? wu : wg, 448 * 3584), rw = mk_rsrc(ws, WS_BYTES);
    const rsrc_t rfr = mk_rsrc(wr, 921600), rfi = mk_rsrc(wi, 921600);
    LOAD_EPOCH();
    u32x4 xr[7];
    {
      const int n2 = ((gst >> 2) & 1) + 8 * (gst >> 3) + 2 * (gst & 3);
      const unsigned xo = (unsigned)((64 * n1s + n2) * 896 + lc * 16);
      sfor<0, 7>([&](auto J) { constexpr int j = decltype(J)::v; ld128<0, j * 128>(xr[j], rx, xo, 0u); });
    }
    constexpr unsigned L_RED = 28672, L_WT = 32768, L_FILT = 32768;
    const unsigned wo = (unsigned)((56 * w + lr) * 3584 + ctt * 256 + ((lc ^ (((lr >> 1) & 3) << 1)) << 4));
    const unsigned lwt = L0 + L_WT + w * 7168;
    const int c31 = l & 31, h = l >> 5;
    {
      const int s = w, par = s >> 2;
      float vc[4], vs[4];
#pragma unroll
      for (int mm = 0; mm < 4; ++mm) {
        int n2 = par + 8 * (2 * (s & 3) + h) + 2 * mm;
        float rev = (float)((n2 * (k1 + 32 * c31)) & 2047) * (1.0f / 2048.0f);
        vc[mm] = __builtin_amdgcn_cosf(rev); vs[mm] = __builtin_amdgcn_sinf(rev);
      }
      *(LAS u32x4*)(uintptr_t)(L0 + (2 * s) * 1024 + l * 16) = u32x4{pk2(vc[0], vs[0]), pk2(vc[1], vs[1]), pk2(vc[2], vs[2]), pk2(vc[3], vs[3])};
      *(LAS u32x4*)(uintptr_t)(L0 + (2 * s + 1) * 1024 + l * 16) = u32x4{pk2(-vs[0], vc[0]), pk2(-vs[1], vc[1]), pk2(-vs[2], vc[2]), pk2(-vs[3], vc[3])};
    }
    {
      const int par = (w >> 1) & 1, s2 = w & 1;
      float v[8];
#pragma unroll
      for (int e = 0; e < 8; ++e) {
        int k2 = 16 * s2 + 8 * (e >> 2) + 4 * h + (e & 3);
        v[e] = __builtin_amdgcn_cosf((float)(((par + 2 * c31) * (k1 + 32 * k2)) & 2047) * (1.0f / 2048.0f) - (w >= 4 ? 0.25f : 0.0f)) * (2.0f / (2048.0f * 448.0f));
      }
      const u32x4 pk = {pk2(v[0], v[1]), pk2(v[2], v[3]), pk2(v[4], v[5]), pk2(v[6], v[7])};
      const unsigned a = L0 + 16384 + (w & 3) * 3072 + (w >> 2) * 1024 + l * 16;
      *(LAS u32x4*)(uintptr_t)(a) = pk;
      if (w >= 4) *(LAS u32x4*)(uintptr_t)(a + 1024) = pk ^ 0x80008000u;
    }
    SB();
    waitv<0, 0>();
    sfor<0, 3>([&](auto J) { constexpr int j = decltype(J)::v; dma_add<0, j * 1024, 0>(rc, wo, lwt, (unsigned)(j * (8 * 3584))); });
    GET_TAG();
    {
      float s1 = 0.f, s2 = 0.f;
#pragma unroll
      for (int j = 0; j < 7; ++j) { PIN(xr[j]); s1 = sum8(xr[j], s1); s2 = sq8(xr[j], s2); }
      s1 += dpp<0xB1>(s1); s2 += dpp<0xB1>(s2);
      s1 += dpp<0x4E>(s1); s2 += dpp<0x4E>(s2);
      s1 += dpp<0x141>(s1); s2 += dpp<0x141>(s2);
      const float mean = s1 * (1.0f / 448.0f);
      float var = __builtin_fmaf(-mean, mean, s2 * (1.0f / 448.0f));
      var = var > 0.f ? var : 0.f;
      const float rstd = __builtin_amdgcn_rsqf(var + 1e-5f);
      st128_sc1<0, 0>(rw, lc ? OOB : (unsigned)(WS_STAT + (gst * 32 + n1s) * 16), 0u, u32x4{asu(mean), tag, asu(rstd), tag});
    }
    STAMP(1);
    sfor<3, 7>([&](auto J) { constexpr int j = decltype(J)::v; dma_add<0, j * 1024, 0>(rc, wo, lwt, (unsigned)(j * (8 * 3584))); });
    const unsigned fo = (((unsigned)((k1 + 32 * (8 * w + h)) * 450)) & ~15u) + c31 * 16, lfb = L0 + L_FILT + w * 4096;
    const u32x4 ones = {0x3f803f80u, 0x3f803f80u, 0x3f803f80u, 0x3f803f80u};
    const unsigned csa = L0 + L_WT + (4 * q + (r >> 2)) * 128 + ((((4 * q + (r >> 2)) >> 1) & 3) << 5) + (r & 3) * 8;
    const unsigned cso = cid < 14 ? (unsigned)(WS_CS + (ctt * 4 + cmat) * 512) : OOB;
    auto colsum = [&](unsigned base, int ps) {
      const unsigned a = (csa ^ ((w & 3) << 5)) + (w >> 2) * 28672 + base;
      f32x4 sm = {0, 0, 0, 0};
#pragma unroll
      for (int j = 0; j < 7; ++j) sm = mfma16(ldstr(a + j * 4096, a + j * 4096 + 2048), ones, sm);
      if (r == 0) *(LAS f32x4*)(uintptr_t)(L0 + L_RED + ps * 512 + w * 64 + q * 16) = sm;
      BARRIER();
      if (w == 0) {
        const float t = *(LAS const float*)(uintptr_t)(L0 + L_RED + ps * 512 + l * 4) + *(LAS const float*)(uintptr_t)(L0 + L_RED + ps * 512 + 256 + l * 4);
        st64_sc1<0>(rw, cso + ps * 1024 + l * 8, 0u, u32x2{asu(t), tag});
      }
    };
    waitv<0, 0>();
    sfor<0, 4>([&](auto J) { constexpr int j = decltype(J)::v; dma_add<0, 65536 + j * 1024, 128>(rc, wo, lwt, (unsigned)(j * (8 * 3584))); });
    BARRIER();
    colsum(0, 0);
    STAMP(2);
    sfor<4, 7>([&](auto J) { constexpr int j = decltype(J)::v; dma_add<0, 65536 + j * 1024, 128>(rc, wo, lwt, (unsigned)(j * (8 * 3584))); });
    u32x4 fm[16];
#pragma unroll
    for (int i = 0; i < 16; ++i) fm[i] = lds128(L0 + i * 1024 + l * 16);
#pragma unroll
    for (int i = 0; i < 16; ++i) PIN(fm[i]);
    waitv<0, 0>();
    sfor<0, 4>([&](auto I) { constexpr int i = decltype(I)::v; dma_add<0, i * 1024, 0>(rfr, fo, lfb, (unsigned)(i * 28800)); });
    BARRIER();
    colsum(65536, 1);
    sfor<0, 4>([&](auto I) { constexpr int i = decltype(I)::v; dma_add<0, 32768 + i * 1024, 0>(rfi, fo, lfb, (unsigned)(i * 28800)); });
    waitv<0, 0>();
    BARRIER();
    STAMP(3);
    const int c = 28 * w + (c31 < 28 ? c31 : 27);
    f32x2 wpr[8], wpi[8], wmr[8], wmi[8];
    {
      const unsigned fa0 = L0 + L_FILT + 4 * h * 512 + ((2 * k1) & 15) + (c ? c : 224) * 2;
      unsigned short t[4][16];
#pragma unroll
      for (int i = 0; i < 16; ++i)
#pragma unroll
        for (int j = 0; j < 4; ++j) t[j][i] = *(LAS const unsigned short*)(uintptr_t)(fa0 + ((i & 3) + 8 * (i >> 2)) * 512 + j * 16384);
#pragma unroll
      for (int i = 0; i < 8; ++i) {
        const f32x2 p0 = {bflo(t[0][2 * i]), bflo(t[0][2 * i + 1])}, p1 = {bflo(t[1][2 * i]), bflo(t[1][2 * i + 1])};
        const f32x2 q0 = {bflo(t[2][2 * i]), bflo(t[2][2 * i + 1])}, q1 = {bflo(t[3][2 * i]), bflo(t[3][2 * i + 1])};
        wpr[i] = p0 + p1; wpi[i] = q0 + q1; wmr[i] = p0 - p1; wmi[i] = q0 - q1;
      }
      if (w == 0) {
        const float sc = c ? 1.0f : 0.5f;
#pragma unroll
        for (int i = 0; i < 8; ++i) { wpr[i] *= sc; wpi[i] *= sc; wmr[i] *= sc; wmi[i] *= sc; }
      }
#pragma unroll
      for (int i = 0; i < 8; ++i) { PIN(wpr[i]); PIN(wpi[i]); PIN(wmr[i]); PIN(wmi[i]); }
    }
    const unsigned zst = L0 + 98304 + w * 4096;
    unsigned zw[4], zr = zst + lr * 128 + ((lc ^ lr) << 4);
    unsigned zg = (unsigned)((8 * (lr >> 2) + (lr & 3)) * Z_G + lc * 16), ma = L0 + 16384 + l * 16;
    unsigned evo = (unsigned)(WS_E + k1 * E_K1 + h * 2 * E_PN + c * 16), eco = (unsigned)(WS_EC + (28 * h + (c >> 4)) * 2048 + k1 * 64 + (c & 15) * 4);
#pragma unroll
    for (int i = 0; i < 4; ++i) {
      zw[i] = zst + c31 * 128 + (((4 * (i >> 1) + 2 * h + (i & 1)) ^ (c31 & 7)) << 4);
      PIN(zw[i]);
    }
    PIN(zr); PIN(zg); PIN(ma); PIN(evo); PIN(eco);
    STAMP(4);
    if (w == 0) poll_flags(rw, (unsigned)(WS_HINT + k1 * 128), tag);
    asm volatile("s_barrier" ::: "memory");
    {
      u32x4 ef[8];
      unsigned ec[8];
      STAMP(5);
      sfor<0, 8>([&](auto S) {
        constexpr int s = decltype(S)::v;
        ld128_sc1<0, 0>(ef[s], rw, evo, (unsigned)(((s >> 2) + 4 * (s & 3)) * E_PN));
        ld32s_sc1<0, 0>(ec[s], rw, eco, (unsigned)(((s >> 2) + 4 * (s & 3)) * 28672));
      });
      f32x16 ac[2];
      f32x2 pr[2][8], pi[2][8];
      u32x4 mi[12];
      auto pair = [&](const f32x16& v, int i) { return f32x2{v[2 * i], v[2 * i + 1]}; };
      sfor<0, 2>([&](auto HF) {
        constexpr int hf = decltype(HF)::v;
#pragma unroll
        for (int i = 0; i < 16; ++i) { ac[0][i] = 0.f; ac[1][i] = 0.f; }
        sfor<0, 4>([&](auto S) {
          constexpr int s = 4 * hf + decltype(S)::v;
          waitv<14 - 2 * s, 0>();
          PIN(ef[s]); PIN(ec[s]);
          {
            if (__builtin_expect(mism((tag + ef[s].x + ef[s].y) + ef[s].z + ef[s].w, ec[s]) != 0, 0))
            { RETRY(16 + s); reload_e(ef[s], ec[s], rw, evo, eco, (unsigned)(((s >> 2) + 4 * (s & 3)) * E_PN), tag); }
          }
          ac[0] = mfma32(fm[2 * s], ef[s], ac[0]);
          ac[1] = mfma32(fm[2 * s + 1], ef[s], ac[1]);
          SB();
        });
        if constexpr (hf == 1) {
#pragma unroll
          for (int i = 0; i < 12; ++i) mi[i] = lds128(ma + i * 1024);
          st32_sc1<0>(rw, tid >= 480 ? (unsigned)(WS_FLAGZ + k1 * 128 + (tid - 480) * 4) : OOB, 0u, tag);
          STAMP(6);
        }
#pragma unroll
        for (int par = 0; par < 2; ++par) {
          const f32x2* ur = (par ^ hf) ? wmr : wpr;
          const f32x2* ui = (par ^ hf) ? wmi : wpi;
#pragma unroll
          for (int i = 0; i < 8; ++i) {
            if (hf == 0) { pr[par][i] = pair(ac[0], i) * ur[i]; pi[par][i] = pair(ac[0], i) * ui[i]; }
            else { pr[par][i] += pair(ac[0], i) * ur[i]; pi[par][i] += pair(ac[0], i) * ui[i]; }
          }
          SB();
#pragma unroll
          for (int i = 0; i < 8; ++i) { pr[par][i] -= pair(ac[1], i) * ui[i]; pi[par][i] += pair(ac[1], i) * ur[i]; }
          SB();
          if constexpr (hf == 1) {
            f32x16 zre, zim;
#pragma unroll
            for (int i = 0; i < 16; ++i) { zre[i] = 0.f; zim[i] = 0.f; }
#pragma unroll
            for (int s2 = 0; s2 < 2; ++s2) {
              const f32x2 *a_ = pr[par] + 4 * s2, *b_ = pi[par] + 4 * s2;
              const u32x4 fr = {pk2(a_[0][0], a_[0][1]), pk2(a_[1][0], a_[1][1]), pk2(a_[2][0], a_[2][1]), pk2(a_[3][0], a_[3][1])};
              const u32x4 fi = {pk2(b_[0][0], b_[0][1]), pk2(b_[1][0], b_[1][1]), pk2(b_[2][0], b_[2][1]), pk2(b_[3][0], b_[3][1])};
              const u32x4* mm = mi + (par * 2 + s2) * 3;
              zre = mfma32(fr, mm[0], zre);
              zim = mfma32(fr, mm[1], zim);
              zre = mfma32(fi, mm[2], zre);
              zim = mfma32(fi, mm[0], zim);
            }
            SB();
            u32x2 zp[2][4];
#pragma unroll
            for (int g4 = 0; g4 < 4; ++g4) {
              zp[0][g4] = u32x2{pk2(zre[4 * g4], zre[4 * g4 + 1]), pk2(zre[4 * g4 + 2], zre[4 * g4 + 3])};
              zp[1][g4] = u32x2{pk2(zim[4 * g4], zim[4 * g4 + 1]), pk2(zim[4 * g4 + 2], zim[4 * g4 + 3])};
              if (g4 < 3) {
                *(LAS u32x2*)(uintptr_t)(zw[g4 >> 1] + (g4 & 1) * 8) = zp[0][g4];
                *(LAS u32x2*)(uintptr_t)(zw[2 + (g4 >> 1)] + (g4 & 1) * 8) = zp[1][g4];
              }
            }
            {
              unsigned t3 = (zp[0][3].x + zp[0][3].y + zp[1][3].x) + zp[1][3].y;
              unsigned sm = csum8(h ? 0u : t3, cat(zp[0][0], zp[0][1]), cat(zp[1][0], zp[1][1]));
              sm = (sm + zp[0][2].x + zp[0][2].y) + zp[1][2].x + zp[1][2].y;
              auto sw = __builtin_amdgcn_permlane32_swap(sm, sm, false, false);
              const unsigned a0 = sw[0], a1 = sw[1], V = tag - a0 - a1, V2 = (V & 0x40004000u) >> 1;
              *(LAS u32x2*)(uintptr_t)(zw[1] + 8) = h ? u32x2{V & ~0x40004000u, V2} : zp[0][3];
              *(LAS u32x2*)(uintptr_t)(zw[3] + 8) = h ? u32x2{V2, 0u} : zp[1][3];
            }
            u32x4 v[4];
#pragma unroll
            for (int i = 0; i < 4; ++i) v[i] = lds128(zr + i * 1024);
            const unsigned zb = (unsigned)(WS_Z + par * 4 * Z_G + w * 4096 + k1 * 128);
#pragma unroll
            for (int i = 0; i < 4; ++i) st128_sc1<0, 0>(rw, zg, zb + i * (16 * Z_G), v[i]);
            SB();
          }
        }
      });
      STAMP(7);
      asm volatile("gf_code_end:");
    }
  }
}

extern "C" void kernel_launch(void* const* d_in, const int* in_sizes, int n_in, void* d_out, int out_size, void* d_ws,
                              size_t ws_size, hipStream_t stream) {
  (void)in_sizes; (void)n_in; (void)out_size; (void)ws_size;
  (void)hipFuncSetAttribute((const void*)gf_glu_kernel, hipFuncAttributeMaxDynamicSharedMemorySize, L_TOTAL);
  gf_glu_kernel<<<256, 512, L_TOTAL, stream>>>((const char*)d_in[0], (const char*)d_in[1], (const char*)d_in[2],
                                               (const char*)d_in[4], (const char*)d_in[5], (const char*)d_in[6],
                                               (char*)d_out, (char*)d_ws);
}
```

```cpp
#include <hip/hip_runtime.h>
#include <hip/hip_bf16.h>
#include <stdint.h>

typedef __bf16 bf16x8 __attribute__((ext_vector_type(8)));
typedef __bf16 bf16x2 __attribute__((ext_vector_type(2)));
typedef short s16x4 __attribute__((ext_vector_type(4)));
typedef float f32x2 __attribute__((ext_vector_type(2)));
typedef float f32x4 __attribute__((ext_vector_type(4)));
typedef float f32x16 __attribute__((ext_vector_type(16)));
typedef unsigned u32x4 __attribute__((ext_vector_type(4)));
typedef unsigned u32x2 __attribute__((ext_vector_type(2)));
#define LAS __attribute__((address_space(3)))
#define DI __device__ __forceinline__

constexpr unsigned WS_EPOCH = 0;
constexpr unsigned WS_STAT = 65536;
constexpr unsigned WS_CS = 131072;
constexpr unsigned WS_FLAGZ = 196608;
constexpr unsigned WS_HINT = 229376;
constexpr unsigned WS_EC = 524288;
constexpr unsigned WS_E = 1u << 20;
constexpr unsigned WS_Z = 3u << 20;
constexpr unsigned WS_BYTES = 14u << 20;
constexpr unsigned WS_H = 0x520000;
constexpr unsigned HBLK = 4608;
constexpr unsigned E_PN = 3584, E_K1 = 16 * 3584 + 256, Z_G = 32768 + 640;

constexpr unsigned L_AUX = 131072;
constexpr unsigned L_PRIV = L_AUX;
constexpr unsigned L_ZOK = L_AUX + 3072;
constexpr unsigned L_EB = L_AUX + 3584;
constexpr unsigned L_PAUX = L_AUX + 12288;
constexpr unsigned L_P1 = L_AUX + 28672;
constexpr unsigned L_TOTAL = 163840;

constexpr unsigned SPIN_CAP = 1u << 17;

template <int N> struct IC { static constexpr int v = N; };
template <int B, int E, class Fn> DI void sfor(Fn&& f) {
  if constexpr (B < E) { f(IC<B>{}); sfor<B + 1, E>(f); }
}

DI unsigned pk2(float a, float b) { f32x2 t = {a, b}; bf16x2 p = __builtin_convertvector(t, bf16x2); return __builtin_bit_cast(unsigned, p); }
DI bf16x8 asbf(u32x4 v) { return __builtin_bit_cast(bf16x8, v); }
DI float asf(unsigned u) { return __builtin_bit_cast(float, u); }
DI unsigned asu(float f) { return __builtin_bit_cast(unsigned, f); }
DI float bflo(unsigned u) { return asf(u << 16); }
DI float bfhi(unsigned u) { return asf(u & 0xffff0000u); }
DI f32x4 mfma16(u32x4 a, u32x4 b, f32x4 c) { return __builtin_amdgcn_mfma_f32_16x16x32_bf16(asbf(a), asbf(b), c, 0, 0, 0); }
DI f32x16 mfma32(u32x4 a, u32x4 b, f32x16 c) { return __builtin_amdgcn_mfma_f32_32x32x16_bf16(asbf(a), asbf(b), c, 0, 0, 0); }
DI void mma(f32x4& c, const u32x4& a, const u32x4& b) { asm volatile("v_mfma_f32_16x16x32_bf16 %0, %1, %2, %0" : "+v"(c) : "v"(a), "v"(b)); }
DI void mma0(f32x4& c, const u32x4& a, const u32x4& b) { asm volatile("v_mfma_f32_16x16x32_bf16 %0, %1, %2, 0" : "=&v"(c) : "v"(a), "v"(b)); }
DI void mmac(f32x4& d, const u32x4& a, const u32x4& b, const f32x4& c) { asm volatile("v_mfma_f32_16x16x32_bf16 %0, %1, %2, %3" : "=&v"(d) : "v"(a), "v"(b), "v"(c)); }
DI float dot2(unsigned a, unsigned b, float c) {
  return __builtin_amdgcn_fdot2_f32_bf16(__builtin_bit_cast(bf16x2, a), __builtin_bit_cast(bf16x2, b), c, false);
}
DI float sum8(u32x4 v, float c) { unsigned a = v.x, b = v.y, d = v.z, e = v.w; const unsigned o = 0x3f803f80u;
  return dot2(e, o, dot2(d, o, dot2(b, o, dot2(a, o, c)))); }
DI float sq8(u32x4 v, float c) { unsigned a = v.x, b = v.y, d = v.z, e = v.w;
  return dot2(e, e, dot2(d, d, dot2(b, b, dot2(a, a, c)))); }
template <int CTRL> DI float dpp(float v) { return asf((unsigned)__builtin_amdgcn_update_dpp(0, (int)asu(v), CTRL, 0xf, 0xf, true)); }
DI u32x4 lds128(unsigned a) { return *(LAS const u32x4*)(uintptr_t)a; }
DI f32x4 lds128f(unsigned a) { return *(LAS const f32x4*)(uintptr_t)a; }
DI u32x2 ldstr1(unsigned a) { return __builtin_bit_cast(u32x2, __builtin_amdgcn_ds_read_tr16_b64_v4i16((LAS s16x4*)(uintptr_t)a)); }
DI u32x4 cat(u32x2 lo, u32x2 hi) { return __builtin_shufflevector(lo, hi, 0, 1, 2, 3); }
DI u32x4 ldstr(unsigned a0, unsigned a1) {
  s16x4 lo = __builtin_amdgcn_ds_read_tr16_b64_v4i16((LAS s16x4*)(uintptr_t)a0);
  s16x4 hi = __builtin_amdgcn_ds_read_tr16_b64_v4i16((LAS s16x4*)(uintptr_t)a1);
  return __builtin_bit_cast(u32x4, __builtin_shufflevector(lo, hi, 0, 1, 2, 3, 4, 5, 6, 7));
}
typedef u32x4 rsrc_t;
DI rsrc_t mk_rsrc(const void* p, unsigned bytes) {
  const unsigned long long a = (unsigned long long)p;
  rsrc_t d = {(unsigned)a, (unsigned)(a >> 32) & 0xffffu, bytes, 0x00020000u};
  asm volatile("" : "+s"(d));
  return d;
}
template <int ID, int LIMM, int MUL> DI void dma_mul(rsrc_t rs, unsigned voff, unsigned lbase, unsigned k) {
  unsigned t;
  asm volatile("s_add_u32 m0, %3, %5\n\ts_mul_i32 %0, %4, %6\n\tbuffer_load_dwordx4 %1, %2, %0 offen lds ; OP %7"
               : "=&s"(t) : "v"(voff), "s"(rs), "s"(lbase), "s"(k), "i"(LIMM), "i"(MUL), "i"(ID) : "memory", "scc");
}
template <int ID, int LIMM, int ADD> DI void dma_add(rsrc_t rs, unsigned voff, unsigned lbase, unsigned k) {
  unsigned t;
  asm volatile("s_add_u32 m0, %3, %5\n\ts_add_u32 %0, %4, %6\n\tbuffer_load_dwordx4 %1, %2, %0 offen lds ; OP %7"
               : "=&s"(t) : "v"(voff), "s"(rs), "s"(lbase), "s"(k), "i"(LIMM), "i"(ADD), "i"(ID) : "memory", "scc");
}
template <int ID, int LIMM, int ADD> DI void dma_add_sc1(rsrc_t rs, unsigned voff, unsigned lbase, unsigned k) {
  unsigned t;
  asm volatile("s_add_u32 m0, %3, %5\n\ts_add_u32 %0, %4, %6\n\tbuffer_load_dwordx4 %1, %2, %0 offen sc1 lds ; OP %7"
               : "=&s"(t) : "v"(voff), "s"(rs), "s"(lbase), "s"(k), "i"(LIMM), "i"(ADD), "i"(ID) : "memory", "scc");
}
template <int ID, int IMM> DI void st128_sc1(rsrc_t rs, unsigned voff, unsigned soff, u32x4 v) {
  asm volatile("buffer_store_dwordx4 %0, %1, %2, %3 offen offset:%4 sc1 ; OP %5\n\ts_nop 1" ::"v"(v), "v"(voff), "s"(rs), "Is"(soff), "i"(IMM), "i"(ID) : "memory");
}
template <int ID> DI void st64_sc1(rsrc_t rs, unsigned voff, unsigned soff, u32x2 v) {
  asm volatile("buffer_store_dwordx2 %0, %1, %2, %3 offen sc1 ; OP %4" ::"v"(v), "v"(voff), "s"(rs), "Is"(soff), "i"(ID) : "memory");
}
template <int ID> DI void st32_sc1(rsrc_t rs, unsigned voff, unsigned soff, unsigned v) {
  asm volatile("buffer_store_dword %0, %1, %2, %3 offen sc1 ; OP %4" ::"v"(v), "v"(voff), "s"(rs), "Is"(soff), "i"(ID) : "memory");
}
template <int ID, int IMM> DI void ld128_sc1(u32x4& dst, rsrc_t rs, unsigned voff, unsigned soff) {
  asm volatile("buffer_load_dwordx4 %0, %1, %2, %3 offen offset:%4 sc1 ; OP %5" : "=v"(dst) : "v"(voff), "s"(rs), "Is"(soff), "i"(IMM), "i"(ID) : "memory");
}
template <int ID, int IMM> DI void ld128(u32x4& dst, rsrc_t rs, unsigned voff, unsigned soff) {
  asm volatile("buffer_load_dwordx4 %0, %1, %2, %3 offen offset:%4 ; OP %5" : "=v"(dst) : "v"(voff), "s"(rs), "Is"(soff), "i"(IMM), "i"(ID) : "memory");
}
template <int ID, int IMM> DI void ld32s_sc1(unsigned& dst, rsrc_t rs, unsigned voff, unsigned soff) {
  asm volatile("buffer_load_dword %0, %1, %2, %3 offen offset:%4 sc1 ; OP %5" : "=v"(dst) : "v"(voff), "s"(rs), "s"(soff), "i"(IMM), "i"(ID) : "memory");
}
template <int ID> DI void ld32_plain(unsigned& dst, rsrc_t rs, unsigned voff) {
  asm volatile("buffer_load_dword %0, %1, %2, 0 offen ; OP %3" : "=v"(dst) : "v"(voff), "s"(rs), "i"(ID) : "memory");
}
template <int ID> DI void ld32_sc1(unsigned& dst, rsrc_t rs, unsigned voff) {
  asm volatile("buffer_load_dword %0, %1, %2, 0 offen sc1 ; OP %3" : "=v"(dst) : "v"(voff), "s"(rs), "i"(ID) : "memory");
}
template <int N, int NEED> DI void waitv() { asm volatile("s_waitcnt vmcnt(%0) ; NEED %1" ::"i"(N), "i"(NEED) : "memory"); }
template <int NEED> DI void need() { asm volatile("; NEED %0" ::"i"(NEED) : "memory"); }
#define BARRIER() asm volatile("s_waitcnt lgkmcnt(0)\n\ts_barrier" ::: "memory")
#define SB() __builtin_amdgcn_sched_barrier(0)
#define ONLY(g) asm volatile("; ONLY " #g ::: "memory")
DI void keep(const f32x4& a, const f32x4& b) { asm volatile("" ::"v"(a), "v"(b)); }
template <class T> DI void PINS(T& x) { asm volatile("" : "+s"(x)); }
DI float opq(float x) { asm volatile("" : "+v"(x)); return x; }
template <class T> DI void PIN(T& x) { asm volatile("" : "+v"(x)); }

DI void poll_flags(rsrc_t rs, unsigned voff, unsigned tag) {
#pragma nounroll
  for (unsigned spins = 0; spins < SPIN_CAP; ++spins) {
    unsigned got;
    asm volatile("buffer_load_dword %0, %1, %2, 0 offen sc1\n\ts_waitcnt vmcnt(0)" : "=v"(got) : "v"(voff), "s"(rs) : "memory");
    if (__all(got == tag)) break;
    __builtin_amdgcn_s_sleep(3);
  }
}
DI void poll_granules(u32x4& g, rsrc_t rs, unsigned voff, unsigned tag) {
#pragma nounroll
  for (unsigned spins = 0; spins < SPIN_CAP; ++spins) {
    __builtin_amdgcn_s_sleep(4);
    asm volatile("buffer_load_dwordx4 %0, %1, %2, 0 offen sc1\n\ts_waitcnt vmcnt(0)" : "=&v"(g) : "v"(voff), "s"(rs) : "memory");
    if (__all(g.y == tag && g.w == tag)) break;
  }
}
DI unsigned csum8(unsigned t, const u32x4& a, const u32x4& b) { return ((t + a.x + a.y) + a.z + a.w) + ((b.x + b.y + b.z) + b.w); }
DI void reload_h(u32x4* d, unsigned* ck, rsrc_t rs, unsigned vo, unsigned vc, unsigned so, unsigned tag) {
#pragma nounroll
  for (unsigned spins = 0; spins < SPIN_CAP; ++spins) {
    __builtin_amdgcn_s_sleep(8);
    asm volatile("buffer_load_dwordx4 %0, %6, %8, %9 offen sc1\n\tbuffer_load_dwordx4 %1, %6, %8, %9 offen offset:1024 sc1\n\t"
                 "buffer_load_dwordx4 %2, %6, %8, %9 offen offset:2048 sc1\n\tbuffer_load_dwordx4 %3, %6, %8, %9 offen offset:3072 sc1\n\t"
                 "buffer_load_dword %4, %7, %8, %9 offen sc1\n\tbuffer_load_dword %5, %7, %8, %9 offen offset:256 sc1\n\ts_waitcnt vmcnt(0)"
                 : "=&v"(d[0]), "=&v"(d[1]), "=&v"(d[2]), "=&v"(d[3]), "=&v"(ck[0]), "=&v"(ck[1])
                 : "v"(vo), "v"(vc), "s"(rs), "s"(so) : "memory");
    if (__all(csum8(tag, d[0], d[1]) == ck[0] && csum8(tag, d[2], d[3]) == ck[1])) break;
  }
}
DI void reload_e(u32x4& d, unsigned& ck, rsrc_t rs, unsigned vo, unsigned vc, unsigned so, unsigned tag) {
#pragma nounroll
  for (unsigned spins = 0; spins < SPIN_CAP; ++spins) {
    __builtin_amdgcn_s_sleep(10);
    asm volatile("buffer_load_dwordx4 %0, %2, %4, %5 offen sc1\n\tbuffer_load_dword %1, %3, %4, %6 offen sc1\n\ts_waitcnt vmcnt(0)"
                 : "=&v"(d), "=&v"(ck) : "v"(vo), "v"(vc), "s"(rs), "s"(so), "s"(so * 8) : "memory");
    if (__all((tag + d.x + d.y) + d.z + d.w == ck)) break;
  }
}
DI unsigned long long mism(unsigned v, unsigned tag) { unsigned long long m_ = __builtin_amdgcn_ballot_w64(v != tag); asm volatile("" : "+s"(m_)); return m_; }

#define STAMP(i)
#define RETRY(i)

constexpr int kTileOfX[7] = {0, 2, 3, 5, 6, 8, 9};
constexpr int kTileOfW[14] = {1, 4, 7, 10, 11, 12, 13, 14, 15, 16, 17, 18, 19, 20};
constexpr int kTileKind[21] = {0, 1, 0, 0, 1, 0, 0, 1, 0, 0, 1, 1, 1, 1, 2, 2, 2, 2, 2, 2, 2};
constexpr int kTileIdx[21] = {0, 0, 1, 2, 1, 3, 4, 2, 5, 6, 3, 4, 5, 6, 0, 1, 2, 3, 4, 5, 6};
constexpr int kSegX0[4] = {0, 1, 3, 5}, kSegNX[4] = {1, 2, 2, 2};
constexpr int kIssue0[5] = {5, 8, 10, 13, 16};
constexpr int tile_of_x(int j) { return kTileOfX[j]; }
constexpr int tile_of_w(int s) { return kTileOfW[s]; }
constexpr int tile_kind(int T) { return kTileKind[T]; }
constexpr int tile_idx(int T) { return kTileIdx[T]; }
constexpr unsigned piece_loc(int g, int P) {
  constexpr unsigned pr[8] = {L_PAUX, 5 * 16384, 6 * 16384, 7 * 16384, 0, 16384, 2 * 16384, 3 * 16384};
  return pr[(g ? P % 8 : P % 6)];
}
enum { OP_EP = 29, OP_P = 30, OP_GS = 50, OP_GC1, OP_GC2, OP_E, OP_H1, OP_H2, OP_POLL, OP_XRES, OP_FL, OP_OPT = 59, OP_HE = 60, OP_HL = 70, OP_Z = 80 };

__global__ __launch_bounds__(512, 2) void gf_glu_kernel(const char* x, const char* wr, const char* wi, const char* wg,
                                                        const char* wu, const char* wd, char* out, char* ws) {
  extern __shared__ __attribute__((aligned(16))) char smem[];
  STAMP(0);
  const int tid = threadIdx.x;
  const int w = __builtin_amdgcn_readfirstlane(tid >> 6);
  const int l = tid & 63, r = l & 15, q = l >> 4;
  const int b = blockIdx.x, xcd = b & 7, bi = b >> 3;
  const unsigned L0 = (unsigned)(uintptr_t)(LAS char*)smem;

  unsigned code_touch;
  {
    unsigned long long pc;
    unsigned code_bytes;
    asm volatile("s_getpc_b64 %0\ngf_code_begin:\n\ts_mov_b32 %1, gf_code_end-gf_code_begin" : "=s"(pc), "=s"(code_bytes));
    const char* pcb = (const char*)(pc & ~127ull);
    unsigned off = (unsigned)((bi * 16 + w * 2 + (l & 1)) * 128);
    off = off < code_bytes ? off : 0;
    asm volatile("global_load_dword %0, %1, %2" : "=v"(code_touch) : "v"(off), "s"(pcb) : "memory");
  }
  unsigned epoch;
#define LOAD_EPOCH() asm volatile("global_load_dword %0, %1, %2 ; OP %3" : "=v"(epoch) : "v"(0u), "s"(ws), "i"(OP_EP) : "memory")
#define GET_TAG() PIN(epoch); PIN(code_touch); const unsigned tag = (unsigned)__builtin_amdgcn_readfirstlane(epoch) * 0x9E3779B1u + 0x7F4A7C15u
  constexpr unsigned OOB = 0x40000000u;

  if (__builtin_expect(bi >= 4, 1)) {
    const int bj = bi - 4;
    const int pn = 4 * (xcd & 3) + (bj & 3), tt = 7 * (xcd >> 2) + (bj >> 2);
    int rot = 2 * (xcd & 3) + 3 * (xcd >> 2);
    rot = rot >= 7 ? rot - 7 : rot;
    const int m = w & 3, nh = w >> 2;
    const int rt = 4 * xcd + (bj & 3), ct = bj >> 2;
    unsigned kx[7];
    kx[0] = (unsigned)(rot * 128);
    unsigned xoff[2], woff, doff;
    const int lr = l >> 3, lc = l & 7;
    {
      const int R = 16 * (w & 1) + lr;
      const int n2 = (pn & 1) + 8 * (pn >> 1) + 2 * (w >> 1);
      const unsigned rb = (unsigned)((64 * R + n2) * 896), cx = (unsigned)((lc ^ (lr >> 1)) << 4);
      xoff[0] = rb + cx;
      xoff[1] = rb + 8 * 64 * 896 + (cx ^ 64);
      const int swz = ((lr >> 1) & 1) | ((w & 1) << 1);
      woff = (unsigned)((8 * w + lr) * 3584 + tt * 256 + ((lc ^ (swz << 1)) << 4));
    }
    unsigned lw1 = L0 + w * 1024, lw2 = L0 + w * 2048;
    PINS(lw1); PINS(lw2); PINS(kx[0]); PIN(xoff[0]); PIN(xoff[1]); PIN(woff);
    SB();
    const rsrc_t rx = mk_rsrc(x, 2048 * 896), rg = mk_rsrc(wg, 448 * 3584), ru = mk_rsrc(wu, 448 * 3584);
    auto issue1 = [&](auto TI, auto HI) {
      constexpr int T = decltype(TI)::v, h = decltype(HI)::v;
      constexpr int kind = tile_kind(T), sl = (T & 7) * 16384;
      if constexpr (T == 14 && h == 0) { woff += 128; PIN(woff); }
      if constexpr (kind == 0) dma_mul<T, sl + h * 1024, 1>(rx, xoff[h], lw2, kx[tile_idx(T)]);
      else dma_mul<T, sl + h * 8192, 1792>(h ? ru : rg, woff, lw1, kx[tile_idx(T)]);
    };
    auto issue = [&](auto TI) { issue1(TI, IC<0>{}); issue1(TI, IC<1>{}); };
    issue(IC<0>{});
    issue(IC<1>{});
    LOAD_EPOCH();
    STAMP(1);
    SB();
#pragma unroll
    for (int j = 1; j < 7; ++j) { kx[j] = kx[j - 1] == 768 ? 0 : kx[j - 1] + 128; PINS(kx[j]); }
    SB();

    {
      int c = 16 * tt + 8 * nh + (r >> 1);
      const float ph = (r & 1) ? 0.25f : 0.0f;
      c = c ? c : 224;
      const float cfl = (float)c, cf = cfl * (1.0f / 448.0f);
      const float k2 = 2.0f * __builtin_amdgcn_cosf(cf);
#pragma unroll
      for (int it = 0; it < 4; ++it) {
        const int ks = m + 4 * it;
        if (it < 3 || m < 2) {
          const unsigned kk = (m & 2) ? kx[it < 3 ? 2 * it + 1 : 0] : kx[2 * it];
          const float t = cfl * (float)(int)((kk >> 1) + 32 * (m & 1) + 8 * q);
          const float rem = __builtin_fmaf(-448.0f, __builtin_floorf(t * (1.0f / 448.0f)), t);
          const float rev = __builtin_fmaf(rem, 1.0f / 448.0f, ph);
          float v[8];
          v[0] = __builtin_amdgcn_cosf(rev);
          v[1] = __builtin_amdgcn_cosf(rev + cf);
#pragma unroll
          for (int e = 2; e < 8; ++e) v[e] = __builtin_fmaf(k2, v[e - 1], -v[e - 2]);
          *(LAS u32x4*)(uintptr_t)(L0 + L_AUX + (nh * 14 + ks) * 1024 + l * 16) =
              u32x4{pk2(v[0], v[1]), pk2(v[2], v[3]), pk2(v[4], v[5]), pk2(v[6], v[7])};
        } else {
          const int k1 = 16 * (m & 1) + r;
          float v[4];
#pragma unroll
          for (int e = 0; e < 4; ++e)
            v[e] = __builtin_amdgcn_cosf((float)((k1 * (4 * q + e)) & 31) * (1.0f / 32.0f) - (nh ? 0.25f : 0.0f));
          const unsigned a0 = pk2(v[0], v[1]), a1 = pk2(v[2], v[3]), sg = (r & 1) ? 0x80008000u : 0u;
          *(LAS u32x4*)(uintptr_t)(L0 + L_P1 + (2 * nh + (m & 1)) * 1024 + l * 16) = u32x4{a0, a1, a0 ^ sg, a1 ^ sg};
        }
      }
    }
    unsigned xa[2][2], wa[2][2];
    {
      const int R = 32 * m + r;
      xa[0][0] = L0 + R * 128 + ((q ^ ((R >> 1) & 7)) << 4);
      xa[0][1] = xa[0][0] ^ 64;
      const int qq = r >> 2, pp = r & 3;
      const int swz = ((qq >> 1) & 1) | ((q & 1) << 1);
      wa[0][0] = L0 + (8 * q + qq) * 128 + (((2 * nh) ^ swz) << 5) + pp * 8;
      wa[0][1] = wa[0][0] ^ 32;
#pragma unroll
      for (int i = 0; i < 2; ++i) { xa[1][i] = xa[0][i] + 65536; wa[1][i] = wa[0][i] + 65536; PIN(xa[1][i]); PIN(wa[1][i]); PIN(xa[0][i]); PIN(wa[0][i]); }
    }
    unsigned l16 = l * 16, aux16 = L0 + L_AUX + l * 16, fad = aux16 + nh * 14336;
    PIN(l16); PIN(aux16); PIN(fad);
    STAMP(2);
    SB();
    const rsrc_t rd = mk_rsrc(wd, 1792 * 896), rw = mk_rsrc(ws, WS_BYTES);
    const rsrc_t rtch = mk_rsrc(nh ? wd : ((m & 1) ? wi : wr), nh ? 1792 * 896 : 921600);
    const int g1 = 4 * pn + m;
    unsigned pso[9], hso[9];
    unsigned hst_d, hst_c, ecs, gs_off, gc_off, pq;
    const unsigned priv = L0 + L_PRIV + w * 256;
    auto step_setup = [&](int g, int s) {
      const int ps = s >= 3 + g;
      int v = (s < 3 ? 8 * s + w : m + (g == 0 ? 8 * (s - 3) : (s == 3 ? 24 : (s == 4 ? 4 : 4 * s - 8))));
      pso[s] = (unsigned)((128 * (v >> 1) + 64 * ps + 32 * (v & 1)) * 896);
      hso[s] = (unsigned)(WS_H + (rt * 56 + 28 * ps + v) * HBLK);
      PINS(pso[s]); PINS(hso[s]);
    };
    constexpr unsigned S_ = 16384;
    auto issue_p1 = [&](auto GI, auto PI, auto HI) {
      constexpr int P = decltype(PI)::v, h = decltype(HI)::v;
      dma_add<OP_P + P, piece_loc(decltype(GI)::v, P) + h * 1024, (16 * (P & 1) + 8 * h) * 896>(rd, doff, lw2, pso[P >> 1]);
    };

    u32x4 xf[14][2], ff[4];
    u32x2 fa_[4][2], fb_[4][2];
    f32x4 accD[2], acc[2][4][2];
    float mrow[2], r2row[2];
    u32x4 gran, ebv;
    unsigned pollv;
    u32x4 hb[4][4];
    {
      const unsigned hst = WS_H + (unsigned)((g1 >> 1) * 56 + 2 * tt + nh) * HBLK;
      hst_d = hst + (m & 1) * 2048; hst_c = hst + (m & 1) * 256;
      ecs = (unsigned)(WS_EC + (pn * 14 + tt) * 2048);
      PINS(hst_d); PINS(hst_c); PINS(ecs);
    }
    SB();
    issue(IC<2>{});
    issue(IC<4>{});
    unsigned tag, tch, tch2, tco;
    f32x4 cs[4];
    unsigned hpk[2][4];
    auto h_part = [&](auto PS, int i2, int sub) {
      constexpr int ps = decltype(PS)::v;
      f32x4 h0 = ((acc[ps][sub][i2] - mrow[i2] * cs[sub]) * r2row[i2]) * (acc[ps][sub + 2][i2] - mrow[i2] * cs[sub + 2]);
      hpk[i2][2 * sub] = pk2(h0[0], h0[1]); hpk[i2][2 * sub + 1] = pk2(h0[2], h0[3]);
      PIN(hpk[i2][2 * sub]); PIN(hpk[i2][2 * sub + 1]);
    };
    auto h_store = [&](auto PS, auto I2) {
      constexpr int ps = decltype(PS)::v, i2 = decltype(I2)::v;
      st128_sc1<OP_H1 + ps, i2 * 1024>(rw, l16, hst_d + ps * (28 * HBLK), u32x4{hpk[i2][0], hpk[i2][1], hpk[i2][2], hpk[i2][3]});
    };
    unsigned hck[4][2], l4c, hsum;
    auto h_check_store = [&](auto PS) { st32_sc1<OP_H1 + decltype(PS)::v>(rw, l4c, hst_c + decltype(PS)::v * (28 * HBLK), hsum); };
    auto load_h1 = [&](auto SI, auto II) {
      constexpr int s = decltype(SI)::v, i = decltype(II)::v;
      if constexpr (i < 4) ld128_sc1<OP_HE + s, (i & 3) * 1024>(hb[s & 3][i & 3], rw, l16, hso[s]);
      else ld32s_sc1<OP_HE + s, (i & 1) * 256>(hck[s & 3][i & 1], rw, l4c, hso[s]);
    };
    u32x4 p1c[2], p1s[2], df;
    f32x4 rD[2], tc[2], ts[2];
    float rsr[1], dv[8], ev[4];
    unsigned dpk[4], ea, eo, ebo;
    auto hop = [&](auto OI) {
      constexpr int o = decltype(OI)::v, sub = o / 36, i2 = (o / 18) & 1, i = o % 18, e = i & 3;
      if constexpr (i < 4) dv[e] = opq(__builtin_fmaf(-mrow[i2], cs[sub][e], acc[0][sub][i2][e]));
      else if constexpr (i < 8) ev[e] = opq(__builtin_fmaf(-mrow[i2], cs[sub + 2][e], acc[0][sub + 2][i2][e]));
      else if constexpr (i < 12) dv[e] = opq(dv[e] * r2row[i2]);
      else if constexpr (i < 16) dv[e] = opq(dv[e] * ev[e]);
      else { hpk[i2][2 * sub + (i & 1)] = pk2(dv[2 * (i & 1)], dv[2 * (i & 1) + 1]); PIN(hpk[i2][2 * sub + (i & 1)]); }
    };
    u32x4 gf[2][2][4];
    float g_d[4], g_f[4], g_k[4], g_t[4], g_u[4], g_c[4][3], g_s[4][3], g_cq;
    unsigned g_mk;
    unsigned g_ny;
    auto gop = [&](auto OI) {
      constexpr int o = decltype(OI)::v, hv = o / 136, a = o & 3, dt = a, i = (o % 136) >> 2;
      if constexpr (hv == 1 && (i == 0 || i == 2 || i == 4 || i == 10)) { if constexpr (o == 136) g_cq = opq(g_cq + 28.0f); }
      else if constexpr (i == 0) g_d[a] = opq((float)(64 * ct + 16 * dt + r));
      else if constexpr (i == 1) g_t[a] = opq(g_d[a] * g_cq);
      else if constexpr (i == 2) g_f[a] = opq(g_d[a] * (1.0f / 448.0f));
      else if constexpr (i == 3) g_u[a] = opq(g_t[a] * (1.0f / 448.0f));
      else if constexpr (i == 4) g_k[a] = opq(__builtin_amdgcn_cosf(g_f[a]));
      else if constexpr (i == 5) g_u[a] = opq(__builtin_floorf(g_u[a]));
      else if constexpr (i == 6) g_t[a] = opq(__builtin_fmaf(-448.0f, g_u[a], g_t[a]));
      else if constexpr (i == 7) g_t[a] = opq(g_t[a] * (-1.0f / 448.0f));
      else if constexpr (i == 8) g_u[a] = opq(g_t[a] - g_f[a]);
      else if constexpr (i == 9) g_c[a][0] = opq(__builtin_amdgcn_cosf(g_t[a]));
      else if constexpr (i == 10) g_k[a] = opq(g_k[a] + g_k[a]);
      else if constexpr (i == 11) g_s[a][0] = opq(__builtin_amdgcn_sinf(g_t[a]));
      else if constexpr (i == 12) { unsigned p_ = pk2(g_c[a][0], g_s[a][0]); if constexpr (hv == 0) p_ ^= g_ny; PIN(p_); gf[hv][0][dt][0] = p_; }
      else if constexpr (i == 13) g_c[a][1] = opq(__builtin_amdgcn_cosf(g_u[a]));
      else if constexpr (i == 14) g_s[a][1] = opq(__builtin_amdgcn_sinf(g_u[a]));
      else if constexpr (i == 15) { unsigned p_ = pk2(g_c[a][1], g_s[a][1]); PIN(p_); gf[hv][0][dt][1] = p_; }
      else {
        constexpr int e = 2 + (i - 16) / 3, j = (i - 16) % 3;
        if constexpr (j == 0) g_c[a][e % 3] = opq(__builtin_fmaf(g_k[a], g_c[a][(e - 1) % 3], -g_c[a][(e - 2) % 3]));
        else if constexpr (j == 1) g_s[a][e % 3] = opq(__builtin_fmaf(g_k[a], g_s[a][(e - 1) % 3], -g_s[a][(e - 2) % 3]));
        else { unsigned p_ = pk2(g_c[a][e % 3], g_s[a][e % 3]); if constexpr (e >= 4) p_ &= g_mk; PIN(p_); gf[hv][e >> 2][dt][e & 3] = p_; }
      }
    };

    unsigned long long bad0, bad1;
    unsigned da[3][4];
    u32x2 af[2][4][2];
    auto rda1 = [&](int g, int s, int dt, int h) { af[s & 1][dt][h] = ldstr1(da[piece_loc(g, 2 * s + h) >> 16][dt] + (piece_loc(g, 2 * s + h) & 0xffff)); };
    auto pin_af = [&](int s) {
#pragma unroll
      for (int dt = 0; dt < 4; ++dt) { PIN(af[s & 1][dt][0]); PIN(af[s & 1][dt][1]); }
    };

    u32x4 ph[3][2], pc, psn;
    unsigned pz_off, zo, za[4], zva, ooff, hs[2];
    const int orow = tid >> 3;
    auto prep = [&](auto GI, auto SI, auto JI) {
      constexpr int g = decltype(GI)::v, s = decltype(SI)::v, j = decltype(JI)::v;
      u32x4* hfr = hb[s & 3];
      if constexpr (j == 0) { need<OP_HE + s>(); need<OP_P + 2 * s + 1>(); PIN(hfr[0]); PIN(hfr[1]); PIN(hfr[2]); PIN(hfr[3]); PIN(hck[s & 3][0]); PIN(hck[s & 3][1]); }
      if constexpr (j < 4) { rda1(g, s, j, 0); rda1(g, s, j, 1); }
      if constexpr (j >= 1 && j < 5) {
        constexpr int f = (j - 1) >> 1, o = 2 * ((j - 1) & 1);
        hs[0] = (j == 1 ? tag : hs[0]) + hfr[f][o] + hfr[f][o + 1];
        hs[1] = (j == 1 ? tag : hs[1]) + hfr[2 + f][o] + hfr[2 + f][o + 1];
        PIN(hs[0]); PIN(hs[1]);
      }
      if constexpr (j == 5) { bad0 = mism(hs[0], hck[s & 3][0]); bad1 = mism(hs[1], hck[s & 3][1]); }
      if constexpr (j == 7) { if (__builtin_expect((bad0 | bad1) != 0, 0)) { RETRY(s); reload_h(hfr, hck[s & 3], rw, l16, l4c, hso[s], tag); } }
    };

    unsigned pw0, pw1;
    auto side = [&](auto SI, auto KI) {
      constexpr int s = decltype(SI)::v, k = decltype(KI)::v;
      constexpr int G = 32 * s + k;
#define AT(ss, kk) (G == 32 * (ss) + (kk))
#define TILE(ss, kk, T, h) if constexpr (AT(ss, kk)) issue1(IC<T>{}, IC<h>{})
#define PIECE(ss, kk, P, h) if constexpr (AT(ss, kk)) issue_p1(IC<0>{}, IC<P>{}, IC<h>{})
      if constexpr (AT(0, 7)) issue(IC<3>{});
      TILE(0, 11, 5, 0); TILE(0, 14, 5, 1); TILE(0, 17, 6, 0); TILE(0, 19, 6, 1);
      if constexpr (s == 1 && k % 4 == 0) issue1(IC<7 + k / 8>{}, IC<(k / 4) & 1>{});
      if constexpr (s == 2 && k % 4 == 1) issue1(IC<10 + k / 8>{}, IC<(k >> 2) & 1>{});
      TILE(3, 3, 13, 0); TILE(3, 7, 13, 1); TILE(3, 17, 14, 0); TILE(3, 22, 14, 1);
      TILE(4, 4, 15, 0); TILE(4, 9, 15, 1); TILE(4, 15, 16, 0); TILE(5, 3, 16, 1);
      TILE(5, 11, 17, 0); TILE(5, 15, 17, 1); TILE(6, 4, 18, 0);
      TILE(6, 13, 18, 1); TILE(7, 1, 19, 0); TILE(7, 5, 19, 1); TILE(7, 9, 20, 0); TILE(7, 13, 20, 1);
      if constexpr (AT(1, 13)) {
        const unsigned gw = (unsigned)((bj * 8 + xcd) * 4 + m);
        tco = l < (nh ? 14 : 17) ? ((nh ? gw * 14 : (gw >> 1) * 17) + l) * 128 : OOB; PIN(tco);
      }
      if constexpr (AT(3, 14)) ld32_plain<OP_EP>(tch, rtch, tco);
      PIECE(8, 1, 0, 0); PIECE(8, 4, 0, 1); PIECE(9, 0, 1, 0); PIECE(9, 9, 1, 1);
      PIECE(11, 1, 2, 0); PIECE(11, 5, 2, 1); PIECE(11, 9, 3, 0); PIECE(11, 13, 3, 1);
      if constexpr (s == 12 && k % 4 == 1) load_h1(IC<0>{}, IC<k / 4>{});
      if constexpr (AT(12, 15)) { load_h1(IC<0>{}, IC<4>{}); load_h1(IC<0>{}, IC<5>{}); }
      if constexpr (s == 13 && k % 4 == 2) load_h1(IC<1>{}, IC<k / 4>{});
      if constexpr (AT(13, 15)) { load_h1(IC<1>{}, IC<4>{}); load_h1(IC<1>{}, IC<5>{}); }
      if constexpr (AT(1, 1)) { need<OP_EP>(); PIN(epoch); PIN(code_touch); tag = (unsigned)__builtin_amdgcn_readfirstlane(epoch); PINS(tag); }
      if constexpr (AT(1, 5)) { tag = tag * 0x9E3779B1u + 0x7F4A7C15u; PINS(tag); }
      if constexpr (AT(1, 7)) { gs_off = (unsigned)(WS_STAT + (g1 * 32 + (l & 31)) * 16); PIN(gs_off); }
      if constexpr (AT(1, 10)) { pq = priv + q * 16; pw0 = priv + l * 4; PIN(pq); PIN(pw0); }
      if constexpr (AT(1, 16)) { ea = L0 + L_EB + r * 264 + m * 64 + nh * 32 + q * 8; PIN(ea); }
      if constexpr (AT(1, 19)) { eo = (unsigned)((tid >> 4) * E_K1 + pn * E_PN + (16 * tt + r) * 16); PIN(eo); }
      if constexpr (AT(1, 22)) { ebo = L0 + L_EB + (tid >> 4) * 264 + r * 4; PIN(ebo); }
      if constexpr (AT(2, 18)) { gc_off = (unsigned)(WS_CS + (tt * 4 + (q & 1)) * 512 + (32 * nh + 2 * r) * 8); PIN(gc_off); }
      if constexpr (AT(2, 15)) { pw1 = priv + (l & 31) * 8; PIN(pw1); }
      if constexpr (AT(5, 13)) *(LAS unsigned*)(uintptr_t)(L0 + L_ZOK) = 0u;
      if constexpr (AT(7, 6)) step_setup(0, 0);
      if constexpr (AT(9, 4)) step_setup(0, 1);
      if constexpr (AT(10, 2)) step_setup(0, 2);
      if constexpr (AT(6, 0)) { doff = (unsigned)(lr * 896 + ct * 128 + ((lc ^ (((lr >> 1) & 3) << 1)) << 4)); PIN(doff); }
      if constexpr (AT(6, 2)) { l4c = l * 4 + 4096; PIN(l4c); }
      if constexpr (AT(9, 2)) { const int row = 4 * q + (r >> 2); da[0][0] = L0 + w * 2048 + row * 128 + (((row >> 1) & 3) << 5) + (r & 3) * 8; PIN(da[0][0]); }
      if constexpr (AT(9, 5) || AT(9, 6) || AT(9, 8)) { constexpr int dt = k == 8 ? 3 : k - 4; da[0][dt] = da[0][0] ^ (dt << 5); PIN(da[0][dt]); }
      if constexpr (s == 9 && k >= 10 && k < 14) { da[1][k - 10] = da[0][k - 10] + 65536; da[2][k - 10] = da[0][k - 10] + 131072; PIN(da[1][k - 10]); PIN(da[2][k - 10]); }
      if constexpr (AT(12, 0)) {
        const int gg = 2 * rt + (w >> 2);
        const int n2 = ((gg >> 2) & 1) + 8 * (gg >> 3) + 2 * (gg & 3);
        ooff = (unsigned)((64 * (orow & 31) + n2) * 896 + ct * 128 + lc * 16); PIN(ooff);
      }
      if constexpr (AT(2, 22)) ld128_sc1<OP_GS, 0>(gran, rw, gs_off, 0u);
      if constexpr (AT(3, 10)) { waitv<2, OP_GS>(); PIN(gran); bad0 = mism(gran.y, tag); bad1 = mism(gran.w, tag); }
      if constexpr (AT(3, 13)) {
        if (__builtin_expect((bad0 | bad1) != 0, 0)) { RETRY(12); poll_granules(gran, rw, gs_off, tag); }
        *(LAS unsigned*)(uintptr_t)(pw0) = gran.z;
      }
      if constexpr (AT(3, 15)) {
        auto sw = __builtin_amdgcn_permlane16_swap(gran.x, gran.x, false, false);
        const unsigned a0 = sw[0], a1 = sw[1];
        mrow[0] = opq(asf(a0)); mrow[1] = opq(asf(a1));
      }
      if constexpr (AT(3, 17)) { rsr[0] = opq(asf(gran.z) * asf(gran.z)); }
      if constexpr (AT(3, 21)) {
        auto sw = __builtin_amdgcn_permlane16_swap(asu(rsr[0]), asu(rsr[0]), false, false);
        const unsigned a0 = sw[0], a1 = sw[1];
        r2row[0] = opq(asf(a0)); r2row[1] = opq(asf(a1));
      }
      if constexpr (AT(3, 16) || AT(3, 18)) rD[(k - 16) >> 1] = lds128f(pq + ((k - 16) >> 1) * 64);
      if constexpr (AT(3, 19) || AT(3, 20)) p1s[k - 19] = lds128(aux16 + (L_P1 - L_AUX) + 2048 + (k - 19) * 1024);
      if constexpr (AT(4, 0) || AT(4, 2)) p1c[k >> 1] = lds128(aux16 + (L_P1 - L_AUX) + (k >> 1) * 1024);
      if constexpr (s == 4 && k >= 1 && k < 5) {
        constexpr int e = 2 * (k - 1);
        dv[e] = opq(rD[e >> 2][e & 3] * accD[e >> 2][e & 3]);
        dv[e + 1] = opq(rD[e >> 2][(e & 3) + 1] * accD[e >> 2][(e & 3) + 1]);
      }
      if constexpr (AT(4, 5) || AT(4, 6)) {
        constexpr int e = 4 * (k - 5);
        dpk[e >> 1] = pk2(dv[e], dv[e + 1]); dpk[(e >> 1) + 1] = pk2(dv[e + 2], dv[e + 3]);
        PIN(dpk[e >> 1]); PIN(dpk[(e >> 1) + 1]);
      }
      if constexpr (AT(4, 7)) {
        df = u32x4{dpk[0], dpk[1], dpk[2], dpk[3]};
        mma0(ts[0], df, p1s[0]); mma0(tc[0], df, p1c[0]); mma0(ts[1], df, p1s[1]); mma0(tc[1], df, p1c[1]);
      }
      if constexpr (s == 4 && k >= 10 && k < 14) {
        constexpr int a = (k - 10) >> 1, e = 2 * (k & 1);
        ev[0] = opq(tc[a][e] + ts[a][e + 1]);
        ev[1] = opq(tc[a][e + 1] - ts[a][e]);
        dpk[k & 1] = pk2(ev[0], ev[1]); PIN(dpk[k & 1]);
      }
      if constexpr (AT(4, 11) || AT(4, 13)) *(LAS u32x2*)(uintptr_t)(ea + ((k - 10) >> 1) * (16 * 264)) = u32x2{dpk[0], dpk[1]};
      if constexpr (AT(5, 0)) {
        waitv<6, 13>();
        asm volatile("s_waitcnt lgkmcnt(3)\n\ts_barrier" ::: "memory");
#pragma unroll
        for (int i = 0; i < 4; ++i) ebv[i] = *(LAS const unsigned*)(uintptr_t)(ebo + i * 64);
      }
      if constexpr (AT(5, 5)) ld128_sc1<OP_GC1, 0>(gran, rw, gc_off, 0u);
      if constexpr (AT(5, 4)) { hsum = (tag + ebv.x + ebv.y) + ebv.z + ebv.w; PIN(hsum); tch2 = tid * 4; PIN(tch2); }
      if constexpr (AT(5, 7)) { st128_sc1<OP_E, 0>(rw, eo, WS_E, ebv); st32_sc1<OP_E>(rw, tch2, ecs, hsum); }
      if constexpr (AT(5, 2)) { tco = (tid >= 480 && bj >= 24) ? (unsigned)(WS_HINT + ((bj - 24) * 8 + (xcd ^ 4)) * 128 + (tid - 480) * 4) : OOB; dpk[2] = tag; PIN(tco); PIN(dpk[2]); }
      if constexpr (AT(5, 9)) st32_sc1<OP_E>(rw, tco, 0u, dpk[2]);
      if constexpr (AT(6, 6)) { waitv<6, OP_GC1>(); need<16>(); PIN(gran); bad0 = mism(gran.y, tag); bad1 = mism(gran.w, tag); }
      if constexpr (AT(6, 7)) asm volatile("s_barrier" ::: "memory");
      if constexpr (AT(12, 11)) { waitv<7, OP_GC2>(); PIN(tch); PIN(gran); bad0 = mism(gran.y, tag); bad1 = mism(gran.w, tag); }
      if constexpr (AT(6, 9) || AT(12, 14)) {
        if (__builtin_expect((bad0 | bad1) != 0, 0)) { RETRY(s == 12 ? 14 : 13); poll_granules(gran, rw, gc_off + (s == 12 ? 1024 : 0), tag); }
        *(LAS unsigned*)(uintptr_t)(pw1) = gran.x; *(LAS unsigned*)(uintptr_t)(pw1 + 4) = gran.z;
      }
      if constexpr (AT(6, 12) || AT(6, 14) || AT(7, 1) || AT(7, 3)) { constexpr int i = s == 6 ? k - 12 : k; cs[i] = lds128f(pq + i * 64); }
      if constexpr (s == 13 && k >= 3 && k < 11 && (k & 1)) cs[(k - 3) >> 1] = lds128f(pq + ((k - 3) >> 1) * 64);
      if constexpr ((s == 7 && k >= 8) || (s == 8 && k < 10)) {
        constexpr int o = 4 * (16 * (s - 7) + k - 8);
        hop(IC<o>{}); hop(IC<o + 1>{}); hop(IC<o + 2>{}); hop(IC<o + 3>{});
      }
      if constexpr (AT(10, 8)) ld128_sc1<OP_GC2, 1024>(gran, rw, gc_off, 0u);
      if constexpr (AT(8, 7)) h_store(IC<0>{}, IC<0>{});
      if constexpr (AT(8, 10)) { h_store(IC<0>{}, IC<1>{}); hsum = (tag + hpk[0][0] + hpk[0][1]) + hpk[0][2] + hpk[0][3]; PIN(hsum); }
      if constexpr (AT(8, 11)) { hsum = (hsum + hpk[1][0] + hpk[1][1]) + hpk[1][2] + hpk[1][3]; PIN(hsum); }
      if constexpr (AT(8, 12)) h_check_store(IC<0>{});
      if constexpr (AT(8, 14)) { waitv<11, 17>(); asm volatile("s_barrier" ::: "memory"); }
      if constexpr (AT(10, 7)) { waitv<7, 20>(); asm volatile("s_barrier" ::: "memory"); }
      if constexpr (AT(13, 7)) asm volatile("s_barrier" ::: "memory");
    };

    sfor<0, 14>([&](auto SI) {
      constexpr int s = decltype(SI)::v;
      constexpr int T = tile_of_w(s), sl = T & 7;
      constexpr int ps = s / 7, j = s % 7, pps = (s - 1) / 7, pj = (s + 6) % 7;
      constexpr int x0 = s < 4 ? kSegX0[s < 4 ? s : 0] : 0, nx = s < 4 ? kSegNX[s < 4 ? s : 0] : 0;
      constexpr int waitB[5] = {5, 4, 4, 5, 6}, lastT[5] = {1, 3, 7, 10, 12};
      if constexpr (s <= 4) { waitv<waitB[s <= 4 ? s : 0], lastT[s <= 4 ? s : 0]>(); BARRIER(); }
      SB();
      sfor<0, 16 + 4 * nx>([&](auto KI) {
        constexpr int k = decltype(KI)::v, i = (k & 7) >> 1, h = k & 1;
        if constexpr (k < 8) {
          if constexpr (s > 0) mma(acc[pps][i][h], cat(fb_[i][0], fb_[i][1]), xf[2 * pj + 1][h]);
        } else if constexpr (k < 16) {
          if constexpr (j == 0) mma0(acc[ps][i][h], cat(fa_[i][0], fa_[i][1]), xf[0][h]);
          else mma(acc[ps][i][h], cat(fa_[i][0], fa_[i][1]), xf[2 * j][h]);
        } else {
          if constexpr (s == 0 && k < 18) mma0(accD[h], xf[0][h], ff[0]);
          else mma(accD[h], xf[2 * x0 + i][h], ff[i]);
        }
        SB();
        constexpr int xr = s == 0 ? k : (k < 2 ? k : ((k & 1) ? -1 : 1 + k / 2));
        if constexpr (xr >= 0 && xr < 4 * nx) {
          constexpr int xt = x0 + (xr >> 2), kk = (xr >> 1) & 1, xs = tile_of_x(xt) & 7;
          xf[2 * xt + kk][xr & 1] = lds128(xa[xs >> 2][kk] + (xs & 3) * 16384 + (xr & 1) * 2048);
        }
        if constexpr (k < 16) {
          const unsigned a = wa[sl >> 2][i & 1] + (sl & 3) * 16384 + (i >> 1) * 8192 + (k >> 3) * 4096 + h * 512;
          if constexpr (k < 8) fa_[i][h] = ldstr1(a); else fb_[i][h] = ldstr1(a);
        }
        if constexpr (k >= 9 && (k & 1) && (k - 9) / 2 < 2 * nx) ff[(k - 9) / 2] = lds128(fad + (2 * x0 + (k - 9) / 2) * 1024);
        SB();
        side(SI, KI);
        SB();
      });
      if constexpr (s == 0) STAMP(3);
      if constexpr (s == 3) STAMP(4);
      if constexpr (s == 5) STAMP(5);
      if constexpr (s == 7) STAMP(6);
      if constexpr (s == 9) STAMP(7);
      if constexpr (s == 11) STAMP(8);
    });
    waitv<6, OP_HE + 0>();
    need<OP_P + 3>();
    sfor<0, 8>([&](auto KI) {
      constexpr int k = decltype(KI)::v, i = ((k >> 1) & 1) * 2 + (k >> 2), h = k & 1;
      mma(acc[1][i][h], cat(fb_[i][0], fb_[i][1]), xf[13][h]);
      SB();
      if constexpr (k < 7) prep(IC<0>{}, IC<0>{}, KI);
      SB();
    });
    STAMP(9);
    h_part(IC<1>{}, 0, 0); h_part(IC<1>{}, 1, 0);
    h_part(IC<1>{}, 0, 1); h_store(IC<1>{}, IC<0>{});
    h_part(IC<1>{}, 1, 1); h_store(IC<1>{}, IC<1>{});
    hsum = csum8(tag, u32x4{hpk[0][0], hpk[0][1], hpk[0][2], hpk[0][3]}, u32x4{hpk[1][0], hpk[1][1], hpk[1][2], hpk[1][3]});
    h_check_store(IC<1>{});
    SB();
    prep(IC<0>{}, IC<0>{}, IC<7>{});
    STAMP(10);

    f32x4 oacc[4][4];
    auto gstep = [&](auto SI, auto&& fn) {
      constexpr int s = decltype(SI)::v;
      sfor<0, 16>([&](auto KI) {
        constexpr int k = decltype(KI)::v, dt = k >> 2, rf = k & 3;
        if constexpr (s == 0) mma0(oacc[dt][rf], cat(af[0][dt][0], af[0][dt][1]), hb[0][rf]);
        else mma(oacc[dt][rf], cat(af[s & 1][dt][0], af[s & 1][dt][1]), hb[s & 3][rf]);
        SB();
        fn(KI);
        SB();
      });
    };
    auto gops = [&](auto B0, auto N) { sfor<decltype(B0)::v, (decltype(B0)::v + decltype(N)::v < 272 ? decltype(B0)::v + decltype(N)::v : 272)>([&](auto O) { gop(O); }); };
    auto step_ld = [&](auto GI, auto SI, auto NI) {
      constexpr int s = decltype(SI)::v, n = decltype(NI)::v;
      if constexpr (n == 0 && s > 2) step_setup(decltype(GI)::v, s);
      if constexpr (n < 4) issue_p1(GI, IC<2 * s + n / 2>{}, IC<n & 1>{});
      else load_h1(SI, IC<n - 4>{});
      if constexpr (n == 7) { load_h1(SI, IC<4>{}); load_h1(SI, IC<5>{}); }
    };
    f32x4 fin[2];
    u32x4 sel[4], xd, xres;
    unsigned pw[4], ra[8], bp;
    auto fin_setup = [&](auto KI) {
      constexpr int k = decltype(KI)::v;
      if constexpr (k == 0) { need<OP_XRES>(); PIN(xres); bp = (unsigned)((8 * (r & 7) + 4 * (r >> 3) + q) * 4); PIN(bp); }
      if constexpr (k >= 1 && k < 5) {
        const unsigned t_ = xres[k - 1]; xd[k - 1] = (unsigned)__builtin_amdgcn_ds_bpermute((int)bp, (int)t_);
        const unsigned o_ = r == 4 * (k - 1) + q ? 0x3f803f80u : 0u; sel[k - 1] = u32x4{o_, o_, o_, o_}; PIN(sel[k - 1]);
      }
      if constexpr (k == 5) { pw[0] = lw2 + r * 128 + ((q ^ r ^ (w & 1) ^ ((w & 2) << 2)) << 3); PIN(pw[0]); }
      if constexpr (k == 6) { pw[1] = pw[0] ^ 32; pw[2] = pw[0] ^ 64; pw[3] = pw[0] ^ 96; PIN(pw[1]); PIN(pw[2]); PIN(pw[3]); }
      if constexpr (k == 7) { bp = (unsigned)((lr + 8 * (lc >> 2) + 16 * (lc & 3)) * 4); PIN(bp); }
      if constexpr (k == 8) {
        const int qq = r >> 2, row = 8 * (w & 1) + q;
        ra[0] = L0 + (w >> 1) * 16384 + qq * 2048 + row * 128 + (((2 * (r & 3)) ^ row ^ (qq & 1) ^ ((qq & 2) << 2)) << 3); PIN(ra[0]);
      }
      if constexpr (k == 9) {
#pragma unroll
        for (int i = 1; i < 8; ++i) { ra[i] = ra[0] ^ (((i >> 1) & 1) << 5) ^ ((i >> 2) << 6) ^ ((i & 1) << 3); PIN(ra[i]); }
      }
      if constexpr (k == 10) {
#pragma unroll
        for (int i = 0; i < 4; ++i) { const unsigned t_ = xd[i]; fin[i >> 1][2 * (i & 1)] = opq(bflo(t_)); fin[i >> 1][2 * (i & 1) + 1] = opq(bfhi(t_)); }
      }
    };
    auto put = [&](int dt, int rf) {
      const f32x4 o = oacc[dt][rf];
      *(LAS u32x2*)(uintptr_t)(pw[dt] + rf * 16384) = u32x2{pk2(o[0], o[1]), pk2(o[2], o[3])};
    };
    waitv<3, OP_HE + 1>();
#pragma unroll
    for (int i = 0; i < 4; ++i) PIN(hb[1][i]);
    PIN(hck[1][0]); PIN(hck[1][1]);
    if (nh) {
      ONLY(Y);
      using G1 = IC<1>;
      gstep(IC<0>{}, [&](auto KI) {
        constexpr int k = decltype(KI)::v;
        if constexpr (k == 0 || k == 3 || k == 6) step_ld(G1{}, IC<2>{}, IC<k / 3>{});
        if constexpr (k >= 8) prep(G1{}, IC<1>{}, IC<k - 8>{});
      });
      gstep(IC<1>{}, [&](auto KI) {
        constexpr int k = decltype(KI)::v;
        if constexpr (k >= 11) step_ld(G1{}, IC<2>{}, IC<k - 8>{});
      });
      asm volatile("s_barrier" ::: "memory");
      STAMP(11);
      sfor<0, 8>([&](auto NI) { step_ld(G1{}, IC<3>{}, NI); });
      ld128<OP_XRES, 0>(xres, rx, ooff, 0u);
      waitv<11, OP_HE + 2>();
      sfor<0, 8>([&](auto JI) { prep(G1{}, IC<2>{}, JI); });
      STAMP(12);
      sfor<2, 9>([&](auto SI) {
        constexpr int s = decltype(SI)::v;
        gstep(SI, [&](auto KI) {
          constexpr int k = decltype(KI)::v;
          if constexpr (s + 2 < 9 && k < 8) step_ld(G1{}, IC<s + 2>{}, KI);
          if constexpr (s < 8 && k == 8) waitv<(s == 2 ? 10 : (s < 7 ? 10 : 0)), OP_HE + (s < 8 ? s + 1 : s)>();
          if constexpr (s < 8 && k >= 8) prep(G1{}, IC<(s < 8 ? s + 1 : s)>{}, IC<k - 8>{});
          if constexpr (s == 3 && k < 8) fin_setup(KI);
          if constexpr (s == 4 && k < 6 && !(k & 1)) fin_setup(IC<8 + k / 2>{});
          if constexpr (s == 8 && k < 3) asm volatile("s_nop 2");
          if constexpr (s == 8 && k >= 3) put((k - 3) >> 2, (k - 3) & 3);
        });
        if constexpr (s == 3) __builtin_amdgcn_s_setprio(1);
        if constexpr (s == 3) STAMP(13);
        if constexpr (s == 5) STAMP(15);
        if constexpr (s == 7) STAMP(17);
      });
      STAMP(19);
      put(3, 1); SB(); put(3, 2); SB(); put(3, 3);
      ONLY(END);
    } else {
      ONLY(O);
      using G0 = IC<0>;
      g_cq = opq((float)(56 * m + 8 * q));
      g_mk = q == 3 ? 0u : ~0u;
      g_ny = (tid < 16 && (tid & 1)) ? 0x8000u : 0u;
      auto phop = [&](auto NI) {
        constexpr int n = decltype(NI)::v, i = n >> 2, v = n & 3;
        const unsigned c_ = pc[i], s_ = psn[i];
        unsigned u0 = (v & 1) ? __builtin_amdgcn_perm(c_, s_, (v & 2) ? 0x07060302u : 0x05040100u) : __builtin_amdgcn_perm(s_, c_, (v & 2) ? 0x07060302u : 0x05040100u);
        if constexpr (v == 0) { u0 ^= 0x80000000u; PIN(u0); ph[0][0][i] = u0; }
        if constexpr (v == 1) { PIN(u0); ph[0][1][i] = u0; }
        if constexpr (v == 2) { unsigned u1 = u0 ^ 0x8000u; u0 ^= 0x80000000u; PIN(u0); PIN(u1); ph[1][0][i] = u0; ph[2][0][i] = u1; }
        if constexpr (v == 3) { unsigned u1 = u0 ^ 0x80008000u; PIN(u0); PIN(u1); ph[1][1][i] = u0; ph[2][1][i] = u1; }
      };
      gstep(IC<0>{}, [&](auto KI) {
        constexpr int k = decltype(KI)::v;
        if constexpr (k == 0 || k == 3 || k == 6) step_ld(G0{}, IC<2>{}, IC<k / 3>{});
        if constexpr (k >= 8) prep(G0{}, IC<1>{}, IC<k - 8>{});
        if constexpr (k < 8) gops(IC<4 * k>{}, IC<4>{}); else gops(IC<32 + 2 * (k - 8)>{}, IC<2>{});
      });
      gstep(IC<1>{}, [&](auto KI) {
        constexpr int k = decltype(KI)::v;
        gops(IC<48 + 4 * k>{}, IC<4>{});
        if constexpr (k >= 11) step_ld(G0{}, IC<2>{}, IC<k - 8>{});
      });
      asm volatile("s_barrier" ::: "memory");
      STAMP(11);
      sfor<0, 8>([&](auto NI) { step_ld(G0{}, IC<3>{}, NI); SB(); gops(IC<112 + 3 * decltype(NI)::v>{}, IC<3>{}); SB(); });
      pz_off = w ? OOB : (unsigned)(WS_FLAGZ + (l & 31) * 128);
      ld128<OP_XRES, 0>(xres, rx, ooff, 0u);
      ld32_sc1<OP_POLL>(pollv, rw, pz_off);
      waitv<12, OP_HE + 2>();
      sfor<0, 8>([&](auto JI) { prep(G0{}, IC<2>{}, JI); });
      STAMP(12);
      gstep(IC<2>{}, [&](auto KI) {
        constexpr int k = decltype(KI)::v;
        if constexpr (k < 8) step_ld(G0{}, IC<4>{}, KI);
        if constexpr (k == 1) { zo = (unsigned)(((l >> 1) & 7) ^ ((q & 1) | ((q & 2) << 1))); PIN(zo); }
        if constexpr (k == 2) { zo = q * 256 + (zo << 5) + (l & 1) * 16; PIN(zo); }
        if constexpr (k == 3) { const int P = 2 * q + (r >> 3); za[0] = (unsigned)(((r >> 1) & 2) ^ ((P & 1) | ((P & 2) << 1))); za[1] = P * 256 + (r & 3) * 8; PIN(za[0]); PIN(za[1]); }
        if constexpr (k == 4) { za[0] = lw2 + za[1] + (za[0] << 5); zva = lw2 + l16; PIN(za[0]); PIN(zva); }
        if constexpr (k == 5) { za[1] = za[0] ^ 128; za[2] = za[0] ^ 32; za[3] = za[0] ^ 160; PIN(za[1]); PIN(za[2]); PIN(za[3]); }
        if constexpr (k == 14) pc = lds128(aux16 + (L_P1 - L_AUX));
        if constexpr (k == 15) psn = lds128(aux16 + (L_P1 - L_AUX) + 2048);
        if constexpr (k == 8) waitv<10, OP_POLL>();
        if constexpr (k >= 8) prep(G0{}, IC<3>{}, IC<k - 8>{});
      });
      STAMP(13);
      PIN(pollv);
      unsigned zso = (unsigned)(WS_Z + 2 * rt * Z_G + 2 * m * 4096);
      if (w == 0) {
        if (mism(pollv, tag) != 0) { RETRY(9); poll_flags(rw, pz_off, tag); }
        *(volatile LAS unsigned*)(uintptr_t)(L0 + L_ZOK) = 1u;
      } else {
#pragma nounroll
        for (unsigned spins = 0; spins < 64 * SPIN_CAP; ++spins) {
          if (__builtin_amdgcn_readfirstlane(*(volatile LAS unsigned*)(uintptr_t)(L0 + L_ZOK))) break;
          __builtin_amdgcn_s_sleep(1);
        }
      }
      asm volatile("" ::: "memory");
      auto issue_z1 = [&](auto HV, auto NI, auto IDI) {
        constexpr int hv = decltype(HV)::v, zi = decltype(NI)::v >> 1, zh = decltype(NI)::v & 1;
        dma_add_sc1<decltype(IDI)::v, (4 * hv + zi) * S_ + zh * 1024, hv * 4096 + (zi >> 1) * Z_G + (zi & 1) * 2048 + zh * 1024>(rw, zo, lw2, zso);
      };
      gstep(IC<3>{}, [&](auto KI) {
        constexpr int k = decltype(KI)::v;
        if constexpr (k < 8) { issue_z1(IC<0>{}, KI, IC<OP_Z>{}); phop(IC<2 * k>{}); phop(IC<2 * k + 1>{}); }
        if constexpr (k == 8) waitv<8, OP_HE + 4>();
        if constexpr (k >= 8) prep(G0{}, IC<4>{}, IC<k - 8>{});
      });
      pin_af(4);
      gstep(IC<4>{}, [&](auto KI) {
        constexpr int k = decltype(KI)::v;
        if constexpr (!(k & 1)) issue_z1(IC<1>{}, IC<k / 2>{}, IC<OP_Z + 1>{});
        gops(IC<136 + 5 * k>{}, IC<5>{});
      });
      STAMP(15);
      sfor<0, 2>([&](auto HV) {
        constexpr int hv = decltype(HV)::v;
        waitv<hv ? 0 : 8, OP_Z + hv>();
        if constexpr (hv == 1) {
#pragma unroll
          for (int i = 0; i < 4; ++i) { za[i] += 65536; PIN(za[i]); }
          zva += 65536; PIN(zva);
        }
        u32x4 z0[2], z1[2];
        f32x4 t[6];
        unsigned yy[2][2][4];
        auto rdz = [&](int u) {
          const unsigned a_ = za[2 * (u & 1)] + (u >> 1) * 2 * S_, b_ = za[2 * (u & 1) + 1] + (u >> 1) * 2 * S_;
          z0[u & 1] = ldstr(a_, a_ + S_); z1[u & 1] = ldstr(b_, b_ + S_);
        };
        auto amma = [&](int u, int i) { if (i < 2) mma0(t[i], z0[u & 1], ph[0][i]); else mmac(t[i], z1[u & 1], ph[i >> 1][i & 1], t[i & 1]); };
        auto ypk = [&](int u, int k) { yy[u & 1][k >> 2][k & 3] = pk2(t[2 + 2 * (k >> 2)][k & 3], t[3 + 2 * (k >> 2)][k & 3]); PIN(yy[u & 1][k >> 2][k & 3]); };
#pragma nounroll
        for (unsigned spins = 0;; ++spins) {
          u32x4 zv[4];
          unsigned zs;
          rdz(0);
#pragma unroll
          for (int i = 0; i < 4; ++i) zv[i] = lds128(zva + (i >> 1) * S_ + (i & 1) * 1024);
          SB();
#pragma unroll
          for (int i = 0; i < 6; ++i) {
            amma(0, i);
            SB();
            if (i >= 1 && i < 5) { zs = ((i == 1 ? 0u : zs) + zv[i - 1].x + zv[i - 1].y) + zv[i - 1].z + zv[i - 1].w; PIN(zs); zv[i - 1] = lds128(zva + (2 + ((i - 1) >> 1)) * S_ + ((i - 1) & 1) * 1024); }
            SB();
          }
          rdz(1);
#pragma unroll
          for (int i = 0; i < 4; ++i) { zs = (zs + zv[i].x + zv[i].y) + zv[i].z + zv[i].w; PIN(zs); }
          keep(t[0], t[1]);
          SB();
#pragma unroll
          for (int k = 0; k < 8; ++k) {
            ypk(0, k);
            if (k == 1) { zs += (unsigned)__builtin_amdgcn_update_dpp(0, (int)zs, 0x128, 0xf, 0xf, true); PIN(zs); }
            if (k == 3) { zs += (unsigned)__builtin_amdgcn_update_dpp(0, (int)zs, 0x124, 0xf, 0xf, true); PIN(zs); }
            if (k == 5) { zs += (unsigned)__builtin_amdgcn_update_dpp(0, (int)zs, 0x122, 0xf, 0xf, true); PIN(zs); }
            if (k == 7) { zs += (unsigned)__builtin_amdgcn_update_dpp(0, (int)zs, 0x121, 0xf, 0xf, true); PIN(zs); }
            SB();
          }
          bad0 = mism(zs, tag * 16u);
#pragma unroll
          for (int i = 0; i < 6; ++i) amma(1, i);
          if (__builtin_expect(bad0 == 0 || spins >= SPIN_CAP, 1)) break;
          RETRY(10 + hv);
          __builtin_amdgcn_s_sleep(10);
          asm volatile("s_waitcnt lgkmcnt(0)" ::: "memory");
          sfor<0, 8>([&](auto NI) { issue_z1(HV, NI, IC<OP_OPT>{}); });
          asm volatile("s_waitcnt vmcnt(0)" ::: "memory");
        }
        sfor<0, 4>([&](auto UI) {
          constexpr int u = decltype(UI)::v;
          const u32x4 y0 = {yy[u & 1][0][0], yy[u & 1][0][1], yy[u & 1][0][2], yy[u & 1][0][3]};
          const u32x4 y1 = {yy[u & 1][1][0], yy[u & 1][1][1], yy[u & 1][1][2], yy[u & 1][1][3]};
          if (u == 1 || u == 2) {
#pragma unroll
            for (int i = 0; i < 6; ++i) amma(u + 1, i);
          } else if (u == 3) asm volatile("s_nop 1");
          sfor<0, 8>([&](auto KI) {
            constexpr int k = decltype(KI)::v;
            mma(oacc[k >> 1][2 * (u >> 1) + (k & 1)], gf[hv][u & 1][k >> 1], (k & 1) ? y1 : y0);
            SB();
            if constexpr (u < 2 && k == 0) rdz(u + 2);
            if constexpr (u == 2 && k == 0) asm volatile("s_nop 1");
            if constexpr (u < 3 && k == 3) keep(t[0], t[1]);
            if constexpr (u < 3 && k >= 4) { ypk(u + 1, 2 * (k - 4)); ypk(u + 1, 2 * (k - 4) + 1); }
            if constexpr (hv == 0 && (u == 3 || k < 4)) gops(IC<216 + 3 * (4 * u + k)>{}, IC<3>{});
            if constexpr (hv == 1 && u < 3 && k < 4 && 4 * u + k < 11) fin_setup(IC<4 * u + k>{});
            if constexpr (hv == 1 && u == 3) put(k >> 1, k & 1);
            SB();
          });
        });
        if constexpr (hv == 0) STAMP(17);
      });
      STAMP(19);
#pragma unroll
      for (int k = 0; k < 8; ++k) { put(k >> 1, 2 + (k & 1)); SB(); }
      ONLY(END);
    }
    BARRIER();
    STAMP(20);
    {
      u32x4 pa[8];
#pragma unroll
      for (int i = 0; i < 8; ++i) pa[i] = ldstr(ra[i] + ((i >> 1) & 1) * 512, ra[i] + ((i >> 1) & 1) * 512 + 8192);
#pragma unroll
      for (int i = 0; i < 8; ++i) fin[i & 1] = mfma16(pa[i], sel[i >> 1], fin[i & 1]);
      const u32x4 o = {pk2(fin[0][0], fin[0][1]), pk2(fin[0][2], fin[0][3]), pk2(fin[1][0], fin[1][1]), pk2(fin[1][2], fin[1][3])};
      u32x4 oc;
#pragma unroll
      for (int i = 0; i < 4; ++i) { const unsigned t_ = o[i]; oc[i] = (unsigned)__builtin_amdgcn_ds_bpermute((int)bp, (int)t_); }
      asm volatile("global_store_dwordx4 %0, %1, %2 sc1" ::"v"(ooff), "v"(oc), "s"(out) : "memory");
    }
    if (b == 32 && tid == 0) *(unsigned*)(ws + WS_EPOCH) = tag;
    STAMP(21);
  } else {
    const int k1 = 8 * bi + xcd;
    const int lr = l >> 3, lc = l & 7;
    const int gst = 2 * (8 * (xcd & 3) + 4 * (xcd >> 2) + bi) + (w >> 2), n1s = 8 * (w & 3) + lr;
    const int cid = 4 * (xcd & 3) + bi;
    const int ctt = 7 * (xcd >> 2) + ((cid < 14 ? cid : 0) >> 1), cmat = cid & 1;
    const rsrc_t rx = mk_rsrc(x, 2048 * 896), rc = mk_rsrc(cmat ? wu : wg, 448 * 3584), rw = mk_rsrc(ws, WS_BYTES);
    const rsrc_t rfr = mk_rsrc(wr, 921600), rfi = mk_rsrc(wi, 921600);
    LOAD_EPOCH();
    u32x4 xr[7];
    {
      const int n2 = ((gst >> 2) & 1) + 8 * (gst >> 3) + 2 * (gst & 3);
      const unsigned xo = (unsigned)((64 * n1s + n2) * 896 + lc * 16);
      sfor<0, 7>([&](auto J) { constexpr int j = decltype(J)::v; ld128<0, j * 128>(xr[j], rx, xo, 0u); });
    }
    constexpr unsigned L_RED = 28672, L_WT = 32768, L_FILT = 32768;
    const unsigned wo = (unsigned)((56 * w + lr) * 3584 + ctt * 256 + ((lc ^ (((lr >> 1) & 3) << 1)) << 4));
    const unsigned lwt = L0 + L_WT + w * 7168;
    const int c31 = l & 31, h = l >> 5;
    {
      const int s = w, par = s >> 2;
      float vc[4], vs[4];
#pragma unroll
      for (int mm = 0; mm < 4; ++mm) {
        int n2 = par + 8 * (2 * (s & 3) + h) + 2 * mm;
        float rev = (float)((n2 * (k1 + 32 * c31)) & 2047) * (1.0f / 2048.0f);
        vc[mm] = __builtin_amdgcn_cosf(rev); vs[mm] = __builtin_amdgcn_sinf(rev);
      }
      *(LAS u32x4*)(uintptr_t)(L0 + (2 * s) * 1024 + l * 16) = u32x4{pk2(vc[0], vs[0]), pk2(vc[1], vs[1]), pk2(vc[2], vs[2]), pk2(vc[3], vs[3])};
      *(LAS u32x4*)(uintptr_t)(L0 + (2 * s + 1) * 1024 + l * 16) = u32x4{pk2(-vs[0], vc[0]), pk2(-vs[1], vc[1]), pk2(-vs[2], vc[2]), pk2(-vs[3], vc[3])};
    }
    {
      const int par = (w >> 1) & 1, s2 = w & 1;
      float v[8];
#pragma unroll
      for (int e = 0; e < 8; ++e) {
        int k2 = 16 * s2 + 8 * (e >> 2) + 4 * h + (e & 3);
        v[e] = __builtin_amdgcn_cosf((float)(((par + 2 * c31) * (k1 + 32 * k2)) & 2047) * (1.0f / 2048.0f) - (w >= 4 ? 0.25f : 0.0f)) * (2.0f / (2048.0f * 448.0f));
      }
      const u32x4 pk = {pk2(v[0], v[1]), pk2(v[2], v[3]), pk2(v[4], v[5]), pk2(v[6], v[7])};
      const unsigned a = L0 + 16384 + (w & 3) * 3072 + (w >> 2) * 1024 + l * 16;
      *(LAS u32x4*)(uintptr_t)(a) = pk;
      if (w >= 4) *(LAS u32x4*)(uintptr_t)(a + 1024) = pk ^ 0x80008000u;
    }
    SB();
    waitv<0, 0>();
    sfor<0, 3>([&](auto J) { constexpr int j = decltype(J)::v; dma_add<0, j * 1024, 0>(rc, wo, lwt, (unsigned)(j * (8 * 3584))); });
    GET_TAG();
    {
      float s1 = 0.f, s2 = 0.f;
#pragma unroll
      for (int j = 0; j < 7; ++j) { PIN(xr[j]); s1 = sum8(xr[j], s1); s2 = sq8(xr[j], s2); }
      s1 += dpp<0xB1>(s1); s2 += dpp<0xB1>(s2);
      s1 += dpp<0x4E>(s1); s2 += dpp<0x4E>(s2);
      s1 += dpp<0x141>(s1); s2 += dpp<0x141>(s2);
      const float mean = s1 * (1.0f / 448.0f);
      float var = __builtin_fmaf(-mean, mean, s2 * (1.0f / 448.0f));
      var = var > 0.f ? var : 0.f;
      const float rstd = __builtin_amdgcn_rsqf(var + 1e-5f);
      st128_sc1<0, 0>(rw, lc ? OOB : (unsigned)(WS_STAT + (gst * 32 + n1s) * 16), 0u, u32x4{asu(mean), tag, asu(rstd), tag});
    }
    STAMP(1);
    sfor<3, 7>([&](auto J) { constexpr int j = decltype(J)::v; dma_add<0, j * 1024, 0>(rc, wo, lwt, (unsigned)(j * (8 * 3584))); });
    const unsigned fo = (((unsigned)((k1 + 32 * (8 * w + h)) * 450)) & ~15u) + c31 * 16, lfb = L0 + L_FILT + w * 4096;
    const u32x4 ones = {0x3f803f80u, 0x3f803f80u, 0x3f803f80u, 0x3f803f80u};
    const unsigned csa = L0 + L_WT + (4 * q + (r >> 2)) * 128 + ((((4 * q + (r >> 2)) >> 1) & 3) << 5) + (r & 3) * 8;
    const unsigned cso = cid < 14 ? (unsigned)(WS_CS + (ctt * 4 + cmat) * 512) : OOB;
    auto colsum = [&](unsigned base, int ps) {
      const unsigned a = (csa ^ ((w & 3) << 5)) + (w >> 2) * 28672 + base;
      f32x4 sm = {0, 0, 0, 0};
#pragma unroll
      for (int j = 0; j < 7; ++j) sm = mfma16(ldstr(a + j * 4096, a + j * 4096 + 2048), ones, sm);
      if (r == 0) *(LAS f32x4*)(uintptr_t)(L0 + L_RED + ps * 512 + w * 64 + q * 16) = sm;
      BARRIER();
      if (w == 0) {
        const float t = *(LAS const float*)(uintptr_t)(L0 + L_RED + ps * 512 + l * 4) + *(LAS const float*)(uintptr_t)(L0 + L_RED + ps * 512 + 256 + l * 4);
        st64_sc1<0>(rw, cso + ps * 1024 + l * 8, 0u, u32x2{asu(t), tag});
      }
    };
    waitv<0, 0>();
    sfor<0, 4>([&](auto J) { constexpr int j = decltype(J)::v; dma_add<0, 65536 + j * 1024, 128>(rc, wo, lwt, (unsigned)(j * (8 * 3584))); });
    BARRIER();
    colsum(0, 0);
    STAMP(2);
    sfor<4, 7>([&](auto J) { constexpr int j = decltype(J)::v; dma_add<0, 65536 + j * 1024, 128>(rc, wo, lwt, (unsigned)(j * (8 * 3584))); });
    u32x4 fm[16];
#pragma unroll
    for (int i = 0; i < 16; ++i) fm[i] = lds128(L0 + i * 1024 + l * 16);
#pragma unroll
    for (int i = 0; i < 16; ++i) PIN(fm[i]);
    waitv<0, 0>();
    sfor<0, 4>([&](auto I) { constexpr int i = decltype(I)::v; dma_add<0, i * 1024, 0>(rfr, fo, lfb, (unsigned)(i * 28800)); });
    BARRIER();
    colsum(65536, 1);
    sfor<0, 4>([&](auto I) { constexpr int i = decltype(I)::v; dma_add<0, 32768 + i * 1024, 0>(rfi, fo, lfb, (unsigned)(i * 28800)); });
    waitv<0, 0>();
    BARRIER();
    STAMP(3);
    const int c = 28 * w + (c31 < 28 ? c31 : 27);
    f32x2 wpr[8], wpi[8], wmr[8], wmi[8];
    {
      const unsigned fa0 = L0 + L_FILT + 4 * h * 512 + ((2 * k1) & 15) + (c ? c : 224) * 2;
      unsigned short t[4][16];
#pragma unroll
      for (int i = 0; i < 16; ++i)
#pragma unroll
        for (int j = 0; j < 4; ++j) t[j][i] = *(LAS const unsigned short*)(uintptr_t)(fa0 + ((i & 3) + 8 * (i >> 2)) * 512 + j * 16384);
#pragma unroll
      for (int i = 0; i < 8; ++i) {
        const f32x2 p0 = {bflo(t[0][2 * i]), bflo(t[0][2 * i + 1])}, p1 = {bflo(t[1][2 * i]), bflo(t[1][2 * i + 1])};
        const f32x2 q0 = {bflo(t[2][2 * i]), bflo(t[2][2 * i + 1])}, q1 = {bflo(t[3][2 * i]), bflo(t[3][2 * i + 1])};
        wpr[i] = p0 + p1; wpi[i] = q0 + q1; wmr[i] = p0 - p1; wmi[i] = q0 - q1;
      }
      if (w == 0) {
        const float sc = c ? 1.0f : 0.5f;
#pragma unroll
        for (int i = 0; i < 8; ++i) { wpr[i] *= sc; wpi[i] *= sc; wmr[i] *= sc; wmi[i] *= sc; }
      }
#pragma unroll
      for (int i = 0; i < 8; ++i) { PIN(wpr[i]); PIN(wpi[i]); PIN(wmr[i]); PIN(wmi[i]); }
    }
    const unsigned zst = L0 + 98304 + w * 4096;
    unsigned zw[4], zr = zst + lr * 128 + ((lc ^ lr) << 4);
    unsigned zg = (unsigned)((8 * (lr >> 2) + (lr & 3)) * Z_G + lc * 16), ma = L0 + 16384 + l * 16;
    unsigned evo = (unsigned)(WS_E + k1 * E_K1 + h * 2 * E_PN + c * 16), eco = (unsigned)(WS_EC + (28 * h + (c >> 4)) * 2048 + k1 * 64 + (c & 15) * 4);
#pragma unroll
    for (int i = 0; i < 4; ++i) {
      zw[i] = zst + c31 * 128 + (((4 * (i >> 1) + 2 * h + (i & 1)) ^ (c31 & 7)) << 4);
      PIN(zw[i]);
    }
    PIN(zr); PIN(zg); PIN(ma); PIN(evo); PIN(eco);
    STAMP(4);
    if (w == 0) poll_flags(rw, (unsigned)(WS_HINT + k1 * 128), tag);
    asm volatile("s_barrier" ::: "memory");
    {
      u32x4 ef[8];
      unsigned ec[8];
      STAMP(5);
      sfor<0, 8>([&](auto S) {
        constexpr int s = decltype(S)::v;
        ld128_sc1<0, 0>(ef[s], rw, evo, (unsigned)(((s >> 2) + 4 * (s & 3)) * E_PN));
        ld32s_sc1<0, 0>(ec[s], rw, eco, (unsigned)(((s >> 2) + 4 * (s & 3)) * 28672));
      });
      f32x16 ac[2];
      f32x2 pr[2][8], pi[2][8];
      u32x4 mi[12];
      auto pair = [&](const f32x16& v, int i) { return f32x2{v[2 * i], v[2 * i + 1]}; };
      sfor<0, 2>([&](auto HF) {
        constexpr int hf = decltype(HF)::v;
#pragma unroll
        for (int i = 0; i < 16; ++i) { ac[0][i] = 0.f; ac[1][i] = 0.f; }
        sfor<0, 4>([&](auto S) {
          constexpr int s = 4 * hf + decltype(S)::v;
          waitv<14 - 2 * s, 0>();
          PIN(ef[s]); PIN(ec[s]);
          {
            if (__builtin_expect(mism((tag + ef[s].x + ef[s].y) + ef[s].z + ef[s].w, ec[s]) != 0, 0))
            { RETRY(16 + s); reload_e(ef[s], ec[s], rw, evo, eco, (unsigned)(((s >> 2) + 4 * (s & 3)) * E_PN), tag); }
          }
          ac[0] = mfma32(fm[2 * s], ef[s], ac[0]);
          ac[1] = mfma32(fm[2 * s + 1], ef[s], ac[1]);
          SB();
        });
        if constexpr (hf == 1) {
#pragma unroll
          for (int i = 0; i < 12; ++i) mi[i] = lds128(ma + i * 1024);
          st32_sc1<0>(rw, tid >= 480 ? (unsigned)(WS_FLAGZ + k1 * 128 + (tid - 480) * 4) : OOB, 0u, tag);
          STAMP(6);
        }
#pragma unroll
        for (int par = 0; par < 2; ++par) {
          const f32x2* ur = (par ^ hf) ? wmr : wpr;
          const f32x2* ui = (par ^ hf) ? wmi : wpi;
#pragma unroll
          for (int i = 0; i < 8; ++i) {
            if (hf == 0) { pr[par][i] = pair(ac[0], i) * ur[i]; pi[par][i] = pair(ac[0], i) * ui[i]; }
            else { pr[par][i] += pair(ac[0], i) * ur[i]; pi[par][i] += pair(ac[0], i) * ui[i]; }
          }
          SB();
#pragma unroll
          for (int i = 0; i < 8; ++i) { pr[par][i] -= pair(ac[1], i) * ui[i]; pi[par][i] += pair(ac[1], i) * ur[i]; }
          SB();
          if constexpr (hf == 1) {
            f32x16 zre, zim;
#pragma unroll
            for (int i = 0; i < 16; ++i) { zre[i] = 0.f; zim[i] = 0.f; }
#pragma unroll
            for (int s2 = 0; s2 < 2; ++s2) {
              const f32x2 *a_ = pr[par] + 4 * s2, *b_ = pi[par] + 4 * s2;
              const u32x4 fr = {pk2(a_[0][0], a_[0][1]), pk2(a_[1][0], a_[1][1]), pk2(a_[2][0], a_[2][1]), pk2(a_[3][0], a_[3][1])};
              const u32x4 fi = {pk2(b_[0][0], b_[0][1]), pk2(b_[1][0], b_[1][1]), pk2(b_[2][0], b_[2][1]), pk2(b_[3][0], b_[3][1])};
              const u32x4* mm = mi + (par * 2 + s2) * 3;
              zre = mfma32(fr, mm[0], zre);
              zim = mfma32(fr, mm[1], zim);
              zre = mfma32(fi, mm[2], zre);
              zim = mfma32(fi, mm[0], zim);
            }
            SB();
            u32x2 zp[2][4];
#pragma unroll
            for (int g4 = 0; g4 < 4; ++g4) {
              zp[0][g4] = u32x2{pk2(zre[4 * g4], zre[4 * g4 + 1]), pk2(zre[4 * g4 + 2], zre[4 * g4 + 3])};
              zp[1][g4] = u32x2{pk2(zim[4 * g4], zim[4 * g4 + 1]), pk2(zim[4 * g4 + 2], zim[4 * g4 + 3])};
              if (g4 < 3) {
                *(LAS u32x2*)(uintptr_t)(zw[g4 >> 1] + (g4 & 1) * 8) = zp[0][g4];
                *(LAS u32x2*)(uintptr_t)(zw[2 + (g4 >> 1)] + (g4 & 1) * 8) = zp[1][g4];
              }
            }
            {
              unsigned t3 = (zp[0][3].x + zp[0][3].y + zp[1][3].x) + zp[1][3].y;
              unsigned sm = csum8(h ? 0u : t3, cat(zp[0][0], zp[0][1]), cat(zp[1][0], zp[1][1]));
              sm = (sm + zp[0][2].x + zp[0][2].y) + zp[1][2].x + zp[1][2].y;
              auto sw = __builtin_amdgcn_permlane32_swap(sm, sm, false, false);
              const unsigned a0 = sw[0], a1 = sw[1], V = tag - a0 - a1, V2 = (V & 0x40004000u) >> 1;
              *(LAS u32x2*)(uintptr_t)(zw[1] + 8) = h ? u32x2{V & ~0x40004000u, V2} : zp[0][3];
              *(LAS u32x2*)(uintptr_t)(zw[3] + 8) = h ? u32x2{V2, 0u} : zp[1][3];
            }
            u32x4 v[4];
#pragma unroll
            for (int i = 0; i < 4; ++i) v[i] = lds128(zr + i * 1024);
            const unsigned zb = (unsigned)(WS_Z + par * 4 * Z_G + w * 4096 + k1 * 128);
#pragma unroll
            for (int i = 0; i < 4; ++i) st128_sc1<0, 0>(rw, zg, zb + i * (16 * Z_G), v[i]);
            SB();
          }
        }
      });
      STAMP(7);
      asm volatile("gf_code_end:");
    }
  }
}

extern "C" void kernel_launch(void* const* d_in, const int* in_sizes, int n_in, void* d_out, int out_size, void* d_ws,
                              size_t ws_size, hipStream_t stream) {
  (void)in_sizes; (void)n_in; (void)out_size; (void)ws_size;
  (void)hipFuncSetAttribute((const void*)gf_glu_kernel, hipFuncAttributeMaxDynamicSharedMemorySize, L_TOTAL);
  gf_glu_kernel<<<256, 512, L_TOTAL, stream>>>((const char*)d_in[0], (const char*)d_in[1], (const char*)d_in[2],
                                               (const char*)d_in[4], (const char*)d_in[5], (const char*)d_in[6],
                                               (char*)d_out, (char*)d_ws);
}
```

```cpp
#include <hip/hip_runtime.h>
#include <hip/hip_bf16.h>
#include <stdint.h>

typedef __bf16 bf16x8 __attribute__((ext_vector_type(8)));
typedef __bf16 bf16x2 __attribute__((ext_vector_type(2)));
typedef short s16x4 __attribute__((ext_vector_type(4)));
typedef float f32x2 __attribute__((ext_vector_type(2)));
typedef float f32x4 __attribute__((ext_vector_type(4)));
typedef float f32x16 __attribute__((ext_vector_type(16)));
typedef unsigned u32x4 __attribute__((ext_vector_type(4)));
typedef unsigned u32x2 __attribute__((ext_vector_type(2)));
#define LAS __attribute__((address_space(3)))
#define DI __device__ __forceinline__

constexpr unsigned WS_EPOCH = 0;
constexpr unsigned WS_STAT = 65536;
constexpr unsigned WS_CS = 131072;
constexpr unsigned WS_FLAGZ = 196608;
constexpr unsigned WS_HINT = 229376;
constexpr unsigned WS_EC = 524288;
constexpr unsigned WS_E = 1u << 20;
constexpr unsigned WS_Z = 3u << 20;
constexpr unsigned WS_BYTES = 14u << 20;
constexpr unsigned WS_H = 0x520000;
constexpr unsigned HBLK = 4608;
constexpr unsigned E_PN = 3584, E_K1 = 16 * 3584 + 256, Z_G = 32768 + 640;

constexpr unsigned L_AUX = 131072;
constexpr unsigned L_PRIV = L_AUX;
constexpr unsigned L_ZOK = L_AUX + 3072;
constexpr unsigned L_EB = L_AUX + 3584;
constexpr unsigned L_PAUX = L_AUX + 12288;
constexpr unsigned L_P1 = L_AUX + 28672;
constexpr unsigned L_TOTAL = 163840;

constexpr unsigned SPIN_CAP = 1u << 17;

template <int N> struct IC { static constexpr int v = N; };
template <int B, int E, class Fn> DI void sfor(Fn&& f) {
  if constexpr (B < E) { f(IC<B>{}); sfor<B + 1, E>(f); }
}

DI unsigned pk2(float a, float b) { f32x2 t = {a, b}; bf16x2 p = __builtin_convertvector(t, bf16x2); return __builtin_bit_cast(unsigned, p); }
DI bf16x8 asbf(u32x4 v) { return __builtin_bit_cast(bf16x8, v); }
DI float asf(unsigned u) { return __builtin_bit_cast(float, u); }
DI unsigned asu(float f) { return __builtin_bit_cast(unsigned, f); }
DI float bflo(unsigned u) { return asf(u << 16); }
DI float bfhi(unsigned u) { return asf(u & 0xffff0000u); }
DI f32x4 mfma16(u32x4 a, u32x4 b, f32x4 c) { return __builtin_amdgcn_mfma_f32_16x16x32_bf16(asbf(a), asbf(b), c, 0, 0, 0); }
DI f32x16 mfma32(u32x4 a, u32x4 b, f32x16 c) { return __builtin_amdgcn_mfma_f32_32x32x16_bf16(asbf(a), asbf(b), c, 0, 0, 0); }
DI void mma(f32x4& c, const u32x4& a, const u32x4& b) { asm volatile("v_mfma_f32_16x16x32_bf16 %0, %1, %2, %0" : "+v"(c) : "v"(a), "v"(b)); }
DI void mma0(f32x4& c, const u32x4& a, const u32x4& b) { asm volatile("v_mfma_f32_16x16x32_bf16 %0, %1, %2, 0" : "=&v"(c) : "v"(a), "v"(b)); }
DI void mmac(f32x4& d, const u32x4& a, const u32x4& b, const f32x4& c) { asm volatile("v_mfma_f32_16x16x32_bf16 %0, %1, %2, %3" : "=&v"(d) : "v"(a), "v"(b), "v"(c)); }
DI float dot2(unsigned a, unsigned b, float c) {
  return __builtin_amdgcn_fdot2_f32_bf16(__builtin_bit_cast(bf16x2, a), __builtin_bit_cast(bf16x2, b), c, false);
}
DI float sum8(u32x4 v, float c) { unsigned a = v.x, b = v.y, d = v.z, e = v.w; const unsigned o = 0x3f803f80u;
  return dot2(e, o, dot2(d, o, dot2(b, o, dot2(a, o, c)))); }
DI float sq8(u32x4 v, float c) { unsigned a = v.x, b = v.y, d = v.z, e = v.w;
  return dot2(e, e, dot2(d, d, dot2(b, b, dot2(a, a, c)))); }
template <int CTRL> DI float dpp(float v) { return asf((unsigned)__builtin_amdgcn_update_dpp(0, (int)asu(v), CTRL, 0xf, 0xf, true)); }
DI u32x4 lds128(unsigned a) { return *(LAS const u32x4*)(uintptr_t)a; }
DI f32x4 lds128f(unsigned a) { return *(LAS const f32x4*)(uintptr_t)a; }
DI u32x2 ldstr1(unsigned a) { return __builtin_bit_cast(u32x2, __builtin_amdgcn_ds_read_tr16_b64_v4i16((LAS s16x4*)(uintptr_t)a)); }
DI u32x4 cat(u32x2 lo, u32x2 hi) { return __builtin_shufflevector(lo, hi, 0, 1, 2, 3); }
DI u32x4 ldstr(unsigned a0, unsigned a1) {
  s16x4 lo = __builtin_amdgcn_ds_read_tr16_b64_v4i16((LAS s16x4*)(uintptr_t)a0);
  s16x4 hi = __builtin_amdgcn_ds_read_tr16_b64_v4i16((LAS s16x4*)(uintptr_t)a1);
  return __builtin_bit_cast(u32x4, __builtin_shufflevector(lo, hi, 0, 1, 2, 3, 4, 5, 6, 7));
}
typedef u32x4 rsrc_t;
DI rsrc_t mk_rsrc(const void* p, unsigned bytes) {
  const unsigned long long a = (unsigned long long)p;
  rsrc_t d = {(unsigned)a, (unsigned)(a >> 32) & 0xffffu, bytes, 0x00020000u};
  asm volatile("" : "+s"(d));
  return d;
}
template <int ID, int LIMM, int MUL> DI void dma_mul(rsrc_t rs, unsigned voff, unsigned lbase, unsigned k) {
  unsigned t;
  asm volatile("s_add_u32 m0, %3, %5\n\ts_mul_i32 %0, %4, %6\n\tbuffer_load_dwordx4 %1, %2, %0 offen lds ; OP %7"
               : "=&s"(t) : "v"(voff), "s"(rs), "s"(lbase), "s"(k), "i"(LIMM), "i"(MUL), "i"(ID) : "memory", "scc");
}
template <int ID, int LIMM, int ADD> DI void dma_add(rsrc_t rs, unsigned voff, unsigned lbase, unsigned k) {
  unsigned t;
  asm volatile("s_add_u32 m0, %3, %5\n\ts_add_u32 %0, %4, %6\n\tbuffer_load_dwordx4 %1, %2, %0 offen lds ; OP %7"
               : "=&s"(t) : "v"(voff), "s"(rs), "s"(lbase), "s"(k), "i"(LIMM), "i"(ADD), "i"(ID) : "memory", "scc");
}
template <int ID, int LIMM, int ADD> DI void dma_add_sc1(rsrc_t rs, unsigned voff, unsigned lbase, unsigned k) {
  unsigned t;
  asm volatile("s_add_u32 m0, %3, %5\n\ts_add_u32 %0, %4, %6\n\tbuffer_load_dwordx4 %1, %2, %0 offen sc1 lds ; OP %7"
               : "=&s"(t) : "v"(voff), "s"(rs), "s"(lbase), "s"(k), "i"(LIMM), "i"(ADD), "i"(ID) : "memory", "scc");
}
template <int ID, int IMM> DI void st128_sc1(rsrc_t rs, unsigned voff, unsigned soff, u32x4 v) {
  asm volatile("buffer_store_dwordx4 %0, %1, %2, %3 offen offset:%4 sc1 ; OP %5\n\ts_nop 1" ::"v"(v), "v"(voff), "s"(rs), "Is"(soff), "i"(IMM), "i"(ID) : "memory");
}
template <int ID> DI void st64_sc1(rsrc_t rs, unsigned voff, unsigned soff, u32x2 v) {
  asm volatile("buffer_store_dwordx2 %0, %1, %2, %3 offen sc1 ; OP %4" ::"v"(v), "v"(voff), "s"(rs), "Is"(soff), "i"(ID) : "memory");
}
template <int ID> DI void st32_sc1(rsrc_t rs, unsigned voff, unsigned soff, unsigned v) {
  asm volatile("buffer_store_dword %0, %1, %2, %3 offen sc1 ; OP %4" ::"v"(v), "v"(voff), "s"(rs), "Is"(soff), "i"(ID) : "memory");
}
template <int ID, int IMM> DI void ld128_sc1(u32x4& dst, rsrc_t rs, unsigned voff, unsigned soff) {
  asm volatile("buffer_load_dwordx4 %0, %1, %2, %3 offen offset:%4 sc1 ; OP %5" : "=v"(dst) : "v"(voff), "s"(rs), "Is"(soff), "i"(IMM), "i"(ID) : "memory");
}
template <int ID, int IMM> DI void ld128(u32x4& dst, rsrc_t rs, unsigned voff, unsigned soff) {
  asm volatile("buffer_load_dwordx4 %0, %1, %2, %3 offen offset:%4 ; OP %5" : "=v"(dst) : "v"(voff), "s"(rs), "Is"(soff), "i"(IMM), "i"(ID) : "memory");
}
template <int ID, int IMM> DI void ld32s_sc1(unsigned& dst, rsrc_t rs, unsigned voff, unsigned soff) {
  asm volatile("buffer_load_dword %0, %1, %2, %3 offen offset:%4 sc1 ; OP %5" : "=v"(dst) : "v"(voff), "s"(rs), "s"(soff), "i"(IMM), "i"(ID) : "memory");
}
template <int ID> DI void ld32_plain(unsigned& dst, rsrc_t rs, unsigned voff) {
  asm volatile("buffer_load_dword %0, %1, %2, 0 offen ; OP %3" : "=v"(dst) : "v"(voff), "s"(rs), "i"(ID) : "memory");
}
template <int ID> DI void ld32_sc1(unsigned& dst, rsrc_t rs, unsigned voff) {
  asm volatile("buffer_load_dword %0, %1, %2, 0 offen sc1 ; OP %3" : "=v"(dst) : "v"(voff), "s"(rs), "i"(ID) : "memory");
}
template <int N, int NEED> DI void waitv() { asm volatile("s_waitcnt vmcnt(%0) ; NEED %1" ::"i"(N), "i"(NEED) : "memory"); }
template <int NEED> DI void need() { asm volatile("; NEED %0" ::"i"(NEED) : "memory"); }
#define BARRIER() asm volatile("s_waitcnt lgkmcnt(0)\n\ts_barrier" ::: "memory")
#define SB() __builtin_amdgcn_sched_barrier(0)
#define ONLY(g) asm volatile("; ONLY " #g ::: "memory")
DI void keep(const f32x4& a, const f32x4& b) { asm volatile("" ::"v"(a), "v"(b)); }
template <class T> DI void PINS(T& x) { asm volatile("" : "+s"(x)); }
DI float opq(float x) { asm volatile("" : "+v"(x)); return x; }
template <class T> DI void PIN(T& x) { asm volatile("" : "+v"(x)); }

DI void poll_flags(rsrc_t rs, unsigned voff, unsigned tag) {
#pragma nounroll
  for (unsigned spins = 0; spins < SPIN_CAP; ++spins) {
    unsigned got;
    asm volatile("buffer_load_dword %0, %1, %2, 0 offen sc1\n\ts_waitcnt vmcnt(0)" : "=v"(got) : "v"(voff), "s"(rs) : "memory");
    if (__all(got == tag)) break;
    __builtin_amdgcn_s_sleep(3);
  }
}
DI void poll_granules(u32x4& g, rsrc_t rs, unsigned voff, unsigned tag) {
#pragma nounroll
  for (unsigned spins = 0; spins < SPIN_CAP; ++spins) {
    __builtin_amdgcn_s_sleep(4);
    asm volatile("buffer_load_dwordx4 %0, %1, %2, 0 offen sc1\n\ts_waitcnt vmcnt(0)" : "=&v"(g) : "v"(voff), "s"(rs) : "memory");
    if (__all(g.y == tag && g.w == tag)) break;
  }
}
DI unsigned csum8(unsigned t, const u32x4& a, const u32x4& b) { return ((t + a.x + a.y) + a.z + a.w) + ((b.x + b.y + b.z) + b.w); }
DI void reload_h(u32x4* d, unsigned* ck, rsrc_t rs, unsigned vo, unsigned vc, unsigned so, unsigned tag) {
#pragma nounroll
  for (unsigned spins = 0; spins < SPIN_CAP; ++spins) {
    __builtin_amdgcn_s_sleep(8);
    asm volatile("buffer_load_dwordx4 %0, %6, %8, %9 offen sc1\n\tbuffer_load_dwordx4 %1, %6, %8, %9 offen offset:1024 sc1\n\t"
                 "buffer_load_dwordx4 %2, %6, %8, %9 offen offset:2048 sc1\n\tbuffer_load_dwordx4 %3, %6, %8, %9 offen offset:3072 sc1\n\t"
                 "buffer_load_dword %4, %7, %8, %9 offen sc1\n\tbuffer_load_dword %5, %7, %8, %9 offen offset:256 sc1\n\ts_waitcnt vmcnt(0)"
                 : "=&v"(d[0]), "=&v"(d[1]), "=&v"(d[2]), "=&v"(d[3]), "=&v"(ck[0]), "=&v"(ck[1])
                 : "v"(vo), "v"(vc), "s"(rs), "s"(so) : "memory");
    if (__all(csum8(tag, d[0], d[1]) == ck[0] && csum8(tag, d[2], d[3]) == ck[1])) break;
  }
}
DI void reload_e(u32x4& d, unsigned& ck, rsrc_t rs, unsigned vo, unsigned vc, unsigned so, unsigned tag) {
#pragma nounroll
  for (unsigned spins = 0; spins < SPIN_CAP; ++spins) {
    __builtin_amdgcn_s_sleep(10);
    asm volatile("buffer_load_dwordx4 %0, %2, %4, %5 offen sc1\n\tbuffer_load_dword %1, %3, %4, %6 offen sc1\n\ts_waitcnt vmcnt(0)"
                 : "=&v"(d), "=&v"(ck) : "v"(vo), "v"(vc), "s"(rs), "s"(so), "s"(so * 8) : "memory");
    if (__all((tag + d.x + d.y) + d.z + d.w == ck)) break;
  }
}
DI unsigned long long mism(unsigned v, unsigned tag) { unsigned long long m_ = __builtin_amdgcn_ballot_w64(v != tag); asm volatile("" : "+s"(m_)); return m_; }

#define STAMP(i)
#define RETRY(i)

constexpr int kTileOfX[7] = {0, 2, 3, 5, 6, 8, 9};
constexpr int kTileOfW[14] = {1, 4, 7, 10, 11, 12, 13, 14, 15, 16, 17, 18, 19, 20};
constexpr int kTileKind[21] = {0, 1, 0, 0, 1, 0, 0, 1, 0, 0, 1, 1, 1, 1, 2, 2, 2, 2, 2, 2, 2};
constexpr int kTileIdx[21] = {0, 0, 1, 2, 1, 3, 4, 2, 5, 6, 3, 4, 5, 6, 0, 1, 2, 3, 4, 5, 6};
constexpr int kSegX0[4] = {0, 1, 3, 5}, kSegNX[4] = {1, 2, 2, 2};
constexpr int kIssue0[5] = {5, 8, 10, 13, 16};
constexpr int tile_of_x(int j) { return kTileOfX[j]; }
constexpr int tile_of_w(int s) { return kTileOfW[s]; }
constexpr int tile_kind(int T) { return kTileKind[T]; }
constexpr int tile_idx(int T) { return kTileIdx[T]; }
constexpr unsigned piece_loc(int g, int P) {
  constexpr unsigned pr[8] = {L_PAUX, 5 * 16384, 6 * 16384, 7 * 16384, 0, 16384, 2 * 16384, 3 * 16384};
  return pr[(g ? P % 8 : P % 6)];
}
enum { OP_EP = 29, OP_P = 30, OP_GS = 50, OP_GC1, OP_GC2, OP_E, OP_H1, OP_H2, OP_POLL, OP_XRES, OP_FL, OP_OPT = 59, OP_HE = 60, OP_HL = 70, OP_Z = 80 };

__global__ __launch_bounds__(512, 2) void gf_glu_kernel(const char* x, const char* wr, const char* wi, const char* wg,
                                                        const char* wu, const char* wd, char* out, char* ws) {
  extern __shared__ __attribute__((aligned(16))) char smem[];
  STAMP(0);
  const int tid = threadIdx.x;
  const int w = __builtin_amdgcn_readfirstlane(tid >> 6);
  const int l = tid & 63, r = l & 15, q = l >> 4;
  const int b = blockIdx.x, xcd = b & 7, bi = b >> 3;
  const unsigned L0 = (unsigned)(uintptr_t)(LAS char*)smem;

  unsigned code_touch;
  {
    unsigned long long pc;
    unsigned code_bytes;
    asm volatile("s_getpc_b64 %0\ngf_code_begin:\n\ts_mov_b32 %1, gf_code_end-gf_code_begin" : "=s"(pc), "=s"(code_bytes));
    const char* pcb = (const char*)(pc & ~127ull);
    unsigned off = (unsigned)((bi * 16 + w * 2 + (l & 1)) * 128);
    off = off < code_bytes ? off : 0;
    asm volatile("global_load_dword %0, %1, %2" : "=v"(code_touch) : "v"(off), "s"(pcb) : "memory");
  }
  unsigned epoch;
#define LOAD_EPOCH() asm volatile("global_load_dword %0, %1, %2 ; OP %3" : "=v"(epoch) : "v"(0u), "s"(ws), "i"(OP_EP) : "memory")
#define GET_TAG() PIN(epoch); PIN(code_touch); const unsigned tag = (unsigned)__builtin_amdgcn_readfirstlane(epoch) * 0x9E3779B1u + 0x7F4A7C15u
  constexpr unsigned OOB = 0x40000000u;

  if (__builtin_expect(bi >= 4, 1)) {
    const int bj = bi - 4;
    const int pn = 4 * (xcd & 3) + (bj & 3), tt = 7 * (xcd >> 2) + (bj >> 2);
    int rot = (bj >> 2) + 2 * (xcd & 3) + (bj & 1);
    rot = rot >= 7 ? rot - 7 : rot;
    const int m = w & 3, nh = w >> 2;
    const int rt = 4 * xcd + (bj & 3), ct = bj >> 2;
    unsigned kx[7];
    kx[0] = (unsigned)(rot * 128);
    unsigned xoff[2], woff, doff;
    const int lr = l >> 3, lc = l & 7;
    {
      const int R = 16 * (w & 1) + lr;
      const int n2 = (pn & 1) + 8 * (pn >> 1) + 2 * (w >> 1);
      const unsigned rb = (unsigned)((64 * R + n2) * 896), cx = (unsigned)((lc ^ (lr >> 1)) << 4);
      xoff[0] = rb + cx;
      xoff[1] = rb + 8 * 64 * 896 + (cx ^ 64);
      const int swz = ((lr >> 1) & 1) | ((w & 1) << 1);
      woff = (unsigned)((8 * w + lr) * 3584 + tt * 256 + ((lc ^ (swz << 1)) << 4));
    }
    unsigned lw1 = L0 + w * 1024, lw2 = L0 + w * 2048;
    PINS(lw1); PINS(lw2); PINS(kx[0]); PIN(xoff[0]); PIN(xoff[1]); PIN(woff);
    SB();
    const rsrc_t rx = mk_rsrc(x, 2048 * 896), rg = mk_rsrc(wg, 448 * 3584), ru = mk_rsrc(wu, 448 * 3584);
    auto issue1 = [&](auto TI, auto HI) {
      constexpr int T = decltype(TI)::v, h = decltype(HI)::v;
      constexpr int kind = tile_kind(T), sl = (T & 7) * 16384;
      if constexpr (T == 14 && h == 0) { woff += 128; PIN(woff); }
      if constexpr (kind == 0) dma_mul<T, sl + h * 1024, 1>(rx, xoff[h], lw2, kx[tile_idx(T)]);
      else dma_mul<T, sl + h * 8192, 1792>(h ? ru : rg, woff, lw1, kx[tile_idx(T)]);
    };
    auto issue = [&](auto TI) { issue1(TI, IC<0>{}); issue1(TI, IC<1>{}); };
    issue(IC<0>{});
    issue(IC<1>{});
    LOAD_EPOCH();
    STAMP(1);
    SB();
#pragma unroll
    for (int j = 1; j < 7; ++j) { kx[j] = kx[j - 1] == 768 ? 0 : kx[j - 1] + 128; PINS(kx[j]); }
    SB();

    {
      int c = 16 * tt + 8 * nh + (r >> 1);
      const float ph = (r & 1) ? 0.25f : 0.0f;
      c = c ? c : 224;
      const float cfl = (float)c, cf = cfl * (1.0f / 448.0f);
      const float k2 = 2.0f * __builtin_amdgcn_cosf(cf);
#pragma unroll
      for (int it = 0; it < 4; ++it) {
        const int ks = m + 4 * it;
        if (it < 3 || m < 2) {
          const unsigned kk = (m & 2) ? kx[it < 3 ? 2 * it + 1 : 0] : kx[2 * it];
          const float t = cfl * (float)(int)((kk >> 1) + 32 * (m & 1) + 8 * q);
          const float rem = __builtin_fmaf(-448.0f, __builtin_floorf(t * (1.0f / 448.0f)), t);
          const float rev = __builtin_fmaf(rem, 1.0f / 448.0f, ph);
          float v[8];
          v[0] = __builtin_amdgcn_cosf(rev);
          v[1] = __builtin_amdgcn_cosf(rev + cf);
#pragma unroll
          for (int e = 2; e < 8; ++e) v[e] = __builtin_fmaf(k2, v[e - 1], -v[e - 2]);
          *(LAS u32x4*)(uintptr_t)(L0 + L_AUX + (nh * 14 + ks) * 1024 + l * 16) =
              u32x4{pk2(v[0], v[1]), pk2(v[2], v[3]), pk2(v[4], v[5]), pk2(v[6], v[7])};
        } else {
          const int k1 = 16 * (m & 1) + r;
          float v[4];
#pragma unroll
          for (int e = 0; e < 4; ++e)
            v[e] = __builtin_amdgcn_cosf((float)((k1 * (4 * q + e)) & 31) * (1.0f / 32.0f) - (nh ? 0.25f : 0.0f));
          const unsigned a0 = pk2(v[0], v[1]), a1 = pk2(v[2], v[3]), sg = (r & 1) ? 0x80008000u : 0u;
          *(LAS u32x4*)(uintptr_t)(L0 + L_P1 + (2 * nh + (m & 1)) * 1024 + l * 16) = u32x4{a0, a1, a0 ^ sg, a1 ^ sg};
        }
      }
    }
    unsigned xa[2][2], wa[2][2];
    {
      const int R = 32 * m + r;
      xa[0][0] = L0 + R * 128 + ((q ^ ((R >> 1) & 7)) << 4);
      xa[0][1] = xa[0][0] ^ 64;
      const int qq = r >> 2, pp = r & 3;
      const int swz = ((qq >> 1) & 1) | ((q & 1) << 1);
      wa[0][0] = L0 + (8 * q + qq) * 128 + (((2 * nh) ^ swz) << 5) + pp * 8;
      wa[0][1] = wa[0][0] ^ 32;
#pragma unroll
      for (int i = 0; i < 2; ++i) { xa[1][i] = xa[0][i] + 65536; wa[1][i] = wa[0][i] + 65536; PIN(xa[1][i]); PIN(wa[1][i]); PIN(xa[0][i]); PIN(wa[0][i]); }
    }
    unsigned l16 = l * 16, aux16 = L0 + L_AUX + l * 16, fad = aux16 + nh * 14336;
    PIN(l16); PIN(aux16); PIN(fad);
    STAMP(2);
    SB();
    const rsrc_t rd = mk_rsrc(wd, 1792 * 896), rw = mk_rsrc(ws, WS_BYTES);
    const rsrc_t rtch = mk_rsrc(nh ? wd : ((m & 1) ? wi : wr), nh ? 1792 * 896 : 921600);
    const int g1 = 4 * pn + m;
    unsigned pso[9], hso[9];
    unsigned hst_d, hst_c, ecs, gs_off, gc_off, pq;
    const unsigned priv = L0 + L_PRIV + w * 256;
    auto step_setup = [&](int g, int s) {
      const int ps = s >= 3 + g;
      int v = (s < 3 ? 8 * s + w : m + (g == 0 ? 8 * (s - 3) : (s == 3 ? 24 : (s == 4 ? 4 : 4 * s - 8))));
      pso[s] = (unsigned)((128 * (v >> 1) + 64 * ps + 32 * (v & 1)) * 896);
      hso[s] = (unsigned)(WS_H + (rt * 56 + 28 * ps + v) * HBLK);
      PINS(pso[s]); PINS(hso[s]);
    };
    constexpr unsigned S_ = 16384;
    auto issue_p1 = [&](auto GI, auto PI, auto HI) {
      constexpr int P = decltype(PI)::v, h = decltype(HI)::v;
      dma_add<OP_P + P, piece_loc(decltype(GI)::v, P) + h * 1024, (16 * (P & 1) + 8 * h) * 896>(rd, doff, lw2, pso[P >> 1]);
    };

    u32x4 xf[14][2], ff[4];
    u32x2 fa_[4][2], fb_[4][2];
    f32x4 accD[2], acc[2][4][2];
    float mrow[2], r2row[2];
    u32x4 gran, ebv;
    unsigned pollv;
    u32x4 hb[4][4];
    {
      const unsigned hst = WS_H + (unsigned)((g1 >> 1) * 56 + 2 * tt + nh) * HBLK;
      hst_d = hst + (m & 1) * 2048; hst_c = hst + (m & 1) * 256;
      ecs = (unsigned)(WS_EC + (pn * 14 + tt) * 2048);
      PINS(hst_d); PINS(hst_c); PINS(ecs);
    }
    SB();
    issue(IC<2>{});
    issue(IC<4>{});
    unsigned tag, tch, tch2, tco;
    f32x4 cs[4];
    unsigned hpk[2][4];
    auto h_part = [&](auto PS, int i2, int sub) {
      constexpr int ps = decltype(PS)::v;
      f32x4 h0 = ((acc[ps][sub][i2] - mrow[i2] * cs[sub]) * r2row[i2]) * (acc[ps][sub + 2][i2] - mrow[i2] * cs[sub + 2]);
      hpk[i2][2 * sub] = pk2(h0[0], h0[1]); hpk[i2][2 * sub + 1] = pk2(h0[2], h0[3]);
      PIN(hpk[i2][2 * sub]); PIN(hpk[i2][2 * sub + 1]);
    };
    auto h_store = [&](auto PS, auto I2) {
      constexpr int ps = decltype(PS)::v, i2 = decltype(I2)::v;
      st128_sc1<OP_H1 + ps, i2 * 1024>(rw, l16, hst_d + ps * (28 * HBLK), u32x4{hpk[i2][0], hpk[i2][1], hpk[i2][2], hpk[i2][3]});
    };
    unsigned hck[4][2], l4c, hsum;
    auto h_check_store = [&](auto PS) { st32_sc1<OP_H1 + decltype(PS)::v>(rw, l4c, hst_c + decltype(PS)::v * (28 * HBLK), hsum); };
    auto load_h1 = [&](auto SI, auto II) {
      constexpr int s = decltype(SI)::v, i = decltype(II)::v;
      if constexpr (i < 4) ld128_sc1<OP_HE + s, (i & 3) * 1024>(hb[s & 3][i & 3], rw, l16, hso[s]);
      else ld32s_sc1<OP_HE + s, (i & 1) * 256>(hck[s & 3][i & 1], rw, l4c, hso[s]);
    };
    u32x4 p1c[2], p1s[2], df;
    f32x4 rD[2], tc[2], ts[2];
    float rsr[1], dv[8], ev[4];
    unsigned dpk[4], ea, eo, ebo;
    auto hop = [&](auto OI) {
      constexpr int o = decltype(OI)::v, sub = o / 36, i2 = (o / 18) & 1, i = o % 18, e = i & 3;
      if constexpr (i < 4) dv[e] = opq(__builtin_fmaf(-mrow[i2], cs[sub][e], acc[0][sub][i2][e]));
      else if constexpr (i < 8) ev[e] = opq(__builtin_fmaf(-mrow[i2], cs[sub + 2][e], acc[0][sub + 2][i2][e]));
      else if constexpr (i < 12) dv[e] = opq(dv[e] * r2row[i2]);
      else if constexpr (i < 16) dv[e] = opq(dv[e] * ev[e]);
      else { hpk[i2][2 * sub + (i & 1)] = pk2(dv[2 * (i & 1)], dv[2 * (i & 1) + 1]); PIN(hpk[i2][2 * sub + (i & 1)]); }
    };
    u32x4 gf[2][2][4];
    float g_d[4], g_f[4], g_k[4], g_t[4], g_u[4], g_c[4][3], g_s[4][3], g_cq;
    unsigned g_mk;
    unsigned g_ny;
    auto gop = [&](auto OI) {
      constexpr int o = decltype(OI)::v, hv = o / 136, a = o & 3, dt = a, i = (o % 136) >> 2;
      if constexpr (hv == 1 && (i == 0 || i == 2 || i == 4 || i == 10)) { if constexpr (o == 136) g_cq = opq(g_cq + 28.0f); }
      else if constexpr (i == 0) g_d[a] = opq((float)(64 * ct + 16 * dt + r));
      else if constexpr (i == 1) g_t[a] = opq(g_d[a] * g_cq);
      else if constexpr (i == 2) g_f[a] = opq(g_d[a] * (1.0f / 448.0f));
      else if constexpr (i == 3) g_u[a] = opq(g_t[a] * (1.0f / 448.0f));
      else if constexpr (i == 4) g_k[a] = opq(__builtin_amdgcn_cosf(g_f[a]));
      else if constexpr (i == 5) g_u[a] = opq(__builtin_floorf(g_u[a]));
      else if constexpr (i == 6) g_t[a] = opq(__builtin_fmaf(-448.0f, g_u[a], g_t[a]));
      else if constexpr (i == 7) g_t[a] = opq(g_t[a] * (-1.0f / 448.0f));
      else if constexpr (i == 8) g_u[a] = opq(g_t[a] - g_f[a]);
      else if constexpr (i == 9) g_c[a][0] = opq(__builtin_amdgcn_cosf(g_t[a]));
      else if constexpr (i == 10) g_k[a] = opq(g_k[a] + g_k[a]);
      else if constexpr (i == 11) g_s[a][0] = opq(__builtin_amdgcn_sinf(g_t[a]));
      else if constexpr (i == 12) { unsigned p_ = pk2(g_c[a][0], g_s[a][0]); if constexpr (hv == 0) p_ ^= g_ny; PIN(p_); gf[hv][0][dt][0] = p_; }
      else if constexpr (i == 13) g_c[a][1] = opq(__builtin_amdgcn_cosf(g_u[a]));
      else if constexpr (i == 14) g_s[a][1] = opq(__builtin_amdgcn_sinf(g_u[a]));
      else if constexpr (i == 15) { unsigned p_ = pk2(g_c[a][1], g_s[a][1]); PIN(p_); gf[hv][0][dt][1] = p_; }
      else {
        constexpr int e = 2 + (i - 16) / 3, j = (i - 16) % 3;
        if constexpr (j == 0) g_c[a][e % 3] = opq(__builtin_fmaf(g_k[a], g_c[a][(e - 1) % 3], -g_c[a][(e - 2) % 3]));
        else if constexpr (j == 1) g_s[a][e % 3] = opq(__builtin_fmaf(g_k[a], g_s[a][(e - 1) % 3], -g_s[a][(e - 2) % 3]));
        else { unsigned p_ = pk2(g_c[a][e % 3], g_s[a][e % 3]); if constexpr (e >= 4) p_ &= g_mk; PIN(p_); gf[hv][e >> 2][dt][e & 3] = p_; }
      }
    };

    unsigned long long bad0, bad1;
    unsigned da[3][4];
    u32x2 af[2][4][2];
    auto rda1 = [&](int g, int s, int dt, int h) { af[s & 1][dt][h] = ldstr1(da[piece_loc(g, 2 * s + h) >> 16][dt] + (piece_loc(g, 2 * s + h) & 0xffff)); };
    auto pin_af = [&](int s) {
#pragma unroll
      for (int dt = 0; dt < 4; ++dt) { PIN(af[s & 1][dt][0]); PIN(af[s & 1][dt][1]); }
    };

    u32x4 ph[3][2], pc, psn;
    unsigned pz_off, zo, za[4], zva, ooff, hs[2];
    const int orow = tid >> 3;
    auto prep = [&](auto GI, auto SI, auto JI) {
      constexpr int g = decltype(GI)::v, s = decltype(SI)::v, j = decltype(JI)::v;
      u32x4* hfr = hb[s & 3];
      if constexpr (j == 0) { need<OP_HE + s>(); need<OP_P + 2 * s + 1>(); PIN(hfr[0]); PIN(hfr[1]); PIN(hfr[2]); PIN(hfr[3]); PIN(hck[s & 3][0]); PIN(hck[s & 3][1]); }
      if constexpr (j < 4) { rda1(g, s, j, 0); rda1(g, s, j, 1); }
      if constexpr (j >= 1 && j < 5) {
        constexpr int f = (j - 1) >> 1, o = 2 * ((j - 1) & 1);
        hs[0] = (j == 1 ? tag : hs[0]) + hfr[f][o] + hfr[f][o + 1];
        hs[1] = (j == 1 ? tag : hs[1]) + hfr[2 + f][o] + hfr[2 + f][o + 1];
        PIN(hs[0]); PIN(hs[1]);
      }
      if constexpr (j == 5) { bad0 = mism(hs[0], hck[s & 3][0]); bad1 = mism(hs[1], hck[s & 3][1]); }
      if constexpr (j == 7) { if (__builtin_expect((bad0 | bad1) != 0, 0)) { RETRY(s); reload_h(hfr, hck[s & 3], rw, l16, l4c, hso[s], tag); } }
    };

    unsigned pw0, pw1;
    auto side = [&](auto SI, auto KI) {
      constexpr int s = decltype(SI)::v, k = decltype(KI)::v;
      constexpr int G = 32 * s + k;
#define AT(ss, kk) (G == 32 * (ss) + (kk))
#define TILE(ss, kk, T, h) if constexpr (AT(ss, kk)) issue1(IC<T>{}, IC<h>{})
#define PIECE(ss, kk, P, h) if constexpr (AT(ss, kk)) issue_p1(IC<0>{}, IC<P>{}, IC<h>{})
      if constexpr (AT(0, 7)) issue(IC<3>{});
      TILE(0, 11, 5, 0); TILE(0, 14, 5, 1); TILE(0, 17, 6, 0); TILE(0, 19, 6, 1);
      if constexpr (s == 1 && k % 4 == 0) issue1(IC<7 + k / 8>{}, IC<(k / 4) & 1>{});
      if constexpr (s == 2 && k % 4 == 1) issue1(IC<10 + k / 8>{}, IC<(k >> 2) & 1>{});
      TILE(3, 3, 13, 0); TILE(3, 7, 13, 1); TILE(3, 17, 14, 0); TILE(3, 22, 14, 1);
      TILE(4, 4, 15, 0); TILE(4, 9, 15, 1); TILE(4, 15, 16, 0); TILE(5, 3, 16, 1);
      TILE(5, 11, 17, 0); TILE(5, 15, 17, 1); TILE(6, 4, 18, 0);
      TILE(6, 13, 18, 1); TILE(7, 1, 19, 0); TILE(7, 5, 19, 1); TILE(7, 9, 20, 0); TILE(7, 13, 20, 1);
      if constexpr (AT(1, 13)) {
        const unsigned gw = (unsigned)((bj * 8 + xcd) * 4 + m);
        tco = l < (nh ? 14 : 17) ? ((nh ? gw * 14 : (gw >> 1) * 17) + l) * 128 : OOB; PIN(tco);
      }
      if constexpr (AT(3, 14)) ld32_plain<OP_EP>(tch, rtch, tco);
      PIECE(8, 1, 0, 0); PIECE(8, 4, 0, 1); PIECE(9, 0, 1, 0); PIECE(9, 9, 1, 1);
      PIECE(11, 1, 2, 0); PIECE(11, 5, 2, 1); PIECE(11, 9, 3, 0); PIECE(11, 13, 3, 1);
      if constexpr (s == 12 && k % 4 == 1) load_h1(IC<0>{}, IC<k / 4>{});
      if constexpr (AT(12, 15)) { load_h1(IC<0>{}, IC<4>{}); load_h1(IC<0>{}, IC<5>{}); }
      if constexpr (s == 13 && k % 4 == 2) load_h1(IC<1>{}, IC<k / 4>{});
      if constexpr (AT(13, 15)) { load_h1(IC<1>{}, IC<4>{}); load_h1(IC<1>{}, IC<5>{}); }
      if constexpr (AT(1, 1)) { need<OP_EP>(); PIN(epoch); PIN(code_touch); tag = (unsigned)__builtin_amdgcn_readfirstlane(epoch); PINS(tag); }
      if constexpr (AT(1, 5)) { tag = tag * 0x9E3779B1u + 0x7F4A7C15u; PINS(tag); }
      if constexpr (AT(1, 7)) { gs_off = (unsigned)(WS_STAT + (g1 * 32 + (l & 31)) * 16); PIN(gs_off); }
      if constexpr (AT(1, 10)) { pq = priv + q * 16; pw0 = priv + l * 4; PIN(pq); PIN(pw0); }
      if constexpr (AT(1, 16)) { ea = L0 + L_EB + r * 264 + m * 64 + nh * 32 + q * 8; PIN(ea); }
      if constexpr (AT(1, 19)) { eo = (unsigned)((tid >> 4) * E_K1 + pn * E_PN + (16 * tt + r) * 16); PIN(eo); }
      if constexpr (AT(1, 22)) { ebo = L0 + L_EB + (tid >> 4) * 264 + r * 4; PIN(ebo); }
      if constexpr (AT(2, 18)) { gc_off = (unsigned)(WS_CS + (tt * 4 + (q & 1)) * 512 + (32 * nh + 2 * r) * 8); PIN(gc_off); }
      if constexpr (AT(2, 15)) { pw1 = priv + (l & 31) * 8; PIN(pw1); }
      if constexpr (AT(5, 13)) *(LAS unsigned*)(uintptr_t)(L0 + L_ZOK) = 0u;
      if constexpr (AT(7, 6)) step_setup(0, 0);
      if constexpr (AT(9, 4)) step_setup(0, 1);
      if constexpr (AT(10, 2)) step_setup(0, 2);
      if constexpr (AT(6, 0)) { doff = (unsigned)(lr * 896 + ct * 128 + ((lc ^ (((lr >> 1) & 3) << 1)) << 4)); PIN(doff); }
      if constexpr (AT(6, 2)) { l4c = l * 4 + 4096; PIN(l4c); }
      if constexpr (AT(9, 2)) { const int row = 4 * q + (r >> 2); da[0][0] = L0 + w * 2048 + row * 128 + (((row >> 1) & 3) << 5) + (r & 3) * 8; PIN(da[0][0]); }
      if constexpr (AT(9, 5) || AT(9, 6) || AT(9, 8)) { constexpr int dt = k == 8 ? 3 : k - 4; da[0][dt] = da[0][0] ^ (dt << 5); PIN(da[0][dt]); }
      if constexpr (s == 9 && k >= 10 && k < 14) { da[1][k - 10] = da[0][k - 10] + 65536; da[2][k - 10] = da[0][k - 10] + 131072; PIN(da[1][k - 10]); PIN(da[2][k - 10]); }
      if constexpr (AT(12, 0)) {
        const int gg = 2 * rt + (w >> 2);
        const int n2 = ((gg >> 2) & 1) + 8 * (gg >> 3) + 2 * (gg & 3);
        ooff = (unsigned)((64 * (orow & 31) + n2) * 896 + ct * 128 + lc * 16); PIN(ooff);
      }
      if constexpr (AT(2, 22)) ld128_sc1<OP_GS, 0>(gran, rw, gs_off, 0u);
      if constexpr (AT(3, 10)) { waitv<2, OP_GS>(); PIN(gran); bad0 = mism(gran.y, tag); bad1 = mism(gran.w, tag); }
      if constexpr (AT(3, 13)) {
        if (__builtin_expect((bad0 | bad1) != 0, 0)) { RETRY(12); poll_granules(gran, rw, gs_off, tag); }
        *(LAS unsigned*)(uintptr_t)(pw0) = gran.z;
      }
      if constexpr (AT(3, 15)) {
        auto sw = __builtin_amdgcn_permlane16_swap(gran.x, gran.x, false, false);
        const unsigned a0 = sw[0], a1 = sw[1];
        mrow[0] = opq(asf(a0)); mrow[1] = opq(asf(a1));
      }
      if constexpr (AT(3, 17)) { rsr[0] = opq(asf(gran.z) * asf(gran.z)); }
      if constexpr (AT(3, 21)) {
        auto sw = __builtin_amdgcn_permlane16_swap(asu(rsr[0]), asu(rsr[0]), false, false);
        const unsigned a0 = sw[0], a1 = sw[1];
        r2row[0] = opq(asf(a0)); r2row[1] = opq(asf(a1));
      }
      if constexpr (AT(3, 16) || AT(3, 18)) rD[(k - 16) >> 1] = lds128f(pq + ((k - 16) >> 1) * 64);
      if constexpr (AT(3, 19) || AT(3, 20)) p1s[k - 19] = lds128(aux16 + (L_P1 - L_AUX) + 2048 + (k - 19) * 1024);
      if constexpr (AT(4, 0) || AT(4, 2)) p1c[k >> 1] = lds128(aux16 + (L_P1 - L_AUX) + (k >> 1) * 1024);
      if constexpr (s == 4 && k >= 1 && k < 5) {
        constexpr int e = 2 * (k - 1);
        dv[e] = opq(rD[e >> 2][e & 3] * accD[e >> 2][e & 3]);
        dv[e + 1] = opq(rD[e >> 2][(e & 3) + 1] * accD[e >> 2][(e & 3) + 1]);
      }
      if constexpr (AT(4, 5) || AT(4, 6)) {
        constexpr int e = 4 * (k - 5);
        dpk[e >> 1] = pk2(dv[e], dv[e + 1]); dpk[(e >> 1) + 1] = pk2(dv[e + 2], dv[e + 3]);
        PIN(dpk[e >> 1]); PIN(dpk[(e >> 1) + 1]);
      }
      if constexpr (AT(4, 7)) {
        df = u32x4{dpk[0], dpk[1], dpk[2], dpk[3]};
        mma0(ts[0], df, p1s[0]); mma0(tc[0], df, p1c[0]); mma0(ts[1], df, p1s[1]); mma0(tc[1], df, p1c[1]);
      }
      if constexpr (s == 4 && k >= 10 && k < 14) {
        constexpr int a = (k - 10) >> 1, e = 2 * (k & 1);
        ev[0] = opq(tc[a][e] + ts[a][e + 1]);
        ev[1] = opq(tc[a][e + 1] - ts[a][e]);
        dpk[k & 1] = pk2(ev[0], ev[1]); PIN(dpk[k & 1]);
      }
      if constexpr (AT(4, 11) || AT(4, 13)) *(LAS u32x2*)(uintptr_t)(ea + ((k - 10) >> 1) * (16 * 264)) = u32x2{dpk[0], dpk[1]};
      if constexpr (AT(5, 0)) {
        waitv<6, 13>();
        asm volatile("s_waitcnt lgkmcnt(3)\n\ts_barrier" ::: "memory");
#pragma unroll
        for (int i = 0; i < 4; ++i) ebv[i] = *(LAS const unsigned*)(uintptr_t)(ebo + i * 64);
      }
      if constexpr (AT(5, 5)) ld128_sc1<OP_GC1, 0>(gran, rw, gc_off, 0u);
      if constexpr (AT(5, 4)) { hsum = (tag + ebv.x + ebv.y) + ebv.z + ebv.w; PIN(hsum); tch2 = tid * 4; PIN(tch2); }
      if constexpr (AT(5, 7)) { st128_sc1<OP_E, 0>(rw, eo, WS_E, ebv); st32_sc1<OP_E>(rw, tch2, ecs, hsum); }
      if constexpr (AT(5, 2)) { tco = (tid >= 480 && bj >= 24) ? (unsigned)(WS_HINT + ((bj - 24) * 8 + (xcd ^ 4)) * 128 + (tid - 480) * 4) : OOB; dpk[2] = tag; PIN(tco); PIN(dpk[2]); }
      if constexpr (AT(5, 9)) st32_sc1<OP_E>(rw, tco, 0u, dpk[2]);
      if constexpr (AT(6, 6)) { waitv<6, OP_GC1>(); need<16>(); PIN(gran); bad0 = mism(gran.y, tag); bad1 = mism(gran.w, tag); }
      if constexpr (AT(6, 7)) asm volatile("s_barrier" ::: "memory");
      if constexpr (AT(12, 11)) { waitv<7, OP_GC2>(); PIN(tch); PIN(gran); bad0 = mism(gran.y, tag); bad1 = mism(gran.w, tag); }
      if constexpr (AT(6, 9) || AT(12, 14)) {
        if (__builtin_expect((bad0 | bad1) != 0, 0)) { RETRY(s == 12 ? 14 : 13); poll_granules(gran, rw, gc_off + (s == 12 ? 1024 : 0), tag); }
        *(LAS unsigned*)(uintptr_t)(pw1) = gran.x; *(LAS unsigned*)(uintptr_t)(pw1 + 4) = gran.z;
      }
      if constexpr (AT(6, 12) || AT(6, 14) || AT(7, 1) || AT(7, 3)) { constexpr int i = s == 6 ? k - 12 : k; cs[i] = lds128f(pq + i * 64); }
      if constexpr (s == 13 && k >= 3 && k < 11 && (k & 1)) cs[(k - 3) >> 1] = lds128f(pq + ((k - 3) >> 1) * 64);
      if constexpr ((s == 7 && k >= 8) || (s == 8 && k < 10)) {
        constexpr int o = 4 * (16 * (s - 7) + k - 8);
        hop(IC<o>{}); hop(IC<o + 1>{}); hop(IC<o + 2>{}); hop(IC<o + 3>{});
      }
      if constexpr (AT(10, 8)) ld128_sc1<OP_GC2, 1024>(gran, rw, gc_off, 0u);
      if constexpr (AT(8, 7)) h_store(IC<0>{}, IC<0>{});
      if constexpr (AT(8, 10)) { h_store(IC<0>{}, IC<1>{}); hsum = (tag + hpk[0][0] + hpk[0][1]) + hpk[0][2] + hpk[0][3]; PIN(hsum); }
      if constexpr (AT(8, 11)) { hsum = (hsum + hpk[1][0] + hpk[1][1]) + hpk[1][2] + hpk[1][3]; PIN(hsum); }
      if constexpr (AT(8, 12)) h_check_store(IC<0>{});
      if constexpr (AT(8, 14)) { waitv<11, 17>(); asm volatile("s_barrier" ::: "memory"); }
      if constexpr (AT(10, 7)) { waitv<7, 20>(); asm volatile("s_barrier" ::: "memory"); }
      if constexpr (AT(13, 7)) asm volatile("s_barrier" ::: "memory");
    };

    sfor<0, 14>([&](auto SI) {
      constexpr int s = decltype(SI)::v;
      constexpr int T = tile_of_w(s), sl = T & 7;
      constexpr int ps = s / 7, j = s % 7, pps = (s - 1) / 7, pj = (s + 6) % 7;
      constexpr int x0 = s < 4 ? kSegX0[s < 4 ? s : 0] : 0, nx = s < 4 ? kSegNX[s < 4 ? s : 0] : 0;
      constexpr int waitB[5] = {5, 4, 4, 5, 6}, lastT[5] = {1, 3, 7, 10, 12};
      if constexpr (s <= 4) { waitv<waitB[s <= 4 ? s : 0], lastT[s <= 4 ? s : 0]>(); BARRIER(); }
      SB();
      sfor<0, 16 + 4 * nx>([&](auto KI) {
        constexpr int k = decltype(KI)::v, i = (k & 7) >> 1, h = k & 1;
        if constexpr (k < 8) {
          if constexpr (s > 0) mma(acc[pps][i][h], cat(fb_[i][0], fb_[i][1]), xf[2 * pj + 1][h]);
        } else if constexpr (k < 16) {
          if constexpr (j == 0) mma0(acc[ps][i][h], cat(fa_[i][0], fa_[i][1]), xf[0][h]);
          else mma(acc[ps][i][h], cat(fa_[i][0], fa_[i][1]), xf[2 * j][h]);
        } else {
          if constexpr (s == 0 && k < 18) mma0(accD[h], xf[0][h], ff[0]);
          else mma(accD[h], xf[2 * x0 + i][h], ff[i]);
        }
        SB();
        constexpr int xr = s == 0 ? k : (k < 2 ? k : ((k & 1) ? -1 : 1 + k / 2));
        if constexpr (xr >= 0 && xr < 4 * nx) {
          constexpr int xt = x0 + (xr >> 2), kk = (xr >> 1) & 1, xs = tile_of_x(xt) & 7;
          xf[2 * xt + kk][xr & 1] = lds128(xa[xs >> 2][kk] + (xs & 3) * 16384 + (xr & 1) * 2048);
        }
        if constexpr (k < 16) {
          const unsigned a = wa[sl >> 2][i & 1] + (sl & 3) * 16384 + (i >> 1) * 8192 + (k >> 3) * 4096 + h * 512;
          if constexpr (k < 8) fa_[i][h] = ldstr1(a); else fb_[i][h] = ldstr1(a);
        }
        if constexpr (k >= 9 && (k & 1) && (k - 9) / 2 < 2 * nx) ff[(k - 9) / 2] = lds128(fad + (2 * x0 + (k - 9) / 2) * 1024);
        SB();
        side(SI, KI);
        SB();
      });
      if constexpr (s == 0) STAMP(3);
      if constexpr (s == 3) STAMP(4);
      if constexpr (s == 5) STAMP(5);
      if constexpr (s == 7) STAMP(6);
      if constexpr (s == 9) STAMP(7);
      if constexpr (s == 11) STAMP(8);
    });
    waitv<6, OP_HE + 0>();
    need<OP_P + 3>();
    sfor<0, 8>([&](auto KI) {
      constexpr int k = decltype(KI)::v, i = ((k >> 1) & 1) * 2 + (k >> 2), h = k & 1;
      mma(acc[1][i][h], cat(fb_[i][0], fb_[i][1]), xf[13][h]);
      SB();
      if constexpr (k < 7) prep(IC<0>{}, IC<0>{}, KI);
      SB();
    });
    STAMP(9);
    h_part(IC<1>{}, 0, 0); h_part(IC<1>{}, 1, 0);
    h_part(IC<1>{}, 0, 1); h_store(IC<1>{}, IC<0>{});
    h_part(IC<1>{}, 1, 1); h_store(IC<1>{}, IC<1>{});
    hsum = csum8(tag, u32x4{hpk[0][0], hpk[0][1], hpk[0][2], hpk[0][3]}, u32x4{hpk[1][0], hpk[1][1], hpk[1][2], hpk[1][3]});
    h_check_store(IC<1>{});
    SB();
    prep(IC<0>{}, IC<0>{}, IC<7>{});
    STAMP(10);

    f32x4 oacc[4][4];
    auto gstep = [&](auto SI, auto&& fn) {
      constexpr int s = decltype(SI)::v;
      sfor<0, 16>([&](auto KI) {
        constexpr int k = decltype(KI)::v, dt = k >> 2, rf = k & 3;
        if constexpr (s == 0) mma0(oacc[dt][rf], cat(af[0][dt][0], af[0][dt][1]), hb[0][rf]);
        else mma(oacc[dt][rf], cat(af[s & 1][dt][0], af[s & 1][dt][1]), hb[s & 3][rf]);
        SB();
        fn(KI);
        SB();
      });
    };
    auto gops = [&](auto B0, auto N) { sfor<decltype(B0)::v, (decltype(B0)::v + decltype(N)::v < 272 ? decltype(B0)::v + decltype(N)::v : 272)>([&](auto O) { gop(O); }); };
    auto step_ld = [&](auto GI, auto SI, auto NI) {
      constexpr int s = decltype(SI)::v, n = decltype(NI)::v;
      if constexpr (n == 0 && s > 2) step_setup(decltype(GI)::v, s);
      if constexpr (n < 4) issue_p1(GI, IC<2 * s + n / 2>{}, IC<n & 1>{});
      else load_h1(SI, IC<n - 4>{});
      if constexpr (n == 7) { load_h1(SI, IC<4>{}); load_h1(SI, IC<5>{}); }
    };
    f32x4 fin[2];
    u32x4 sel[4], xd, xres;
    unsigned pw[4], ra[8], bp;
    auto fin_setup = [&](auto KI) {
      constexpr int k = decltype(KI)::v;
      if constexpr (k == 0) { need<OP_XRES>(); PIN(xres); bp = (unsigned)((8 * (r & 7) + 4 * (r >> 3) + q) * 4); PIN(bp); }
      if constexpr (k >= 1 && k < 5) {
        const unsigned t_ = xres[k - 1]; xd[k - 1] = (unsigned)__builtin_amdgcn_ds_bpermute((int)bp, (int)t_);
        const unsigned o_ = r == 4 * (k - 1) + q ? 0x3f803f80u : 0u; sel[k - 1] = u32x4{o_, o_, o_, o_}; PIN(sel[k - 1]);
      }
      if constexpr (k == 5) { pw[0] = lw2 + r * 128 + ((q ^ r ^ (w & 1) ^ ((w & 2) << 2)) << 3); PIN(pw[0]); }
      if constexpr (k == 6) { pw[1] = pw[0] ^ 32; pw[2] = pw[0] ^ 64; pw[3] = pw[0] ^ 96; PIN(pw[1]); PIN(pw[2]); PIN(pw[3]); }
      if constexpr (k == 7) { bp = (unsigned)((lr + 8 * (lc >> 2) + 16 * (lc & 3)) * 4); PIN(bp); }
      if constexpr (k == 8) {
        const int qq = r >> 2, row = 8 * (w & 1) + q;
        ra[0] = L0 + (w >> 1) * 16384 + qq * 2048 + row * 128 + (((2 * (r & 3)) ^ row ^ (qq & 1) ^ ((qq & 2) << 2)) << 3); PIN(ra[0]);
      }
      if constexpr (k == 9) {
#pragma unroll
        for (int i = 1; i < 8; ++i) { ra[i] = ra[0] ^ (((i >> 1) & 1) << 5) ^ ((i >> 2) << 6) ^ ((i & 1) << 3); PIN(ra[i]); }
      }
      if constexpr (k == 10) {
#pragma unroll
        for (int i = 0; i < 4; ++i) { const unsigned t_ = xd[i]; fin[i >> 1][2 * (i & 1)] = opq(bflo(t_)); fin[i >> 1][2 * (i & 1) + 1] = opq(bfhi(t_)); }
      }
    };
    auto put = [&](int dt, int rf) {
      const f32x4 o = oacc[dt][rf];
      *(LAS u32x2*)(uintptr_t)(pw[dt] + rf * 16384) = u32x2{pk2(o[0], o[1]), pk2(o[2], o[3])};
    };
    waitv<3, OP_HE + 1>();
#pragma unroll
    for (int i = 0; i < 4; ++i) PIN(hb[1][i]);
    PIN(hck[1][0]); PIN(hck[1][1]);
    if (nh) {
      ONLY(Y);
      using G1 = IC<1>;
      gstep(IC<0>{}, [&](auto KI) {
        constexpr int k = decltype(KI)::v;
        if constexpr (k == 0 || k == 3 || k == 6) step_ld(G1{}, IC<2>{}, IC<k / 3>{});
        if constexpr (k >= 8) prep(G1{}, IC<1>{}, IC<k - 8>{});
      });
      gstep(IC<1>{}, [&](auto KI) {
        constexpr int k = decltype(KI)::v;
        if constexpr (k >= 11) step_ld(G1{}, IC<2>{}, IC<k - 8>{});
      });
      asm volatile("s_barrier" ::: "memory");
      STAMP(11);
      sfor<0, 8>([&](auto NI) { step_ld(G1{}, IC<3>{}, NI); });
      ld128<OP_XRES, 0>(xres, rx, ooff, 0u);
      waitv<11, OP_HE + 2>();
      sfor<0, 8>([&](auto JI) { prep(G1{}, IC<2>{}, JI); });
      STAMP(12);
      sfor<2, 9>([&](auto SI) {
        constexpr int s = decltype(SI)::v;
        gstep(SI, [&](auto KI) {
          constexpr int k = decltype(KI)::v;
          if constexpr (s + 2 < 9 && k < 8) step_ld(G1{}, IC<s + 2>{}, KI);
          if constexpr (s < 8 && k == 8) waitv<(s == 2 ? 10 : (s < 7 ? 10 : 0)), OP_HE + (s < 8 ? s + 1 : s)>();
          if constexpr (s < 8 && k >= 8) prep(G1{}, IC<(s < 8 ? s + 1 : s)>{}, IC<k - 8>{});
          if constexpr (s == 3 && k < 8) fin_setup(KI);
          if constexpr (s == 4 && k < 6 && !(k & 1)) fin_setup(IC<8 + k / 2>{});
          if constexpr (s == 8 && k < 3) asm volatile("s_nop 2");
          if constexpr (s == 8 && k >= 3) put((k - 3) >> 2, (k - 3) & 3);
        });
        if constexpr (s == 3) __builtin_amdgcn_s_setprio(1);
        if constexpr (s == 3) STAMP(13);
        if constexpr (s == 5) STAMP(15);
        if constexpr (s == 7) STAMP(17);
      });
      STAMP(19);
      put(3, 1); SB(); put(3, 2); SB(); put(3, 3);
      ONLY(END);
    } else {
      ONLY(O);
      using G0 = IC<0>;
      g_cq = opq((float)(56 * m + 8 * q));
      g_mk = q == 3 ? 0u : ~0u;
      g_ny = (tid < 16 && (tid & 1)) ? 0x8000u : 0u;
      auto phop = [&](auto NI) {
        constexpr int n = decltype(NI)::v, i = n >> 2, v = n & 3;
        const unsigned c_ = pc[i], s_ = psn[i];
        unsigned u0 = (v & 1) ? __builtin_amdgcn_perm(c_, s_, (v & 2) ? 0x07060302u : 0x05040100u) : __builtin_amdgcn_perm(s_, c_, (v & 2) ? 0x07060302u : 0x05040100u);
        if constexpr (v == 0) { u0 ^= 0x80000000u; PIN(u0); ph[0][0][i] = u0; }
        if constexpr (v == 1) { PIN(u0); ph[0][1][i] = u0; }
        if constexpr (v == 2) { unsigned u1 = u0 ^ 0x8000u; u0 ^= 0x80000000u; PIN(u0); PIN(u1); ph[1][0][i] = u0; ph[2][0][i] = u1; }
        if constexpr (v == 3) { unsigned u1 = u0 ^ 0x80008000u; PIN(u0); PIN(u1); ph[1][1][i] = u0; ph[2][1][i] = u1; }
      };
      gstep(IC<0>{}, [&](auto KI) {
        constexpr int k = decltype(KI)::v;
        if constexpr (k == 0 || k == 3 || k == 6) step_ld(G0{}, IC<2>{}, IC<k / 3>{});
        if constexpr (k >= 8) prep(G0{}, IC<1>{}, IC<k - 8>{});
        if constexpr (k < 8) gops(IC<4 * k>{}, IC<4>{}); else gops(IC<32 + 2 * (k - 8)>{}, IC<2>{});
      });
      gstep(IC<1>{}, [&](auto KI) {
        constexpr int k = decltype(KI)::v;
        gops(IC<48 + 4 * k>{}, IC<4>{});
        if constexpr (k >= 11) step_ld(G0{}, IC<2>{}, IC<k - 8>{});
      });
      asm volatile("s_barrier" ::: "memory");
      STAMP(11);
      sfor<0, 8>([&](auto NI) { step_ld(G0{}, IC<3>{}, NI); SB(); gops(IC<112 + 3 * decltype(NI)::v>{}, IC<3>{}); SB(); });
      pz_off = w ? OOB : (unsigned)(WS_FLAGZ + (l & 31) * 128);
      ld128<OP_XRES, 0>(xres, rx, ooff, 0u);
      ld32_sc1<OP_POLL>(pollv, rw, pz_off);
      waitv<12, OP_HE + 2>();
      sfor<0, 8>([&](auto JI) { prep(G0{}, IC<2>{}, JI); });
      STAMP(12);
      gstep(IC<2>{}, [&](auto KI) {
        constexpr int k = decltype(KI)::v;
        if constexpr (k < 8) step_ld(G0{}, IC<4>{}, KI);
        if constexpr (k == 1) { zo = (unsigned)(((l >> 1) & 7) ^ ((q & 1) | ((q & 2) << 1))); PIN(zo); }
        if constexpr (k == 2) { zo = q * 256 + (zo << 5) + (l & 1) * 16; PIN(zo); }
        if constexpr (k == 3) { const int P = 2 * q + (r >> 3); za[0] = (unsigned)(((r >> 1) & 2) ^ ((P & 1) | ((P & 2) << 1))); za[1] = P * 256 + (r & 3) * 8; PIN(za[0]); PIN(za[1]); }
        if constexpr (k == 4) { za[0] = lw2 + za[1] + (za[0] << 5); zva = lw2 + l16; PIN(za[0]); PIN(zva); }
        if constexpr (k == 5) { za[1] = za[0] ^ 128; za[2] = za[0] ^ 32; za[3] = za[0] ^ 160; PIN(za[1]); PIN(za[2]); PIN(za[3]); }
        if constexpr (k == 14) pc = lds128(aux16 + (L_P1 - L_AUX));
        if constexpr (k == 15) psn = lds128(aux16 + (L_P1 - L_AUX) + 2048);
        if constexpr (k == 8) waitv<10, OP_POLL>();
        if constexpr (k >= 8) prep(G0{}, IC<3>{}, IC<k - 8>{});
      });
      STAMP(13);
      PIN(pollv);
      unsigned zso = (unsigned)(WS_Z + 2 * rt * Z_G + 2 * m * 4096);
      if (w == 0) {
        if (mism(pollv, tag) != 0) { RETRY(9); poll_flags(rw, pz_off, tag); }
        *(volatile LAS unsigned*)(uintptr_t)(L0 + L_ZOK) = 1u;
      } else {
#pragma nounroll
        for (unsigned spins = 0; spins < 64 * SPIN_CAP; ++spins) {
          if (__builtin_amdgcn_readfirstlane(*(volatile LAS unsigned*)(uintptr_t)(L0 + L_ZOK))) break;
          __builtin_amdgcn_s_sleep(1);
        }
      }
      asm volatile("" ::: "memory");
      auto issue_z1 = [&](auto HV, auto NI, auto IDI) {
        constexpr int hv = decltype(HV)::v, zi = decltype(NI)::v >> 1, zh = decltype(NI)::v & 1;
        dma_add_sc1<decltype(IDI)::v, (4 * hv + zi) * S_ + zh * 1024, hv * 4096 + (zi >> 1) * Z_G + (zi & 1) * 2048 + zh * 1024>(rw, zo, lw2, zso);
      };
      gstep(IC<3>{}, [&](auto KI) {
        constexpr int k = decltype(KI)::v;
        if constexpr (k < 8) { issue_z1(IC<0>{}, KI, IC<OP_Z>{}); phop(IC<2 * k>{}); phop(IC<2 * k + 1>{}); }
        if constexpr (k == 8) waitv<8, OP_HE + 4>();
        if constexpr (k >= 8) prep(G0{}, IC<4>{}, IC<k - 8>{});
      });
      pin_af(4);
      gstep(IC<4>{}, [&](auto KI) {
        constexpr int k = decltype(KI)::v;
        if constexpr (!(k & 1)) issue_z1(IC<1>{}, IC<k / 2>{}, IC<OP_Z + 1>{});
        gops(IC<136 + 5 * k>{}, IC<5>{});
      });
      STAMP(15);
      sfor<0, 2>([&](auto HV) {
        constexpr int hv = decltype(HV)::v;
        waitv<hv ? 0 : 8, OP_Z + hv>();
        if constexpr (hv == 1) {
#pragma unroll
          for (int i = 0; i < 4; ++i) { za[i] += 65536; PIN(za[i]); }
          zva += 65536; PIN(zva);
        }
        u32x4 z0[2], z1[2];
        f32x4 t[6];
        unsigned yy[2][2][4];
        auto rdz = [&](int u) {
          const unsigned a_ = za[2 * (u & 1)] + (u >> 1) * 2 * S_, b_ = za[2 * (u & 1) + 1] + (u >> 1) * 2 * S_;
          z0[u & 1] = ldstr(a_, a_ + S_); z1[u & 1] = ldstr(b_, b_ + S_);
        };
        auto amma = [&](int u, int i) { if (i < 2) mma0(t[i], z0[u & 1], ph[0][i]); else mmac(t[i], z1[u & 1], ph[i >> 1][i & 1], t[i & 1]); };
        auto ypk = [&](int u, int k) { yy[u & 1][k >> 2][k & 3] = pk2(t[2 + 2 * (k >> 2)][k & 3], t[3 + 2 * (k >> 2)][k & 3]); PIN(yy[u & 1][k >> 2][k & 3]); };
#pragma nounroll
        for (unsigned spins = 0;; ++spins) {
          u32x4 zv[4];
          unsigned zs;
          rdz(0);
#pragma unroll
          for (int i = 0; i < 4; ++i) zv[i] = lds128(zva + (i >> 1) * S_ + (i & 1) * 1024);
          SB();
#pragma unroll
          for (int i = 0; i < 6; ++i) {
            amma(0, i);
            SB();
            if (i >= 1 && i < 5) { zs = ((i == 1 ? 0u : zs) + zv[i - 1].x + zv[i - 1].y) + zv[i - 1].z + zv[i - 1].w; PIN(zs); zv[i - 1] = lds128(zva + (2 + ((i - 1) >> 1)) * S_ + ((i - 1) & 1) * 1024); }
            SB();
          }
          rdz(1);
#pragma unroll
          for (int i = 0; i < 4; ++i) { zs = (zs + zv[i].x + zv[i].y) + zv[i].z + zv[i].w; PIN(zs); }
          keep(t[0], t[1]);
          SB();
#pragma unroll
          for (int k = 0; k < 8; ++k) {
            ypk(0, k);
            if (k == 1) { zs += (unsigned)__builtin_amdgcn_update_dpp(0, (int)zs, 0x128, 0xf, 0xf, true); PIN(zs); }
            if (k == 3) { zs += (unsigned)__builtin_amdgcn_update_dpp(0, (int)zs, 0x124, 0xf, 0xf, true); PIN(zs); }
            if (k == 5) { zs += (unsigned)__builtin_amdgcn_update_dpp(0, (int)zs, 0x122, 0xf, 0xf, true); PIN(zs); }
            if (k == 7) { zs += (unsigned)__builtin_amdgcn_update_dpp(0, (int)zs, 0x121, 0xf, 0xf, true); PIN(zs); }
            SB();
          }
          bad0 = mism(zs, tag * 16u);
#pragma unroll
          for (int i = 0; i < 6; ++i) amma(1, i);
          if (__builtin_expect(bad0 == 0 || spins >= SPIN_CAP, 1)) break;
          RETRY(10 + hv);
          __builtin_amdgcn_s_sleep(10);
          asm volatile("s_waitcnt lgkmcnt(0)" ::: "memory");
          sfor<0, 8>([&](auto NI) { issue_z1(HV, NI, IC<OP_OPT>{}); });
          asm volatile("s_waitcnt vmcnt(0)" ::: "memory");
        }
        sfor<0, 4>([&](auto UI) {
          constexpr int u = decltype(UI)::v;
          const u32x4 y0 = {yy[u & 1][0][0], yy[u & 1][0][1], yy[u & 1][0][2], yy[u & 1][0][3]};
          const u32x4 y1 = {yy[u & 1][1][0], yy[u & 1][1][1], yy[u & 1][1][2], yy[u & 1][1][3]};
          if (u == 1 || u == 2) {
#pragma unroll
            for (int i = 0; i < 6; ++i) amma(u + 1, i);
          } else if (u == 3) asm volatile("s_nop 1");
          sfor<0, 8>([&](auto KI) {
            constexpr int k = decltype(KI)::v;
            mma(oacc[k >> 1][2 * (u >> 1) + (k & 1)], gf[hv][u & 1][k >> 1], (k & 1) ? y1 : y0);
            SB();
            if constexpr (u < 2 && k == 0) rdz(u + 2);
            if constexpr (u == 2 && k == 0) asm volatile("s_nop 1");
            if constexpr (u < 3 && k == 3) keep(t[0], t[1]);
            if constexpr (u < 3 && k >= 4) { ypk(u + 1, 2 * (k - 4)); ypk(u + 1, 2 * (k - 4) + 1); }
            if constexpr (hv == 0 && (u == 3 || k < 4)) gops(IC<216 + 3 * (4 * u + k)>{}, IC<3>{});
            if constexpr (hv == 1 && u < 3 && k < 4 && 4 * u + k < 11) fin_setup(IC<4 * u + k>{});
            if constexpr (hv == 1 && u == 3) put(k >> 1, k & 1);
            SB();
          });
        });
        if constexpr (hv == 0) STAMP(17);
      });
      STAMP(19);
#pragma unroll
      for (int k = 0; k < 8; ++k) { put(k >> 1, 2 + (k & 1)); SB(); }
      ONLY(END);
    }
    BARRIER();
    STAMP(20);
    {
      u32x4 pa[8];
#pragma unroll
      for (int i = 0; i < 8; ++i) pa[i] = ldstr(ra[i] + ((i >> 1) & 1) * 512, ra[i] + ((i >> 1) & 1) * 512 + 8192);
#pragma unroll
      for (int i = 0; i < 8; ++i) fin[i & 1] = mfma16(pa[i], sel[i >> 1], fin[i & 1]);
      const u32x4 o = {pk2(fin[0][0], fin[0][1]), pk2(fin[0][2], fin[0][3]), pk2(fin[1][0], fin[1][1]), pk2(fin[1][2], fin[1][3])};
      u32x4 oc;
#pragma unroll
      for (int i = 0; i < 4; ++i) { const unsigned t_ = o[i]; oc[i] = (unsigned)__builtin_amdgcn_ds_bpermute((int)bp, (int)t_); }
      asm volatile("global_store_dwordx4 %0, %1, %2 sc1" ::"v"(ooff), "v"(oc), "s"(out) : "memory");
    }
    if (b == 32 && tid == 0) *(unsigned*)(ws + WS_EPOCH) = tag;
    STAMP(21);
  } else {
    const int k1 = 8 * bi + xcd;
    const int lr = l >> 3, lc = l & 7;
    const int gst = 2 * (8 * (xcd & 3) + 4 * (xcd >> 2) + bi) + (w >> 2), n1s = 8 * (w & 3) + lr;
    const int cid = 4 * (xcd & 3) + bi;
    const int ctt = 7 * (xcd >> 2) + ((cid < 14 ? cid : 0) >> 1), cmat = cid & 1;
    const rsrc_t rx = mk_rsrc(x, 2048 * 896);
    u32x4 xr[7];
    {
      const int n2 = ((gst >> 2) & 1) + 8 * (gst >> 3) + 2 * (gst & 3);
      const unsigned xo = (unsigned)((64 * n1s + n2) * 896 + lc * 16);
      sfor<0, 7>([&](auto J) { constexpr int j = decltype(J)::v; ld128<0, j * 128>(xr[j], rx, xo, 0u); });
    }
    LOAD_EPOCH();
    SB();
    const rsrc_t rc = mk_rsrc(cmat ? wu : wg, 448 * 3584), rw = mk_rsrc(ws, WS_BYTES);
    const rsrc_t rfr = mk_rsrc(wr, 921600), rfi = mk_rsrc(wi, 921600);
    constexpr unsigned L_RED = 28672, L_WT = 32768, L_FILT = 32768;
    const unsigned wo = (unsigned)((56 * w + lr) * 3584 + ctt * 256 + ((lc ^ (((lr >> 1) & 3) << 1)) << 4));
    const unsigned lwt = L0 + L_WT + w * 7168;
    const int c31 = l & 31, h = l >> 5;
    {
      const int s = w, par = s >> 2;
      float vc[4], vs[4];
#pragma unroll
      for (int mm = 0; mm < 4; ++mm) {
        int n2 = par + 8 * (2 * (s & 3) + h) + 2 * mm;
        float rev = (float)((n2 * (k1 + 32 * c31)) & 2047) * (1.0f / 2048.0f);
        vc[mm] = __builtin_amdgcn_cosf(rev); vs[mm] = __builtin_amdgcn_sinf(rev);
      }
      *(LAS u32x4*)(uintptr_t)(L0 + (2 * s) * 1024 + l * 16) = u32x4{pk2(vc[0], vs[0]), pk2(vc[1], vs[1]), pk2(vc[2], vs[2]), pk2(vc[3], vs[3])};
      *(LAS u32x4*)(uintptr_t)(L0 + (2 * s + 1) * 1024 + l * 16) = u32x4{pk2(-vs[0], vc[0]), pk2(-vs[1], vc[1]), pk2(-vs[2], vc[2]), pk2(-vs[3], vc[3])};
    }
    {
      const int par = (w >> 1) & 1, s2 = w & 1;
      float v[8];
#pragma unroll
      for (int e = 0; e < 8; ++e) {
        int k2 = 16 * s2 + 8 * (e >> 2) + 4 * h + (e & 3);
        v[e] = __builtin_amdgcn_cosf((float)(((par + 2 * c31) * (k1 + 32 * k2)) & 2047) * (1.0f / 2048.0f) - (w >= 4 ? 0.25f : 0.0f)) * (2.0f / (2048.0f * 448.0f));
      }
      const u32x4 pk = {pk2(v[0], v[1]), pk2(v[2], v[3]), pk2(v[4], v[5]), pk2(v[6], v[7])};
      const unsigned a = L0 + 16384 + (w & 3) * 3072 + (w >> 2) * 1024 + l * 16;
      *(LAS u32x4*)(uintptr_t)(a) = pk;
      if (w >= 4) *(LAS u32x4*)(uintptr_t)(a + 1024) = pk ^ 0x80008000u;
    }
    SB();
    waitv<0, 0>();
    sfor<0, 3>([&](auto J) { constexpr int j = decltype(J)::v; dma_add<0, j * 1024, 0>(rc, wo, lwt, (unsigned)(j * (8 * 3584))); });
    GET_TAG();
    {
      float s1 = 0.f, s2 = 0.f;
#pragma unroll
      for (int j = 0; j < 7; ++j) { PIN(xr[j]); s1 = sum8(xr[j], s1); s2 = sq8(xr[j], s2); }
      s1 += dpp<0xB1>(s1); s2 += dpp<0xB1>(s2);
      s1 += dpp<0x4E>(s1); s2 += dpp<0x4E>(s2);
      s1 += dpp<0x141>(s1); s2 += dpp<0x141>(s2);
      const float mean = s1 * (1.0f / 448.0f);
      float var = __builtin_fmaf(-mean, mean, s2 * (1.0f / 448.0f));
      var = var > 0.f ? var : 0.f;
      const float rstd = __builtin_amdgcn_rsqf(var + 1e-5f);
      st128_sc1<0, 0>(rw, lc ? OOB : (unsigned)(WS_STAT + (gst * 32 + n1s) * 16), 0u, u32x4{asu(mean), tag, asu(rstd), tag});
    }
    STAMP(1);
    sfor<3, 7>([&](auto J) { constexpr int j = decltype(J)::v; dma_add<0, j * 1024, 0>(rc, wo, lwt, (unsigned)(j * (8 * 3584))); });
    const unsigned fo = (((unsigned)((k1 + 32 * (8 * w + h)) * 450)) & ~15u) + c31 * 16, lfb = L0 + L_FILT + w * 4096;
    const u32x4 ones = {0x3f803f80u, 0x3f803f80u, 0x3f803f80u, 0x3f803f80u};
    const unsigned csa = L0 + L_WT + (4 * q + (r >> 2)) * 128 + ((((4 * q + (r >> 2)) >> 1) & 3) << 5) + (r & 3) * 8;
    const unsigned cso = cid < 14 ? (unsigned)(WS_CS + (ctt * 4 + cmat) * 512) : OOB;
    auto colsum = [&](unsigned base, int ps) {
      const unsigned a = (csa ^ ((w & 3) << 5)) + (w >> 2) * 28672 + base;
      f32x4 sm = {0, 0, 0, 0};
#pragma unroll
      for (int j = 0; j < 7; ++j) sm = mfma16(ldstr(a + j * 4096, a + j * 4096 + 2048), ones, sm);
      if (r == 0) *(LAS f32x4*)(uintptr_t)(L0 + L_RED + ps * 512 + w * 64 + q * 16) = sm;
      BARRIER();
      if (w == 0) {
        const float t = *(LAS const float*)(uintptr_t)(L0 + L_RED + ps * 512 + l * 4) + *(LAS const float*)(uintptr_t)(L0 + L_RED + ps * 512 + 256 + l * 4);
        st64_sc1<0>(rw, cso + ps * 1024 + l * 8, 0u, u32x2{asu(t), tag});
      }
    };
    waitv<0, 0>();
    sfor<0, 4>([&](auto J) { constexpr int j = decltype(J)::v; dma_add<0, 65536 + j * 1024, 128>(rc, wo, lwt, (unsigned)(j * (8 * 3584))); });
    BARRIER();
    colsum(0, 0);
    STAMP(2);
    sfor<4, 7>([&](auto J) { constexpr int j = decltype(J)::v; dma_add<0, 65536 + j * 1024, 128>(rc, wo, lwt, (unsigned)(j * (8 * 3584))); });
    u32x4 fm[16];
#pragma unroll
    for (int i = 0; i < 16; ++i) fm[i] = lds128(L0 + i * 1024 + l * 16);
#pragma unroll
    for (int i = 0; i < 16; ++i) PIN(fm[i]);
    waitv<0, 0>();
    sfor<0, 4>([&](auto I) { constexpr int i = decltype(I)::v; dma_add<0, i * 1024, 0>(rfr, fo, lfb, (unsigned)(i * 28800)); });
    BARRIER();
    colsum(65536, 1);
    sfor<0, 4>([&](auto I) { constexpr int i = decltype(I)::v; dma_add<0, 32768 + i * 1024, 0>(rfi, fo, lfb, (unsigned)(i * 28800)); });
    waitv<0, 0>();
    BARRIER();
    STAMP(3);
    const int c = 28 * w + (c31 < 28 ? c31 : 27);
    f32x2 wpr[8], wpi[8], wmr[8], wmi[8];
    {
      const unsigned fa0 = L0 + L_FILT + 4 * h * 512 + ((2 * k1) & 15) + (c ? c : 224) * 2;
      unsigned short t[4][16];
#pragma unroll
      for (int i = 0; i < 16; ++i)
#pragma unroll
        for (int j = 0; j < 4; ++j) t[j][i] = *(LAS const unsigned short*)(uintptr_t)(fa0 + ((i & 3) + 8 * (i >> 2)) * 512 + j * 16384);
#pragma unroll
      for (int i = 0; i < 8; ++i) {
        const f32x2 p0 = {bflo(t[0][2 * i]), bflo(t[0][2 * i + 1])}, p1 = {bflo(t[1][2 * i]), bflo(t[1][2 * i + 1])};
        const f32x2 q0 = {bflo(t[2][2 * i]), bflo(t[2][2 * i + 1])}, q1 = {bflo(t[3][2 * i]), bflo(t[3][2 * i + 1])};
        wpr[i] = p0 + p1; wpi[i] = q0 + q1; wmr[i] = p0 - p1; wmi[i] = q0 - q1;
      }
      if (w == 0) {
        const float sc = c ? 1.0f : 0.5f;
#pragma unroll
        for (int i = 0; i < 8; ++i) { wpr[i] *= sc; wpi[i] *= sc; wmr[i] *= sc; wmi[i] *= sc; }
      }
#pragma unroll
      for (int i = 0; i < 8; ++i) { PIN(wpr[i]); PIN(wpi[i]); PIN(wmr[i]); PIN(wmi[i]); }
    }
    const unsigned zst = L0 + 98304 + w * 4096;
    unsigned zw[4], zr = zst + lr * 128 + ((lc ^ lr) << 4);
    unsigned zg = (unsigned)((8 * (lr >> 2) + (lr & 3)) * Z_G + lc * 16), ma = L0 + 16384 + l * 16;
    unsigned evo = (unsigned)(WS_E + k1 * E_K1 + h * 2 * E_PN + c * 16), eco = (unsigned)(WS_EC + (28 * h + (c >> 4)) * 2048 + k1 * 64 + (c & 15) * 4);
#pragma unroll
    for (int i = 0; i < 4; ++i) {
      zw[i] = zst + c31 * 128 + (((4 * (i >> 1) + 2 * h + (i & 1)) ^ (c31 & 7)) << 4);
      PIN(zw[i]);
    }
    PIN(zr); PIN(zg); PIN(ma); PIN(evo); PIN(eco);
    STAMP(4);
    if (w == 0) poll_flags(rw, (unsigned)(WS_HINT + k1 * 128), tag);
    asm volatile("s_barrier" ::: "memory");
    {
      u32x4 ef[8];
      unsigned ec[8];
      STAMP(5);
      sfor<0, 8>([&](auto S) {
        constexpr int s = decltype(S)::v;
        ld128_sc1<0, 0>(ef[s], rw, evo, (unsigned)(((s >> 2) + 4 * (s & 3)) * E_PN));
        ld32s_sc1<0, 0>(ec[s], rw, eco, (unsigned)(((s >> 2) + 4 * (s & 3)) * 28672));
      });
      f32x16 ac[4];
      u32x4 mi[12];
      auto pair = [&](const f32x16& v, int i) { return f32x2{v[2 * i], v[2 * i + 1]}; };
      sfor<0, 2>([&](auto HF) {
        constexpr int hf = decltype(HF)::v;
        waitv<8 - 8 * hf, 0>();
        unsigned df = 0;
#pragma unroll
        for (int s = 4 * hf; s < 4 * hf + 4; ++s) { PIN(ef[s]); PIN(ec[s]); df |= ((tag + ef[s].x + ef[s].y) + ef[s].z + ef[s].w) ^ ec[s]; }
        if (__builtin_expect(mism(df, 0u) != 0, 0)) {
          RETRY(16 + hf);
          sfor<4 * hf, 4 * hf + 4>([&](auto S) {
            constexpr int s = decltype(S)::v;
            if (mism((tag + ef[s].x + ef[s].y) + ef[s].z + ef[s].w, ec[s]) != 0) reload_e(ef[s], ec[s], rw, evo, eco, (unsigned)(((s >> 2) + 4 * (s & 3)) * E_PN), tag);
          });
        }
        SB();
#pragma unroll
        for (int i = 0; i < 16; ++i) { ac[2 * hf][i] = 0.f; ac[2 * hf + 1][i] = 0.f; }
#pragma unroll
        for (int s = 4 * hf; s < 4 * hf + 4; ++s) {
          ac[2 * hf] = mfma32(fm[2 * s], ef[s], ac[2 * hf]);
          ac[2 * hf + 1] = mfma32(fm[2 * s + 1], ef[s], ac[2 * hf + 1]);
        }
        SB();
      });
#pragma unroll
      for (int i = 0; i < 12; ++i) mi[i] = lds128(ma + i * 1024);
      STAMP(6);
      f32x16 zre[2], zim[2];
      auto filt = [&](int par) {
        const f32x2 *ur = par ? wmr : wpr, *ui = par ? wmi : wpi, *vr = par ? wpr : wmr, *vi = par ? wpi : wmi;
        f32x2 pr[8], pi[8];
#pragma unroll
        for (int i = 0; i < 8; ++i) { pr[i] = pair(ac[0], i) * ur[i]; pi[i] = pair(ac[0], i) * ui[i]; }
        SB();
#pragma unroll
        for (int i = 0; i < 8; ++i) { pr[i] -= pair(ac[1], i) * ui[i]; pi[i] += pair(ac[1], i) * ur[i]; }
        SB();
#pragma unroll
        for (int i = 0; i < 8; ++i) { pr[i] += pair(ac[2], i) * vr[i]; pi[i] += pair(ac[2], i) * vi[i]; }
        SB();
#pragma unroll
        for (int i = 0; i < 8; ++i) { pr[i] -= pair(ac[3], i) * vi[i]; pi[i] += pair(ac[3], i) * vr[i]; PIN(pr[i]); PIN(pi[i]); }
        SB();
#pragma unroll
        for (int i = 0; i < 16; ++i) { zre[par][i] = 0.f; zim[par][i] = 0.f; }
#pragma unroll
        for (int s2 = 0; s2 < 2; ++s2) {
          const f32x2 *a_ = pr + 4 * s2, *b_ = pi + 4 * s2;
          const u32x4 fr = {pk2(a_[0][0], a_[0][1]), pk2(a_[1][0], a_[1][1]), pk2(a_[2][0], a_[2][1]), pk2(a_[3][0], a_[3][1])};
          const u32x4 fi = {pk2(b_[0][0], b_[0][1]), pk2(b_[1][0], b_[1][1]), pk2(b_[2][0], b_[2][1]), pk2(b_[3][0], b_[3][1])};
          const u32x4* mm = mi + (par * 2 + s2) * 3;
          zre[par] = mfma32(fr, mm[0], zre[par]);
          zim[par] = mfma32(fr, mm[1], zim[par]);
          zre[par] = mfma32(fi, mm[2], zre[par]);
          zim[par] = mfma32(fi, mm[0], zim[par]);
        }
      };
      auto zout = [&](int par) {
        u32x2 zp[2][4];
#pragma unroll
        for (int g4 = 0; g4 < 4; ++g4) {
          zp[0][g4] = u32x2{pk2(zre[par][4 * g4], zre[par][4 * g4 + 1]), pk2(zre[par][4 * g4 + 2], zre[par][4 * g4 + 3])};
          zp[1][g4] = u32x2{pk2(zim[par][4 * g4], zim[par][4 * g4 + 1]), pk2(zim[par][4 * g4 + 2], zim[par][4 * g4 + 3])};
          if (g4 < 3) {
            *(LAS u32x2*)(uintptr_t)(zw[g4 >> 1] + (g4 & 1) * 8) = zp[0][g4];
            *(LAS u32x2*)(uintptr_t)(zw[2 + (g4 >> 1)] + (g4 & 1) * 8) = zp[1][g4];
          }
        }
        {
          unsigned t3 = (zp[0][3].x + zp[0][3].y + zp[1][3].x) + zp[1][3].y;
          unsigned sm = csum8(h ? 0u : t3, cat(zp[0][0], zp[0][1]), cat(zp[1][0], zp[1][1]));
          sm = (sm + zp[0][2].x + zp[0][2].y) + zp[1][2].x + zp[1][2].y;
          auto sw = __builtin_amdgcn_permlane32_swap(sm, sm, false, false);
          const unsigned a0 = sw[0], a1 = sw[1], V = tag - a0 - a1, V2 = (V & 0x40004000u) >> 1;
          *(LAS u32x2*)(uintptr_t)(zw[1] + 8) = h ? u32x2{V & ~0x40004000u, V2} : zp[0][3];
          *(LAS u32x2*)(uintptr_t)(zw[3] + 8) = h ? u32x2{V2, 0u} : zp[1][3];
        }
        u32x4 v[4];
#pragma unroll
        for (int i = 0; i < 4; ++i) v[i] = lds128(zr + i * 1024);
        const unsigned zb = (unsigned)(WS_Z + par * 4 * Z_G + w * 4096 + k1 * 128);
#pragma unroll
        for (int i = 0; i < 4; ++i) st128_sc1<0, 0>(rw, zg, zb + i * (16 * Z_G), v[i]);
      };
      filt(0);
      SB();
      st32_sc1<0>(rw, tid >= 480 ? (unsigned)(WS_FLAGZ + k1 * 128 + (tid - 480) * 4) : OOB, 0u, tag);
      filt(1);
      zout(0);
      SB();
      STAMP(8);
      zout(1);
      STAMP(7);
      asm volatile("gf_code_end:");
    }
  }
}

extern "C" void kernel_launch(void* const* d_in, const int* in_sizes, int n_in, void* d_out, int out_size, void* d_ws,
                              size_t ws_size, hipStream_t stream) {
  (void)in_sizes; (void)n_in; (void)out_size; (void)ws_size;
  (void)hipFuncSetAttribute((const void*)gf_glu_kernel, hipFuncAttributeMaxDynamicSharedMemorySize, L_TOTAL);
  gf_glu_kernel<<<256, 512, L_TOTAL, stream>>>((const char*)d_in[0], (const char*)d_in[1], (const char*)d_in[2],
                                               (const char*)d_in[4], (const char*)d_in[5], (const char*)d_in[6],
                                               (char*)d_out, (char*)d_ws);
}
```
